```python
import torch

def _softmax(x, axis=-1):
    return torch.softmax(x, dim=axis)
def _gelu(x):
    return torch.nn.functional.gelu(x, approximate="tanh")
def _silu(x):
    return torch.nn.functional.silu(x)
def _sigmoid(x):
    return torch.sigmoid(x)
def _layernorm(x, axis=-1, eps=1e-5):
    m = x.mean(dim=axis, keepdim=True)
    v = x.var(dim=axis, keepdim=True, unbiased=False)
    return (x - m) / torch.sqrt(v + eps)
def _scatter_add(tgt, idx, src, axis=0):
    return tgt.clone().index_add_(axis, idx, src)
def _segment_sum(src, seg, n):
    out = torch.zeros((n,) + src.shape[1:], dtype=src.dtype)
    return out.index_add_(0, seg, src)
def _pad(x, pw):
    flat = tuple(v for pair in reversed(pw) for v in pair)
    return torch.nn.functional.pad(x, flat)

kernel_name = 'concat_heads__mha_block_cached__gqa_block_cached__near_ridge'

def setup_inputs():

    _w = (lambda a: (lambda t: t.to(torch.bfloat16) if t.is_floating_point() else t)(torch.as_tensor(a)))
    def _A_setup():
        B, Sq, D = 8, 192, 640
        H, Sk, Dh = 32, 1024, 128
        return {
            "x": _w((lambda *s: torch.randn(*s, dtype=torch.bfloat16) * 0.8)(B, Sq, D)),
            "k_cache": _w((lambda *s: torch.randn(*s, dtype=torch.bfloat16) * 0.8)(B, H, Sk, Dh)),
            "v_cache": _w((lambda *s: torch.randn(*s, dtype=torch.bfloat16) * 0.05)(B, H, Sk, Dh)),
            "wq": _w((lambda *s: torch.randn(*s, dtype=torch.bfloat16) * 0.8)(D, H * Dh) * (D ** -0.5)),
            "wo": _w((lambda *s: torch.randn(*s, dtype=torch.bfloat16) * 0.05)(H * Dh, D) * ((H * Dh) ** -0.5)),
        }
    _A_in = _A_setup()

    def _B_setup():
        B, Sq, D = 8, 192, 640
        Hq, Hkv, Sk, Dh = 32, 32, 1024, 128
        assert Hq % Hkv == 0, (Hq, Hkv)
        return {
            "x": _w((lambda *s: torch.randn(*s, dtype=torch.bfloat16) * 0.8)(B, Sq, D)),
            "k_cache": _w((lambda *s: torch.randn(*s, dtype=torch.bfloat16) * 0.8)(B, Hkv, Sk, Dh)),
            "v_cache": _w((lambda *s: torch.randn(*s, dtype=torch.bfloat16) * 0.05)(B, Hkv, Sk, Dh)),
            "wq": _w((lambda *s: torch.randn(*s, dtype=torch.bfloat16) * 0.8)(D, Hq * Dh) * (D ** -0.5)),
            "wo": _w((lambda *s: torch.randn(*s, dtype=torch.bfloat16) * 0.05)(Hq * Dh, D) * ((Hq * Dh) ** -0.5)),
        }
    _B_in = _B_setup()

    return {
        "A_x": _A_in["x"],
        "A_k_cache": _A_in["k_cache"],
        "A_v_cache": _A_in["v_cache"],
        "A_wq": _A_in["wq"],
        "A_wo": _A_in["wo"],
        "B_x": _B_in["x"],
        "B_k_cache": _B_in["k_cache"],
        "B_v_cache": _B_in["v_cache"],
        "B_wq": _B_in["wq"],
        "B_wo": _B_in["wo"],
    }

def reference(A_x, A_k_cache, A_v_cache, A_wq, A_wo, B_x, B_k_cache, B_v_cache, B_wq, B_wo):
    def _A_ref(x, k_cache, v_cache, wq, wo):
        B, Sq, D = x.shape
        H, Dh = 32, 128
        q = (lambda x, *d: x.permute(*d))((x @ wq).reshape(B, Sq, H, Dh), 0, 2, 1, 3)
        scores = torch.einsum("bhqd,bhkd->bhqk", q, k_cache) * (Dh ** -0.5)
        p = _softmax(scores, axis=-1)
        o = torch.einsum("bhqk,bhkd->bhqd", p, v_cache)
        return (lambda x, *d: x.permute(*d))(o, 0, 2, 1, 3).reshape(B, Sq, H * Dh) @ wo

    def _B_ref(x, k_cache, v_cache, wq, wo):
        B, Sq, D = x.shape
        Hq, Dh = 32, 128
        Hkv = k_cache.shape[1]
        rep = Hq // Hkv
        q = (lambda x, *d: x.permute(*d))((x @ wq).reshape(B, Sq, Hq, Dh), 0, 2, 1, 3)
        k = (lambda x, n, axis: torch.repeat_interleave(x, n, dim=axis))(k_cache, rep, axis=1)
        v = (lambda x, n, axis: torch.repeat_interleave(x, n, dim=axis))(v_cache, rep, axis=1)
        scores = torch.einsum("bhqd,bhkd->bhqk", q, k) * (Dh ** -0.5)
        p = _softmax(scores, axis=-1)
        o = torch.einsum("bhqk,bhkd->bhqd", p, v)
        return (lambda x, *d: x.permute(*d))(o, 0, 2, 1, 3).reshape(B, Sq, Hq * Dh) @ wo

    a = _A_ref(A_x, A_k_cache, A_v_cache, A_wq, A_wo)
    b = _B_ref(B_x, B_k_cache, B_v_cache, B_wq, B_wo)
    return torch.concatenate([a, b], axis=-1)
```

```cpp
#include <hip/hip_runtime.h>
#include <hip/hip_bf16.h>
typedef unsigned short u16;
__device__ __forceinline__ float bf2f(u16 x){ return __uint_as_float(((unsigned)x)<<16); }
__device__ __forceinline__ u16 f2bf(float f){ unsigned u=__float_as_uint(f); u += 0x7FFF + ((u>>16)&1); return (u16)(u>>16); }

__global__ void gemm_naive(const u16* A, const u16* B, u16* C, int M, int N, int K, int ldc){
  __shared__ float As[16][65]; __shared__ float Bs[16][65];
  int tx=threadIdx.x&15, ty=threadIdx.x>>4; int m0=blockIdx.y*64, n0=blockIdx.x*64;
  float acc[4][4]={};
  for(int k0=0;k0<K;k0+=16){
    for(int i=threadIdx.x;i<1024;i+=256){ int r=i>>4,c=i&15; As[c][r]=bf2f(A[(size_t)(m0+r)*K+k0+c]); }
    for(int i=threadIdx.x;i<1024;i+=256){ int r=i>>6,c=i&63; Bs[r][c]=bf2f(B[(size_t)(k0+r)*N+n0+c]); }
    __syncthreads();
    for(int k=0;k<16;k++){ float a[4],b[4];
      for(int i=0;i<4;i++){a[i]=As[k][ty*4+i]; b[i]=Bs[k][tx*4+i];}
      for(int i=0;i<4;i++)for(int j=0;j<4;j++)acc[i][j]+=a[i]*b[j]; }
    __syncthreads();
  }
  for(int i=0;i<4;i++)for(int j=0;j<4;j++) C[(size_t)(m0+ty*4+i)*ldc+n0+tx*4+j]=f2bf(acc[i][j]);
}
__global__ void attn_naive(const u16* Q, const u16* Kc, const u16* Vc, u16* O){
  __shared__ float q[8][128]; __shared__ float s[8][1024]; __shared__ float red[8];
  int qt=blockIdx.x, h=blockIdx.y, b=blockIdx.z; int t=threadIdx.x;
  for(int i=t;i<1024;i+=256){int r=i>>7,c=i&127; q[r][c]=bf2f(Q[((size_t)(b*192+qt*8+r))*4096+h*128+c]);}
  __syncthreads();
  const u16* Kp=Kc+((size_t)(b*32+h))*1024*128; const u16* Vp=Vc+((size_t)(b*32+h))*1024*128;
  for(int k=t;k<1024;k+=256){ float a[8]={}; for(int d=0;d<128;d++){ float kv=bf2f(Kp[k*128+d]); for(int r=0;r<8;r++)a[r]+=q[r][d]*kv; }
    for(int r=0;r<8;r++) s[r][k]=a[r]*0.08838834764831845f; }
  __syncthreads();
  { int r=t>>5, l=t&31; float m=-1e30f; for(int k=l;k<1024;k+=32)m=fmaxf(m,s[r][k]); for(int o=16;o;o>>=1)m=fmaxf(m,__shfl_xor(m,o,32));
    float sum=0; for(int k=l;k<1024;k+=32){float e=__expf(s[r][k]-m); s[r][k]=e; sum+=e;} for(int o=16;o;o>>=1)sum+=__shfl_xor(sum,o,32);
    if(l==0)red[r]=1.f/sum; }
  __syncthreads();
  { int d=t&127, rh=t>>7; float a[4]={}; for(int k=0;k<1024;k++){ float v=bf2f(Vp[k*128+d]); for(int r=0;r<4;r++)a[r]+=s[rh*4+r][k]*v; }
    for(int r=0;r<4;r++) O[((size_t)(b*192+qt*8+rh*4+r))*4096+h*128+d]=f2bf(a[r]*red[rh*4+r]); }
}
extern "C" void kernel_launch(void* const* d_in, const int* in_sizes, int n_in, void* d_out, int out_size, void* d_ws, size_t ws_size, hipStream_t stream){
  u16* ws=(u16*)d_ws; u16* out=(u16*)d_out;
  for(int br=0;br<2;br++){
    const u16* x=(const u16*)d_in[br*5+0]; const u16* k=(const u16*)d_in[br*5+1]; const u16* v=(const u16*)d_in[br*5+2];
    const u16* wq=(const u16*)d_in[br*5+3]; const u16* wo=(const u16*)d_in[br*5+4];
    u16* Q=ws+(size_t)br*2*1536*4096; u16* O=Q+(size_t)1536*4096;
    gemm_naive<<<dim3(4096/64,1536/64),256,0,stream>>>(x,wq,Q,1536,4096,640,4096);
    attn_naive<<<dim3(24,32,8),256,0,stream>>>(Q,k,v,O);
    gemm_naive<<<dim3(640/64,1536/64),256,0,stream>>>(O,wo,out+br*640,1536,640,4096,1280);
  }
}
```

```cpp
#include <hip/hip_runtime.h>
#include <hip/hip_bf16.h>
typedef unsigned long long u64;
#define LO(p) ((unsigned)(u64)(p))
#define HI(p) ((unsigned)((u64)(p) >> 32))

__global__ __launch_bounds__(256, 1) void fused_kernel(const char* xA, const char* kA, const char* vA, const char* wqA, const char* woA,
                                                       const char* xB, const char* kB, const char* vB, const char* wqB, const char* woB,
                                                       char* out, char* ws) {
  const unsigned bid = blockIdx.x, xcd = bid & 7u, slot = bid >> 3;
  const unsigned wid = __builtin_amdgcn_readfirstlane(threadIdx.x >> 6);
  const unsigned br = xcd >> 2, hg = xcd & 3u, b = slot >> 2, h0 = hg * 8u + (slot & 3u) * 2u, h = h0 + (wid >> 1);
  const char* xp = (br ? xB : xA) + (size_t)b * 245760u;
  const char* wp = (br ? wqB : wqA) + h0 * 256u;
  const unsigned kvi = b * 32u + h;
  const char* kp = (br ? kB : kA) + (size_t)kvi * 262144u;
  const char* vp = (br ? vB : vA) + (size_t)kvi * 262144u;
  const char* wo = (br ? woB : woA) + hg * 1310720u;
  const unsigned headi = br * 256u + kvi;
  asm volatile(
"L_beg%=:\n"
"s_mov_b32 s44, %2\n"
"s_and_b32 s45, %3, 0xffff\n"
"s_mov_b32 s46, 5235200\n"
"s_mov_b32 s47, 0x20000\n"
"s_mov_b32 s60, %14\n"
"v_mbcnt_lo_u32_b32 v239, -1, 0\n"
"v_mbcnt_hi_u32_b32 v239, -1, v239\n"
"s_and_b32 s70, s60, 1\n"
"s_lshl_b32 s71, s70, 1\n"
"v_and_b32_e32 v2, 3, v239\n"
"v_bfe_u32 v5, v239, 2, 3\n"
"v_lshrrev_b32_e32 v6, 5, v239\n"
"v_lshlrev_b32_e32 v6, 6, v6\n"
"v_xor_b32_e32 v3, s71, v2\n"
"v_lshlrev_b32_e32 v3, 4, v3\n"
"v_lshlrev_b32_e32 v4, 13, v5\n"
"v_add3_u32 v230, v4, v6, v3\n"
"s_lshl_b32 s73, s60, 16\n"
"s_lshl_b32 s78, s60, 10\n"
"s_add_u32 m0, s78, 65536\n"
"s_nop 0\n"
"buffer_load_dwordx4 v230, s[44:47], s73 offen lds\n"
"s_add_u32 m0, s78, 73472\n"
"s_nop 0\n"
"buffer_load_dwordx4 v230, s[44:47], s73 offen offset:256 lds\n"
"s_add_u32 s70, s73, 262144\n"
"s_add_u32 m0, s78, 69632\n"
"s_nop 0\n"
"buffer_load_dwordx4 v230, s[44:47], s70 offen lds\n"
"s_add_u32 m0, s78, 77568\n"
"s_nop 0\n"
"buffer_load_dwordx4 v230, s[44:47], s70 offen offset:256 lds\n"
"s_mov_b32 s40, %0\n"
"s_and_b32 s41, %1, 0xffff\n"
"s_mov_b32 s42, 245760\n"
"s_mov_b32 s43, 0x20000\n"
"v_lshrrev_b32_e32 v0, 3, v239\n"
"v_lshrrev_b32_e32 v1, 4, v239\n"
"v_and_b32_e32 v3, 7, v239\n"
"v_xor_b32_e32 v1, v1, v3\n"
"v_lshlrev_b32_e32 v1, 4, v1\n"
"v_mul_u32_u24_e32 v3, 0x500, v0\n"
"v_add_u32_e32 v228, v3, v1\n"
"v_xor_b32_e32 v229, 64, v228\n"
"s_mul_i32 s72, s60, 61440\n"
"s_mul_i32 s77, s60, 6144\n"
"s_add_u32 m0, s77, 0\n"
"s_add_u32 s70, s72, 0\n"
"buffer_load_dwordx4 v228, s[40:43], s70 offen lds\n"
"s_add_u32 m0, s77, 1024\n"
"s_add_u32 s70, s72, 10240\n"
"buffer_load_dwordx4 v229, s[40:43], s70 offen lds\n"
"s_add_u32 m0, s77, 2048\n"
"s_add_u32 s70, s72, 20480\n"
"buffer_load_dwordx4 v228, s[40:43], s70 offen lds\n"
"s_add_u32 m0, s77, 3072\n"
"s_add_u32 s70, s72, 30720\n"
"buffer_load_dwordx4 v229, s[40:43], s70 offen lds\n"
"s_add_u32 m0, s77, 4096\n"
"s_add_u32 s70, s72, 40960\n"
"buffer_load_dwordx4 v228, s[40:43], s70 offen lds\n"
"s_add_u32 m0, s77, 5120\n"
"s_add_u32 s70, s72, 51200\n"
"buffer_load_dwordx4 v229, s[40:43], s70 offen lds\n"
"s_lshr_b32 s80, %15, 3\n"
"s_lshl_b32 s80, s80, 2\n"
"s_add_u32 s80, s80, s60\n"
"s_getpc_b64 s[36:37]\n"
"L_pc%=:\n"
"s_and_b32 s37, s37, 0xffff\n"
"s_mov_b32 s38, L_end%=-L_pc%=\n"
"s_mov_b32 s39, 0x20000\n"
"s_lshl_b32 s70, s80, 9\n"
"v_and_b32_e32 v8, 3, v239\n"
"v_lshlrev_b32_e32 v8, 7, v8\n"
"buffer_load_dword v231, v8, s[36:39], s70 offen \n"
"s_mov_b32 s48, %4\n"
"s_and_b32 s49, %5, 0xffff\n"
"s_mov_b32 s50, 262144\n"
"s_mov_b32 s51, 0x20000\n"
"s_mov_b32 s52, %6\n"
"s_and_b32 s53, %7, 0xffff\n"
"s_mov_b32 s54, 262144\n"
"s_mov_b32 s55, 0x20000\n"
"s_add_u32 m0, s78, 81792\n"
"s_nop 0\n"
"buffer_load_dwordx4 v230, s[44:47], s73 offen offset:128 lds\n"
"s_add_u32 m0, s78, 89728\n"
"s_nop 0\n"
"buffer_load_dwordx4 v230, s[44:47], s73 offen offset:384 lds\n"
"s_add_u32 s70, s73, 262144\n"
"s_add_u32 m0, s78, 85888\n"
"s_nop 0\n"
"buffer_load_dwordx4 v230, s[44:47], s70 offen offset:128 lds\n"
"s_add_u32 m0, s78, 93824\n"
"s_nop 0\n"
"buffer_load_dwordx4 v230, s[44:47], s70 offen offset:384 lds\n"
"s_add_u32 s73, s73, 524288\n"
"v_mov_b32_e32 v9, 38797312\n"
"s_mov_b32 s70, %8\n"
"s_mov_b32 s71, %9\n"
"global_load_dword v238, v9, s[70:71] sc1\n"
"v_lshl_add_u32 v244, v0, 8, v1\n"
"v_xor_b32_e32 v245, 64, v244\n"
"v_lshrrev_b32_e32 v3, 2, v5\n"
"v_lshlrev_b32_e32 v3, 1, v3\n"
"v_xor_b32_e32 v3, v3, v2\n"
"v_lshlrev_b32_e32 v3, 4, v3\n"
"v_lshlrev_b32_e32 v4, 8, v5\n"
"v_add3_u32 v246, v4, v6, v3\n"
"s_add_u32 m0, s78, 24576\n"
"s_nop 0\n"
"buffer_load_dwordx4 v230, s[44:47], s73 offen lds\n"
"s_add_u32 m0, s78, 32512\n"
"s_nop 0\n"
"buffer_load_dwordx4 v230, s[44:47], s73 offen offset:256 lds\n"
"s_add_u32 s70, s73, 262144\n"
"s_add_u32 m0, s78, 28672\n"
"s_nop 0\n"
"buffer_load_dwordx4 v230, s[44:47], s70 offen lds\n"
"s_add_u32 m0, s78, 36608\n"
"s_nop 0\n"
"buffer_load_dwordx4 v230, s[44:47], s70 offen offset:256 lds\n"
"s_add_u32 m0, s77, 40960\n"
"s_add_u32 s70, s72, 128\n"
"buffer_load_dwordx4 v228, s[40:43], s70 offen lds\n"
"s_add_u32 m0, s77, 41984\n"
"s_add_u32 s70, s72, 10368\n"
"buffer_load_dwordx4 v229, s[40:43], s70 offen lds\n"
"s_add_u32 m0, s77, 43008\n"
"s_add_u32 s70, s72, 20608\n"
"buffer_load_dwordx4 v228, s[40:43], s70 offen lds\n"
"s_add_u32 m0, s77, 44032\n"
"s_add_u32 s70, s72, 30848\n"
"buffer_load_dwordx4 v229, s[40:43], s70 offen lds\n"
"s_add_u32 m0, s77, 45056\n"
"s_add_u32 s70, s72, 41088\n"
"buffer_load_dwordx4 v228, s[40:43], s70 offen lds\n"
"s_add_u32 m0, s77, 46080\n"
"s_add_u32 s70, s72, 51328\n"
"buffer_load_dwordx4 v229, s[40:43], s70 offen lds\n"
"s_and_b32 s70, s60, 1\n"
"s_lshl_b32 s64, s70, 12\n"
"s_lshr_b32 s71, s60, 1\n"
"s_lshl_b32 s66, s71, 14\n"
"s_add_u32 s66, s66, s64\n"
"s_mov_b32 s67, 98304\n"
"s_mov_b32 s76, 0\n"
"s_and_b32 s70, s60, 1\n"
"s_lshl_b32 s64, s70, 13\n"
"s_lshr_b32 s71, s60, 1\n"
"s_lshl_b32 s66, s71, 14\n"
"s_lshl_b32 s67, s70, 15\n"
"s_add_u32 s67, s67, s66\n"
"s_add_u32 s67, s67, 98304\n"
"s_add_u32 s66, s66, 163840\n"
"s_mul_i32 s80, %17, 6\n"
"s_and_b32 s80, s80, 30\n"
"s_lshl_b32 s80, s80, 13\n"
"s_add_u32 s64, s64, s80\n"
"s_mov_b32 s90, s64\n"
"s_mov_b32 s91, s67\n"
"v_and_b32_e32 v0, 15, v239\n"
"v_lshrrev_b32_e32 v1, 4, v239\n"
"v_lshrrev_b32_e32 v2, 2, v0\n"
"v_and_b32_e32 v4, 3, v239\n"
"v_lshlrev_b32_e32 v4, 3, v4\n"
"v_lshl_add_u32 v4, v2, 6, v4\n"
"v_and_b32_e32 v5, 1, v1\n"
"v_lshl_add_u32 v6, v5, 5, v4\n"
"s_lshl_b32 s80, s71, 13\n"
"s_add_u32 s80, s80, 24576\n"
"v_lshl_add_u32 v7, v1, 11, v6\n"
"v_add_u32_e32 v226, s80, v7\n"
"v_xor_b32_e32 v227, 32, v226\n"
"s_lshl_b32 s80, s71, 14\n"
"s_add_u32 s80, s80, 98304\n"
"v_lshl_add_u32 v6, v5, 8, v6\n"
"v_lshrrev_b32_e32 v7, 1, v1\n"
"v_lshl_add_u32 v6, v7, 11, v6\n"
"v_add_u32_e32 v242, s80, v6\n"
"v_xor_b32_e32 v243, 32, v242\n"
"v_lshrrev_b32_e32 v4, 1, v0\n"
"v_xor_b32_e32 v4, v4, v1\n"
"v_lshlrev_b32_e32 v4, 4, v4\n"
"v_and_b32_e32 v5, 7, v0\n"
"v_lshl_add_u32 v4, v5, 7, v4\n"
"v_lshrrev_b32_e32 v5, 3, v0\n"
"v_lshl_add_u32 v6, v5, 11, v4\n"
"v_add_u32_e32 v240, s80, v6\n"
"v_xor_b32_e32 v241, 64, v240\n"
"v_lshl_add_u32 v4, v5, 10, v4\n"
"s_mul_i32 s80, s70, 12288\n"
"s_mul_i32 s81, s71, 6144\n"
"s_add_u32 s63, s80, s81\n"
"s_sub_u32 s80, s80, s81\n"
"s_add_u32 s80, s80, 6144\n"
"v_add_u32_e32 v224, s63, v4\n"
"v_xor_b32_e32 v225, 64, v224\n"
"v_add_u32_e32 v234, s80, v4\n"
"v_xor_b32_e32 v235, 64, v234\n"
"v_bfe_u32 v4, v239, 4, 1\n"
"v_lshlrev_b32_e32 v4, 1, v4\n"
"v_and_b32_e32 v5, 3, v239\n"
"v_xor_b32_e32 v4, v4, v5\n"
"v_lshlrev_b32_e32 v4, 4, v4\n"
"v_lshl_add_u32 v4, v2, 6, v4\n"
"v_lshl_add_u32 v4, v1, 10, v4\n"
"s_lshl_b32 s80, s71, 13\n"
"s_add_u32 s80, s80, 24576\n"
"v_add_u32_e32 v226, s80, v4\n"
"s_mov_b32 s69, 98304\n"
"s_mov_b32 s95, 98304\n"
"s_mov_b32 s81, 32768\n"
"s_waitcnt vmcnt(16)\n"
"s_barrier\n"
"ds_read_b128 a[0:3], v224 offset:0\n"
"ds_read_b128 a[4:7], v225 offset:0\n"
"ds_read_b128 a[8:11], v224 offset:2048\n"
"ds_read_b128 a[12:15], v225 offset:2048\n"
"ds_read_b128 a[16:19], v224 offset:4096\n"
"ds_read_b128 a[20:23], v225 offset:4096\n"
"ds_read_b128 a[24:27], v234 offset:0\n"
"ds_read_b128 a[28:31], v235 offset:0\n"
"ds_read_b128 a[32:35], v234 offset:2048\n"
"ds_read_b128 a[36:39], v235 offset:2048\n"
"ds_read_b128 a[40:43], v234 offset:4096\n"
"ds_read_b128 a[44:47], v235 offset:4096\n"
"ds_read_b64_tr_b16 a[128:129], v226 offset:40960\n"
"ds_read_b64_tr_b16 a[130:131], v226 offset:41216\n"
"ds_read_b64_tr_b16 a[132:133], v226 offset:45056\n"
"ds_read_b64_tr_b16 a[134:135], v226 offset:45312\n"
"ds_read_b64_tr_b16 a[136:137], v226 offset:40968\n"
"ds_read_b64_tr_b16 a[138:139], v226 offset:41224\n"
"ds_read_b64_tr_b16 a[140:141], v226 offset:45064\n"
"ds_read_b64_tr_b16 a[142:143], v226 offset:45320\n"
"ds_read_b64_tr_b16 a[144:145], v226 offset:41472\n"
"ds_read_b64_tr_b16 a[146:147], v226 offset:41728\n"
"ds_read_b64_tr_b16 a[148:149], v226 offset:45568\n"
"ds_read_b64_tr_b16 a[150:151], v226 offset:45824\n"
"ds_read_b64_tr_b16 a[152:153], v226 offset:41480\n"
"ds_read_b64_tr_b16 a[154:155], v226 offset:41736\n"
"ds_read_b64_tr_b16 a[156:157], v226 offset:45576\n"
"ds_read_b64_tr_b16 a[158:159], v226 offset:45832\n"
"s_waitcnt vmcnt(11) lgkmcnt(0)\n"
"s_barrier\n"
"v_mfma_f32_16x16x32_bf16 v[0:3], a[128:131], a[0:3], 0\n"
"ds_read_b64_tr_b16 a[160:161], v226 offset:57344\n"
"v_mfma_f32_16x16x32_bf16 v[0:3], a[132:135], a[4:7], v[0:3]\n"
"s_add_u32 m0, s77, 0\n"
"s_add_u32 s70, s72, 256\n"
"v_mfma_f32_16x16x32_bf16 v[32:35], a[128:131], a[8:11], 0\n"
"buffer_load_dwordx4 v228, s[40:43], s70 offen lds\n"
"v_mfma_f32_16x16x32_bf16 v[32:35], a[132:135], a[12:15], v[32:35]\n"
"ds_read_b64_tr_b16 a[162:163], v226 offset:57600\n"
"v_mfma_f32_16x16x32_bf16 v[64:67], a[128:131], a[16:19], 0\n"
"s_add_u32 m0, s77, 1024\n"
"s_add_u32 s70, s72, 10496\n"
"v_mfma_f32_16x16x32_bf16 v[64:67], a[132:135], a[20:23], v[64:67]\n"
"ds_read_b64_tr_b16 a[164:165], v226 offset:61440\n"
"v_mfma_f32_16x16x32_bf16 v[96:99], a[128:131], a[24:27], 0\n"
"buffer_load_dwordx4 v229, s[40:43], s70 offen lds\n"
"v_mfma_f32_16x16x32_bf16 v[96:99], a[132:135], a[28:31], v[96:99]\n"
"ds_read_b64_tr_b16 a[166:167], v226 offset:61696\n"
"v_mfma_f32_16x16x32_bf16 v[128:131], a[128:131], a[32:35], 0\n"
"s_add_u32 m0, s77, 2048\n"
"s_add_u32 s70, s72, 20736\n"
"v_mfma_f32_16x16x32_bf16 v[128:131], a[132:135], a[36:39], v[128:131]\n"
"ds_read_b64_tr_b16 a[168:169], v226 offset:57352\n"
"v_mfma_f32_16x16x32_bf16 v[160:163], a[128:131], a[40:43], 0\n"
"buffer_load_dwordx4 v228, s[40:43], s70 offen lds\n"
"v_mfma_f32_16x16x32_bf16 v[160:163], a[132:135], a[44:47], v[160:163]\n"
"s_add_u32 m0, s78, 65408\n"
"v_mfma_f32_16x16x32_bf16 v[4:7], a[136:139], a[0:3], 0\n"
"ds_read_b64_tr_b16 a[170:171], v226 offset:57608\n"
"v_mfma_f32_16x16x32_bf16 v[4:7], a[140:143], a[4:7], v[4:7]\n"
"buffer_load_dwordx4 v230, s[44:47], s73 offen offset:128 lds\n"
"v_mfma_f32_16x16x32_bf16 v[36:39], a[136:139], a[8:11], 0\n"
"ds_read_b64_tr_b16 a[172:173], v226 offset:61448\n"
"v_mfma_f32_16x16x32_bf16 v[36:39], a[140:143], a[12:15], v[36:39]\n"
"s_add_u32 m0, s78, 73344\n"
"v_mfma_f32_16x16x32_bf16 v[68:71], a[136:139], a[16:19], 0\n"
"v_mfma_f32_16x16x32_bf16 v[68:71], a[140:143], a[20:23], v[68:71]\n"
"ds_read_b64_tr_b16 a[174:175], v226 offset:61704\n"
"v_mfma_f32_16x16x32_bf16 v[100:103], a[136:139], a[24:27], 0\n"
"buffer_load_dwordx4 v230, s[44:47], s73 offen offset:384 lds\n"
"v_mfma_f32_16x16x32_bf16 v[100:103], a[140:143], a[28:31], v[100:103]\n"
"ds_read_b64_tr_b16 a[176:177], v226 offset:57856\n"
"s_add_u32 s70, s73, 262144\n"
"v_mfma_f32_16x16x32_bf16 v[132:135], a[136:139], a[32:35], 0\n"
"v_mfma_f32_16x16x32_bf16 v[132:135], a[140:143], a[36:39], v[132:135]\n"
"ds_read_b64_tr_b16 a[178:179], v226 offset:58112\n"
"s_add_u32 m0, s78, 69504\n"
"v_mfma_f32_16x16x32_bf16 v[164:167], a[136:139], a[40:43], 0\n"
"v_mfma_f32_16x16x32_bf16 v[164:167], a[140:143], a[44:47], v[164:167]\n"
"buffer_load_dwordx4 v230, s[44:47], s70 offen offset:128 lds\n"
"v_mfma_f32_16x16x32_bf16 v[8:11], a[144:147], a[0:3], 0\n"
"ds_read_b64_tr_b16 a[180:181], v226 offset:61952\n"
"v_mfma_f32_16x16x32_bf16 v[8:11], a[148:151], a[4:7], v[8:11]\n"
"s_add_u32 m0, s78, 77440\n"
"v_mfma_f32_16x16x32_bf16 v[40:43], a[144:147], a[8:11], 0\n"
"ds_read_b64_tr_b16 a[182:183], v226 offset:62208\n"
"v_mfma_f32_16x16x32_bf16 v[40:43], a[148:151], a[12:15], v[40:43]\n"
"buffer_load_dwordx4 v230, s[44:47], s70 offen offset:384 lds\n"
"v_mfma_f32_16x16x32_bf16 v[72:75], a[144:147], a[16:19], 0\n"
"v_mfma_f32_16x16x32_bf16 v[72:75], a[148:151], a[20:23], v[72:75]\n"
"ds_read_b64_tr_b16 a[184:185], v226 offset:57864\n"
"s_add_u32 s73, s73, 524288\n"
"v_mfma_f32_16x16x32_bf16 v[104:107], a[144:147], a[24:27], 0\n"
"v_mfma_f32_16x16x32_bf16 v[104:107], a[148:151], a[28:31], v[104:107]\n"
"ds_read_b64_tr_b16 a[186:187], v226 offset:58120\n"
"v_mfma_f32_16x16x32_bf16 v[136:139], a[144:147], a[32:35], 0\n"
"v_mfma_f32_16x16x32_bf16 v[136:139], a[148:151], a[36:39], v[136:139]\n"
"v_mfma_f32_16x16x32_bf16 v[168:171], a[144:147], a[40:43], 0\n"
"ds_read_b64_tr_b16 a[188:189], v226 offset:61960\n"
"v_mfma_f32_16x16x32_bf16 v[168:171], a[148:151], a[44:47], v[168:171]\n"
"v_mfma_f32_16x16x32_bf16 v[12:15], a[152:155], a[0:3], 0\n"
"ds_read_b64_tr_b16 a[190:191], v226 offset:62216\n"
"v_mfma_f32_16x16x32_bf16 v[12:15], a[156:159], a[4:7], v[12:15]\n"
"v_mfma_f32_16x16x32_bf16 v[44:47], a[152:155], a[8:11], 0\n"
"v_mfma_f32_16x16x32_bf16 v[44:47], a[156:159], a[12:15], v[44:47]\n"
"v_mfma_f32_16x16x32_bf16 v[76:79], a[152:155], a[16:19], 0\n"
"v_mfma_f32_16x16x32_bf16 v[76:79], a[156:159], a[20:23], v[76:79]\n"
"v_mfma_f32_16x16x32_bf16 v[108:111], a[152:155], a[24:27], 0\n"
"v_mfma_f32_16x16x32_bf16 v[108:111], a[156:159], a[28:31], v[108:111]\n"
"v_mfma_f32_16x16x32_bf16 v[140:143], a[152:155], a[32:35], 0\n"
"v_mfma_f32_16x16x32_bf16 v[140:143], a[156:159], a[36:39], v[140:143]\n"
"v_mfma_f32_16x16x32_bf16 v[172:175], a[152:155], a[40:43], 0\n"
"v_mfma_f32_16x16x32_bf16 v[172:175], a[156:159], a[44:47], v[172:175]\n"
"s_waitcnt vmcnt(7) lgkmcnt(0)\n"
"s_barrier\n"
"v_mfma_f32_16x16x32_bf16 v[16:19], a[160:163], a[0:3], 0\n"
"ds_read_b128 a[192:195], v224 offset:40960\n"
"v_mfma_f32_16x16x32_bf16 v[16:19], a[164:167], a[4:7], v[16:19]\n"
"s_add_u32 m0, s77, 3072\n"
"s_add_u32 s70, s72, 30976\n"
"v_mfma_f32_16x16x32_bf16 v[48:51], a[160:163], a[8:11], 0\n"
"ds_read_b128 a[196:199], v225 offset:40960\n"
"ds_read_b128 a[200:203], v224 offset:43008\n"
"v_mfma_f32_16x16x32_bf16 v[48:51], a[164:167], a[12:15], v[48:51]\n"
"buffer_load_dwordx4 v229, s[40:43], s70 offen lds\n"
"v_mfma_f32_16x16x32_bf16 v[80:83], a[160:163], a[16:19], 0\n"
"s_add_u32 m0, s77, 4096\n"
"s_add_u32 s70, s72, 41216\n"
"v_mfma_f32_16x16x32_bf16 v[80:83], a[164:167], a[20:23], v[80:83]\n"
"ds_read_b128 a[204:207], v225 offset:43008\n"
"ds_read_b128 a[208:211], v224 offset:45056\n"
"v_mfma_f32_16x16x32_bf16 v[112:115], a[160:163], a[24:27], 0\n"
"buffer_load_dwordx4 v228, s[40:43], s70 offen lds\n"
"v_mfma_f32_16x16x32_bf16 v[112:115], a[164:167], a[28:31], v[112:115]\n"
"ds_read_b128 a[212:215], v225 offset:45056\n"
"v_mfma_f32_16x16x32_bf16 v[144:147], a[160:163], a[32:35], 0\n"
"s_add_u32 m0, s77, 5120\n"
"s_add_u32 s70, s72, 51456\n"
"v_mfma_f32_16x16x32_bf16 v[144:147], a[164:167], a[36:39], v[144:147]\n"
"ds_read_b128 a[216:219], v234 offset:40960\n"
"ds_read_b128 a[220:223], v235 offset:40960\n"
"v_mfma_f32_16x16x32_bf16 v[176:179], a[160:163], a[40:43], 0\n"
"buffer_load_dwordx4 v229, s[40:43], s70 offen lds\n"
"v_mfma_f32_16x16x32_bf16 v[176:179], a[164:167], a[44:47], v[176:179]\n"
"s_add_u32 m0, s78, 81920\n"
"ds_read_b128 a[224:227], v234 offset:43008\n"
"v_mfma_f32_16x16x32_bf16 v[20:23], a[168:171], a[0:3], 0\n"
"ds_read_b128 a[228:231], v235 offset:43008\n"
"v_mfma_f32_16x16x32_bf16 v[20:23], a[172:175], a[4:7], v[20:23]\n"
"buffer_load_dwordx4 v230, s[44:47], s73 offen lds\n"
"v_mfma_f32_16x16x32_bf16 v[52:55], a[168:171], a[8:11], 0\n"
"ds_read_b128 a[232:235], v234 offset:45056\n"
"s_add_u32 m0, s78, 89856\n"
"v_mfma_f32_16x16x32_bf16 v[52:55], a[172:175], a[12:15], v[52:55]\n"
"ds_read_b128 a[236:239], v235 offset:45056\n"
"v_mfma_f32_16x16x32_bf16 v[84:87], a[168:171], a[16:19], 0\n"
"ds_read_b64_tr_b16 a[128:129], v226 offset:0\n"
"v_mfma_f32_16x16x32_bf16 v[84:87], a[172:175], a[20:23], v[84:87]\n"
"buffer_load_dwordx4 v230, s[44:47], s73 offen offset:256 lds\n"
"v_mfma_f32_16x16x32_bf16 v[116:119], a[168:171], a[24:27], 0\n"
"ds_read_b64_tr_b16 a[130:131], v226 offset:256\n"
"s_add_u32 s70, s73, 262144\n"
"v_mfma_f32_16x16x32_bf16 v[116:119], a[172:175], a[28:31], v[116:119]\n"
"ds_read_b64_tr_b16 a[132:133], v226 offset:4096\n"
"v_mfma_f32_16x16x32_bf16 v[148:151], a[168:171], a[32:35], 0\n"
"ds_read_b64_tr_b16 a[134:135], v226 offset:4352\n"
"s_add_u32 m0, s78, 86016\n"
"v_mfma_f32_16x16x32_bf16 v[148:151], a[172:175], a[36:39], v[148:151]\n"
"buffer_load_dwordx4 v230, s[44:47], s70 offen lds\n"
"v_mfma_f32_16x16x32_bf16 v[180:183], a[168:171], a[40:43], 0\n"
"ds_read_b64_tr_b16 a[136:137], v226 offset:8\n"
"v_mfma_f32_16x16x32_bf16 v[180:183], a[172:175], a[44:47], v[180:183]\n"
"ds_read_b64_tr_b16 a[138:139], v226 offset:264\n"
"s_add_u32 m0, s78, 93952\n"
"v_mfma_f32_16x16x32_bf16 v[24:27], a[176:179], a[0:3], 0\n"
"v_mfma_f32_16x16x32_bf16 v[24:27], a[180:183], a[4:7], v[24:27]\n"
"ds_read_b64_tr_b16 a[140:141], v226 offset:4104\n"
"v_mfma_f32_16x16x32_bf16 v[56:59], a[176:179], a[8:11], 0\n"
"buffer_load_dwordx4 v230, s[44:47], s70 offen offset:256 lds\n"
"v_mfma_f32_16x16x32_bf16 v[56:59], a[180:183], a[12:15], v[56:59]\n"
"ds_read_b64_tr_b16 a[142:143], v226 offset:4360\n"
"ds_read_b64_tr_b16 a[144:145], v226 offset:512\n"
"v_mfma_f32_16x16x32_bf16 v[88:91], a[176:179], a[16:19], 0\n"
"s_mov_b32 m0, s67\n"
"v_mfma_f32_16x16x32_bf16 v[88:91], a[180:183], a[20:23], v[88:91]\n"
"ds_read_b64_tr_b16 a[146:147], v226 offset:768\n"
"v_mfma_f32_16x16x32_bf16 v[120:123], a[176:179], a[24:27], 0\n"
"buffer_load_dwordx4 v244, s[48:51], s64 offen nt lds\n"
"s_add_u32 m0, m0, 896\n"
"s_nop 0\n"
"buffer_load_dwordx4 v244, s[48:51], s64 offen offset:128 nt lds\n"
"v_mfma_f32_16x16x32_bf16 v[120:123], a[180:183], a[28:31], v[120:123]\n"
"ds_read_b64_tr_b16 a[148:149], v226 offset:4608\n"
"v_mfma_f32_16x16x32_bf16 v[152:155], a[176:179], a[32:35], 0\n"
"ds_read_b64_tr_b16 a[150:151], v226 offset:4864\n"
"v_mfma_f32_16x16x32_bf16 v[152:155], a[180:183], a[36:39], v[152:155]\n"
"ds_read_b64_tr_b16 a[152:153], v226 offset:520\n"
"v_mfma_f32_16x16x32_bf16 v[184:187], a[176:179], a[40:43], 0\n"
"ds_read_b64_tr_b16 a[154:155], v226 offset:776\n"
"v_mfma_f32_16x16x32_bf16 v[184:187], a[180:183], a[44:47], v[184:187]\n"
"v_mfma_f32_16x16x32_bf16 v[28:31], a[184:187], a[0:3], 0\n"
"ds_read_b64_tr_b16 a[156:157], v226 offset:4616\n"
"v_mfma_f32_16x16x32_bf16 v[28:31], a[188:191], a[4:7], v[28:31]\n"
"ds_read_b64_tr_b16 a[158:159], v226 offset:4872\n"
"v_mfma_f32_16x16x32_bf16 v[60:63], a[184:187], a[8:11], 0\n"
"v_mfma_f32_16x16x32_bf16 v[60:63], a[188:191], a[12:15], v[60:63]\n"
"v_mfma_f32_16x16x32_bf16 v[92:95], a[184:187], a[16:19], 0\n"
"v_mfma_f32_16x16x32_bf16 v[92:95], a[188:191], a[20:23], v[92:95]\n"
"v_mfma_f32_16x16x32_bf16 v[124:127], a[184:187], a[24:27], 0\n"
"v_mfma_f32_16x16x32_bf16 v[124:127], a[188:191], a[28:31], v[124:127]\n"
"v_mfma_f32_16x16x32_bf16 v[156:159], a[184:187], a[32:35], 0\n"
"v_mfma_f32_16x16x32_bf16 v[156:159], a[188:191], a[36:39], v[156:159]\n"
"v_mfma_f32_16x16x32_bf16 v[188:191], a[184:187], a[40:43], 0\n"
"v_mfma_f32_16x16x32_bf16 v[188:191], a[188:191], a[44:47], v[188:191]\n"
"s_waitcnt vmcnt(9) lgkmcnt(0)\n"
"s_barrier\n"
"v_mfma_f32_16x16x32_bf16 v[0:3], a[128:131], a[192:195], v[0:3]\n"
"ds_read_b64_tr_b16 a[160:161], v226 offset:40960\n"
"v_readfirstlane_b32 s62, v238\n"
"v_mfma_f32_16x16x32_bf16 v[0:3], a[132:135], a[196:199], v[0:3]\n"
"s_add_u32 s62, s62, 1\n"
"v_mfma_f32_16x16x32_bf16 v[32:35], a[128:131], a[200:203], v[32:35]\n"
"ds_read_b64_tr_b16 a[162:163], v226 offset:41216\n"
"v_mfma_f32_16x16x32_bf16 v[32:35], a[132:135], a[204:207], v[32:35]\n"
"s_xor_b32 s65, s62, 0xa5c3e187\n"
"v_mfma_f32_16x16x32_bf16 v[64:67], a[128:131], a[208:211], v[64:67]\n"
"ds_read_b64_tr_b16 a[164:165], v226 offset:45056\n"
"v_mfma_f32_16x16x32_bf16 v[64:67], a[132:135], a[212:215], v[64:67]\n"
"s_add_u32 m0, s77, 40960\n"
"s_add_u32 s70, s72, 384\n"
"v_mfma_f32_16x16x32_bf16 v[96:99], a[128:131], a[216:219], v[96:99]\n"
"buffer_load_dwordx4 v228, s[40:43], s70 offen lds\n"
"v_mfma_f32_16x16x32_bf16 v[96:99], a[132:135], a[220:223], v[96:99]\n"
"ds_read_b64_tr_b16 a[166:167], v226 offset:45312\n"
"v_mfma_f32_16x16x32_bf16 v[128:131], a[128:131], a[224:227], v[128:131]\n"
"s_add_u32 m0, s77, 41984\n"
"s_add_u32 s70, s72, 10624\n"
"v_mfma_f32_16x16x32_bf16 v[128:131], a[132:135], a[228:231], v[128:131]\n"
"ds_read_b64_tr_b16 a[168:169], v226 offset:40968\n"
"v_mfma_f32_16x16x32_bf16 v[160:163], a[128:131], a[232:235], v[160:163]\n"
"buffer_load_dwordx4 v229, s[40:43], s70 offen lds\n"
"v_mfma_f32_16x16x32_bf16 v[160:163], a[132:135], a[236:239], v[160:163]\n"
"s_add_u32 m0, s77, 43008\n"
"s_add_u32 s70, s72, 20864\n"
"v_mfma_f32_16x16x32_bf16 v[4:7], a[136:139], a[192:195], v[4:7]\n"
"ds_read_b64_tr_b16 a[170:171], v226 offset:41224\n"
"v_mfma_f32_16x16x32_bf16 v[4:7], a[140:143], a[196:199], v[4:7]\n"
"buffer_load_dwordx4 v228, s[40:43], s70 offen lds\n"
"v_mfma_f32_16x16x32_bf16 v[36:39], a[136:139], a[200:203], v[36:39]\n"
"ds_read_b64_tr_b16 a[172:173], v226 offset:45064\n"
"s_add_u32 m0, s78, 24448\n"
"v_mfma_f32_16x16x32_bf16 v[36:39], a[140:143], a[204:207], v[36:39]\n"
"v_mfma_f32_16x16x32_bf16 v[68:71], a[136:139], a[208:211], v[68:71]\n"
"buffer_load_dwordx4 v230, s[44:47], s73 offen offset:128 lds\n"
"v_mfma_f32_16x16x32_bf16 v[68:71], a[140:143], a[212:215], v[68:71]\n"
"ds_read_b64_tr_b16 a[174:175], v226 offset:45320\n"
"s_add_u32 m0, s78, 32384\n"
"v_mfma_f32_16x16x32_bf16 v[100:103], a[136:139], a[216:219], v[100:103]\n"
"v_mfma_f32_16x16x32_bf16 v[100:103], a[140:143], a[220:223], v[100:103]\n"
"ds_read_b64_tr_b16 a[176:177], v226 offset:41472\n"
"v_mfma_f32_16x16x32_bf16 v[132:135], a[136:139], a[224:227], v[132:135]\n"
"buffer_load_dwordx4 v230, s[44:47], s73 offen offset:384 lds\n"
"v_mfma_f32_16x16x32_bf16 v[132:135], a[140:143], a[228:231], v[132:135]\n"
"ds_read_b64_tr_b16 a[178:179], v226 offset:41728\n"
"s_add_u32 s70, s73, 262144\n"
"v_mfma_f32_16x16x32_bf16 v[164:167], a[136:139], a[232:235], v[164:167]\n"
"s_add_u32 m0, s78, 28544\n"
"v_mfma_f32_16x16x32_bf16 v[164:167], a[140:143], a[236:239], v[164:167]\n"
"v_mfma_f32_16x16x32_bf16 v[8:11], a[144:147], a[192:195], v[8:11]\n"
"ds_read_b64_tr_b16 a[180:181], v226 offset:45568\n"
"v_mfma_f32_16x16x32_bf16 v[8:11], a[148:151], a[196:199], v[8:11]\n"
"buffer_load_dwordx4 v230, s[44:47], s70 offen offset:128 lds\n"
"v_mfma_f32_16x16x32_bf16 v[40:43], a[144:147], a[200:203], v[40:43]\n"
"ds_read_b64_tr_b16 a[182:183], v226 offset:45824\n"
"s_add_u32 m0, s78, 36480\n"
"v_mfma_f32_16x16x32_bf16 v[40:43], a[148:151], a[204:207], v[40:43]\n"
"buffer_load_dwordx4 v230, s[44:47], s70 offen offset:384 lds\n"
"v_mfma_f32_16x16x32_bf16 v[72:75], a[144:147], a[208:211], v[72:75]\n"
"v_mfma_f32_16x16x32_bf16 v[72:75], a[148:151], a[212:215], v[72:75]\n"
"ds_read_b64_tr_b16 a[184:185], v226 offset:41480\n"
"s_add_u32 s73, s73, 524288\n"
"v_mfma_f32_16x16x32_bf16 v[104:107], a[144:147], a[216:219], v[104:107]\n"
"v_mfma_f32_16x16x32_bf16 v[104:107], a[148:151], a[220:223], v[104:107]\n"
"ds_read_b64_tr_b16 a[186:187], v226 offset:41736\n"
"v_mfma_f32_16x16x32_bf16 v[136:139], a[144:147], a[224:227], v[136:139]\n"
"v_mfma_f32_16x16x32_bf16 v[136:139], a[148:151], a[228:231], v[136:139]\n"
"v_mfma_f32_16x16x32_bf16 v[168:171], a[144:147], a[232:235], v[168:171]\n"
"ds_read_b64_tr_b16 a[188:189], v226 offset:45576\n"
"v_mfma_f32_16x16x32_bf16 v[168:171], a[148:151], a[236:239], v[168:171]\n"
"v_mfma_f32_16x16x32_bf16 v[12:15], a[152:155], a[192:195], v[12:15]\n"
"ds_read_b64_tr_b16 a[190:191], v226 offset:45832\n"
"v_mfma_f32_16x16x32_bf16 v[12:15], a[156:159], a[196:199], v[12:15]\n"
"v_mfma_f32_16x16x32_bf16 v[44:47], a[152:155], a[200:203], v[44:47]\n"
"v_mfma_f32_16x16x32_bf16 v[44:47], a[156:159], a[204:207], v[44:47]\n"
"v_mfma_f32_16x16x32_bf16 v[76:79], a[152:155], a[208:211], v[76:79]\n"
"v_mfma_f32_16x16x32_bf16 v[76:79], a[156:159], a[212:215], v[76:79]\n"
"v_mfma_f32_16x16x32_bf16 v[108:111], a[152:155], a[216:219], v[108:111]\n"
"v_mfma_f32_16x16x32_bf16 v[108:111], a[156:159], a[220:223], v[108:111]\n"
"v_mfma_f32_16x16x32_bf16 v[140:143], a[152:155], a[224:227], v[140:143]\n"
"v_mfma_f32_16x16x32_bf16 v[140:143], a[156:159], a[228:231], v[140:143]\n"
"v_mfma_f32_16x16x32_bf16 v[172:175], a[152:155], a[232:235], v[172:175]\n"
"v_mfma_f32_16x16x32_bf16 v[172:175], a[156:159], a[236:239], v[172:175]\n"
"s_waitcnt vmcnt(9) lgkmcnt(0)\n"
"s_barrier\n"
"v_mfma_f32_16x16x32_bf16 v[16:19], a[160:163], a[192:195], v[16:19]\n"
"ds_read_b128 a[0:3], v224 offset:0\n"
"v_mfma_f32_16x16x32_bf16 v[16:19], a[164:167], a[196:199], v[16:19]\n"
"s_add_u32 m0, s77, 44032\n"
"s_add_u32 s70, s72, 31104\n"
"v_mfma_f32_16x16x32_bf16 v[48:51], a[160:163], a[200:203], v[48:51]\n"
"ds_read_b128 a[4:7], v225 offset:0\n"
"ds_read_b128 a[8:11], v224 offset:2048\n"
"v_mfma_f32_16x16x32_bf16 v[48:51], a[164:167], a[204:207], v[48:51]\n"
"buffer_load_dwordx4 v229, s[40:43], s70 offen lds\n"
"v_mfma_f32_16x16x32_bf16 v[80:83], a[160:163], a[208:211], v[80:83]\n"
"s_add_u32 m0, s77, 45056\n"
"s_add_u32 s70, s72, 41344\n"
"v_mfma_f32_16x16x32_bf16 v[80:83], a[164:167], a[212:215], v[80:83]\n"
"ds_read_b128 a[12:15], v225 offset:2048\n"
"ds_read_b128 a[16:19], v224 offset:4096\n"
"v_mfma_f32_16x16x32_bf16 v[112:115], a[160:163], a[216:219], v[112:115]\n"
"buffer_load_dwordx4 v228, s[40:43], s70 offen lds\n"
"v_mfma_f32_16x16x32_bf16 v[112:115], a[164:167], a[220:223], v[112:115]\n"
"ds_read_b128 a[20:23], v225 offset:4096\n"
"v_mfma_f32_16x16x32_bf16 v[144:147], a[160:163], a[224:227], v[144:147]\n"
"s_add_u32 m0, s77, 46080\n"
"s_add_u32 s70, s72, 51584\n"
"v_mfma_f32_16x16x32_bf16 v[144:147], a[164:167], a[228:231], v[144:147]\n"
"ds_read_b128 a[24:27], v234 offset:0\n"
"ds_read_b128 a[28:31], v235 offset:0\n"
"v_mfma_f32_16x16x32_bf16 v[176:179], a[160:163], a[232:235], v[176:179]\n"
"buffer_load_dwordx4 v229, s[40:43], s70 offen lds\n"
"v_mfma_f32_16x16x32_bf16 v[176:179], a[164:167], a[236:239], v[176:179]\n"
"s_add_u32 m0, s78, 65536\n"
"ds_read_b128 a[32:35], v234 offset:2048\n"
"v_mfma_f32_16x16x32_bf16 v[20:23], a[168:171], a[192:195], v[20:23]\n"
"ds_read_b128 a[36:39], v235 offset:2048\n"
"v_mfma_f32_16x16x32_bf16 v[20:23], a[172:175], a[196:199], v[20:23]\n"
"buffer_load_dwordx4 v230, s[44:47], s73 offen lds\n"
"v_mfma_f32_16x16x32_bf16 v[52:55], a[168:171], a[200:203], v[52:55]\n"
"ds_read_b128 a[40:43], v234 offset:4096\n"
"s_add_u32 m0, s78, 73472\n"
"v_mfma_f32_16x16x32_bf16 v[52:55], a[172:175], a[204:207], v[52:55]\n"
"ds_read_b128 a[44:47], v235 offset:4096\n"
"v_mfma_f32_16x16x32_bf16 v[84:87], a[168:171], a[208:211], v[84:87]\n"
"ds_read_b64_tr_b16 a[128:129], v226 offset:57344\n"
"v_mfma_f32_16x16x32_bf16 v[84:87], a[172:175], a[212:215], v[84:87]\n"
"buffer_load_dwordx4 v230, s[44:47], s73 offen offset:256 lds\n"
"v_mfma_f32_16x16x32_bf16 v[116:119], a[168:171], a[216:219], v[116:119]\n"
"ds_read_b64_tr_b16 a[130:131], v226 offset:57600\n"
"s_add_u32 s70, s73, 262144\n"
"v_mfma_f32_16x16x32_bf16 v[116:119], a[172:175], a[220:223], v[116:119]\n"
"ds_read_b64_tr_b16 a[132:133], v226 offset:61440\n"
"v_mfma_f32_16x16x32_bf16 v[148:151], a[168:171], a[224:227], v[148:151]\n"
"ds_read_b64_tr_b16 a[134:135], v226 offset:61696\n"
"s_add_u32 m0, s78, 69632\n"
"v_mfma_f32_16x16x32_bf16 v[148:151], a[172:175], a[228:231], v[148:151]\n"
"buffer_load_dwordx4 v230, s[44:47], s70 offen lds\n"
"v_mfma_f32_16x16x32_bf16 v[180:183], a[168:171], a[232:235], v[180:183]\n"
"ds_read_b64_tr_b16 a[136:137], v226 offset:57352\n"
"v_mfma_f32_16x16x32_bf16 v[180:183], a[172:175], a[236:239], v[180:183]\n"
"ds_read_b64_tr_b16 a[138:139], v226 offset:57608\n"
"s_add_u32 m0, s78, 77568\n"
"v_mfma_f32_16x16x32_bf16 v[24:27], a[176:179], a[192:195], v[24:27]\n"
"v_mfma_f32_16x16x32_bf16 v[24:27], a[180:183], a[196:199], v[24:27]\n"
"ds_read_b64_tr_b16 a[140:141], v226 offset:61448\n"
"v_mfma_f32_16x16x32_bf16 v[56:59], a[176:179], a[200:203], v[56:59]\n"
"buffer_load_dwordx4 v230, s[44:47], s70 offen offset:256 lds\n"
"v_mfma_f32_16x16x32_bf16 v[56:59], a[180:183], a[204:207], v[56:59]\n"
"ds_read_b64_tr_b16 a[142:143], v226 offset:61704\n"
"ds_read_b64_tr_b16 a[144:145], v226 offset:57856\n"
"v_mfma_f32_16x16x32_bf16 v[88:91], a[176:179], a[208:211], v[88:91]\n"
"s_add_u32 m0, s67, 896\n"
"v_mfma_f32_16x16x32_bf16 v[88:91], a[180:183], a[212:215], v[88:91]\n"
"ds_read_b64_tr_b16 a[146:147], v226 offset:58112\n"
"v_mfma_f32_16x16x32_bf16 v[120:123], a[176:179], a[216:219], v[120:123]\n"
"buffer_load_dwordx4 v245, s[48:51], s64 offen offset:2176 nt lds\n"
"s_sub_u32 m0, m0, 896\n"
"s_nop 0\n"
"buffer_load_dwordx4 v245, s[48:51], s64 offen offset:2048 nt lds\n"
"v_mfma_f32_16x16x32_bf16 v[120:123], a[180:183], a[220:223], v[120:123]\n"
"ds_read_b64_tr_b16 a[148:149], v226 offset:61952\n"
"v_mfma_f32_16x16x32_bf16 v[152:155], a[176:179], a[224:227], v[152:155]\n"
"ds_read_b64_tr_b16 a[150:151], v226 offset:62208\n"
"v_mfma_f32_16x16x32_bf16 v[152:155], a[180:183], a[228:231], v[152:155]\n"
"ds_read_b64_tr_b16 a[152:153], v226 offset:57864\n"
"v_mfma_f32_16x16x32_bf16 v[184:187], a[176:179], a[232:235], v[184:187]\n"
"ds_read_b64_tr_b16 a[154:155], v226 offset:58120\n"
"v_mfma_f32_16x16x32_bf16 v[184:187], a[180:183], a[236:239], v[184:187]\n"
"v_mfma_f32_16x16x32_bf16 v[28:31], a[184:187], a[192:195], v[28:31]\n"
"ds_read_b64_tr_b16 a[156:157], v226 offset:61960\n"
"v_mfma_f32_16x16x32_bf16 v[28:31], a[188:191], a[196:199], v[28:31]\n"
"ds_read_b64_tr_b16 a[158:159], v226 offset:62216\n"
"v_mfma_f32_16x16x32_bf16 v[60:63], a[184:187], a[200:203], v[60:63]\n"
"v_mfma_f32_16x16x32_bf16 v[60:63], a[188:191], a[204:207], v[60:63]\n"
"v_mfma_f32_16x16x32_bf16 v[92:95], a[184:187], a[208:211], v[92:95]\n"
"v_mfma_f32_16x16x32_bf16 v[92:95], a[188:191], a[212:215], v[92:95]\n"
"v_mfma_f32_16x16x32_bf16 v[124:127], a[184:187], a[216:219], v[124:127]\n"
"v_mfma_f32_16x16x32_bf16 v[124:127], a[188:191], a[220:223], v[124:127]\n"
"v_mfma_f32_16x16x32_bf16 v[156:159], a[184:187], a[224:227], v[156:159]\n"
"v_mfma_f32_16x16x32_bf16 v[156:159], a[188:191], a[228:231], v[156:159]\n"
"v_mfma_f32_16x16x32_bf16 v[188:191], a[184:187], a[232:235], v[188:191]\n"
"v_mfma_f32_16x16x32_bf16 v[188:191], a[188:191], a[236:239], v[188:191]\n"
"s_waitcnt vmcnt(9) lgkmcnt(0)\n"
"s_barrier\n"
"v_mfma_f32_16x16x32_bf16 v[0:3], a[128:131], a[0:3], v[0:3]\n"
"ds_read_b64_tr_b16 a[160:161], v226 offset:0\n"
"v_mfma_f32_16x16x32_bf16 v[0:3], a[132:135], a[4:7], v[0:3]\n"
"s_add_u32 m0, s77, 0\n"
"s_add_u32 s70, s72, 512\n"
"v_mfma_f32_16x16x32_bf16 v[32:35], a[128:131], a[8:11], v[32:35]\n"
"buffer_load_dwordx4 v228, s[40:43], s70 offen lds\n"
"v_mfma_f32_16x16x32_bf16 v[32:35], a[132:135], a[12:15], v[32:35]\n"
"ds_read_b64_tr_b16 a[162:163], v226 offset:256\n"
"v_mfma_f32_16x16x32_bf16 v[64:67], a[128:131], a[16:19], v[64:67]\n"
"s_add_u32 m0, s77, 1024\n"
"s_add_u32 s70, s72, 10752\n"
"v_mfma_f32_16x16x32_bf16 v[64:67], a[132:135], a[20:23], v[64:67]\n"
"ds_read_b64_tr_b16 a[164:165], v226 offset:4096\n"
"v_mfma_f32_16x16x32_bf16 v[96:99], a[128:131], a[24:27], v[96:99]\n"
"buffer_load_dwordx4 v229, s[40:43], s70 offen lds\n"
"v_mfma_f32_16x16x32_bf16 v[96:99], a[132:135], a[28:31], v[96:99]\n"
"ds_read_b64_tr_b16 a[166:167], v226 offset:4352\n"
"v_mfma_f32_16x16x32_bf16 v[128:131], a[128:131], a[32:35], v[128:131]\n"
"s_add_u32 m0, s77, 2048\n"
"s_add_u32 s70, s72, 20992\n"
"v_mfma_f32_16x16x32_bf16 v[128:131], a[132:135], a[36:39], v[128:131]\n"
"ds_read_b64_tr_b16 a[168:169], v226 offset:8\n"
"v_mfma_f32_16x16x32_bf16 v[160:163], a[128:131], a[40:43], v[160:163]\n"
"buffer_load_dwordx4 v228, s[40:43], s70 offen lds\n"
"v_mfma_f32_16x16x32_bf16 v[160:163], a[132:135], a[44:47], v[160:163]\n"
"s_add_u32 m0, s78, 81792\n"
"v_mfma_f32_16x16x32_bf16 v[4:7], a[136:139], a[0:3], v[4:7]\n"
"ds_read_b64_tr_b16 a[170:171], v226 offset:264\n"
"v_mfma_f32_16x16x32_bf16 v[4:7], a[140:143], a[4:7], v[4:7]\n"
"buffer_load_dwordx4 v230, s[44:47], s73 offen offset:128 lds\n"
"v_mfma_f32_16x16x32_bf16 v[36:39], a[136:139], a[8:11], v[36:39]\n"
"ds_read_b64_tr_b16 a[172:173], v226 offset:4104\n"
"v_mfma_f32_16x16x32_bf16 v[36:39], a[140:143], a[12:15], v[36:39]\n"
"s_add_u32 m0, s78, 89728\n"
"v_mfma_f32_16x16x32_bf16 v[68:71], a[136:139], a[16:19], v[68:71]\n"
"v_mfma_f32_16x16x32_bf16 v[68:71], a[140:143], a[20:23], v[68:71]\n"
"ds_read_b64_tr_b16 a[174:175], v226 offset:4360\n"
"v_mfma_f32_16x16x32_bf16 v[100:103], a[136:139], a[24:27], v[100:103]\n"
"buffer_load_dwordx4 v230, s[44:47], s73 offen offset:384 lds\n"
"v_mfma_f32_16x16x32_bf16 v[100:103], a[140:143], a[28:31], v[100:103]\n"
"ds_read_b64_tr_b16 a[176:177], v226 offset:512\n"
"s_add_u32 s70, s73, 262144\n"
"v_mfma_f32_16x16x32_bf16 v[132:135], a[136:139], a[32:35], v[132:135]\n"
"v_mfma_f32_16x16x32_bf16 v[132:135], a[140:143], a[36:39], v[132:135]\n"
"ds_read_b64_tr_b16 a[178:179], v226 offset:768\n"
"s_add_u32 m0, s78, 85888\n"
"v_mfma_f32_16x16x32_bf16 v[164:167], a[136:139], a[40:43], v[164:167]\n"
"v_mfma_f32_16x16x32_bf16 v[164:167], a[140:143], a[44:47], v[164:167]\n"
"buffer_load_dwordx4 v230, s[44:47], s70 offen offset:128 lds\n"
"v_mfma_f32_16x16x32_bf16 v[8:11], a[144:147], a[0:3], v[8:11]\n"
"ds_read_b64_tr_b16 a[180:181], v226 offset:4608\n"
"v_mfma_f32_16x16x32_bf16 v[8:11], a[148:151], a[4:7], v[8:11]\n"
"s_add_u32 m0, s78, 93824\n"
"v_mfma_f32_16x16x32_bf16 v[40:43], a[144:147], a[8:11], v[40:43]\n"
"ds_read_b64_tr_b16 a[182:183], v226 offset:4864\n"
"v_mfma_f32_16x16x32_bf16 v[40:43], a[148:151], a[12:15], v[40:43]\n"
"buffer_load_dwordx4 v230, s[44:47], s70 offen offset:384 lds\n"
"v_mfma_f32_16x16x32_bf16 v[72:75], a[144:147], a[16:19], v[72:75]\n"
"v_mfma_f32_16x16x32_bf16 v[72:75], a[148:151], a[20:23], v[72:75]\n"
"ds_read_b64_tr_b16 a[184:185], v226 offset:520\n"
"s_add_u32 s73, s73, 524288\n"
"v_mfma_f32_16x16x32_bf16 v[104:107], a[144:147], a[24:27], v[104:107]\n"
"v_mfma_f32_16x16x32_bf16 v[104:107], a[148:151], a[28:31], v[104:107]\n"
"ds_read_b64_tr_b16 a[186:187], v226 offset:776\n"
"v_mfma_f32_16x16x32_bf16 v[136:139], a[144:147], a[32:35], v[136:139]\n"
"v_mfma_f32_16x16x32_bf16 v[136:139], a[148:151], a[36:39], v[136:139]\n"
"v_mfma_f32_16x16x32_bf16 v[168:171], a[144:147], a[40:43], v[168:171]\n"
"ds_read_b64_tr_b16 a[188:189], v226 offset:4616\n"
"v_mfma_f32_16x16x32_bf16 v[168:171], a[148:151], a[44:47], v[168:171]\n"
"v_mfma_f32_16x16x32_bf16 v[12:15], a[152:155], a[0:3], v[12:15]\n"
"ds_read_b64_tr_b16 a[190:191], v226 offset:4872\n"
"v_mfma_f32_16x16x32_bf16 v[12:15], a[156:159], a[4:7], v[12:15]\n"
"v_mfma_f32_16x16x32_bf16 v[44:47], a[152:155], a[8:11], v[44:47]\n"
"v_mfma_f32_16x16x32_bf16 v[44:47], a[156:159], a[12:15], v[44:47]\n"
"v_mfma_f32_16x16x32_bf16 v[76:79], a[152:155], a[16:19], v[76:79]\n"
"v_mfma_f32_16x16x32_bf16 v[76:79], a[156:159], a[20:23], v[76:79]\n"
"v_mfma_f32_16x16x32_bf16 v[108:111], a[152:155], a[24:27], v[108:111]\n"
"v_mfma_f32_16x16x32_bf16 v[108:111], a[156:159], a[28:31], v[108:111]\n"
"v_mfma_f32_16x16x32_bf16 v[140:143], a[152:155], a[32:35], v[140:143]\n"
"v_mfma_f32_16x16x32_bf16 v[140:143], a[156:159], a[36:39], v[140:143]\n"
"v_mfma_f32_16x16x32_bf16 v[172:175], a[152:155], a[40:43], v[172:175]\n"
"v_mfma_f32_16x16x32_bf16 v[172:175], a[156:159], a[44:47], v[172:175]\n"
"s_waitcnt vmcnt(9) lgkmcnt(0)\n"
"s_barrier\n"
"v_mfma_f32_16x16x32_bf16 v[16:19], a[160:163], a[0:3], v[16:19]\n"
"ds_read_b128 a[192:195], v224 offset:40960\n"
"v_mfma_f32_16x16x32_bf16 v[16:19], a[164:167], a[4:7], v[16:19]\n"
"s_add_u32 m0, s77, 3072\n"
"s_add_u32 s70, s72, 31232\n"
"v_mfma_f32_16x16x32_bf16 v[48:51], a[160:163], a[8:11], v[48:51]\n"
"ds_read_b128 a[196:199], v225 offset:40960\n"
"ds_read_b128 a[200:203], v224 offset:43008\n"
"v_mfma_f32_16x16x32_bf16 v[48:51], a[164:167], a[12:15], v[48:51]\n"
"buffer_load_dwordx4 v229, s[40:43], s70 offen lds\n"
"v_mfma_f32_16x16x32_bf16 v[80:83], a[160:163], a[16:19], v[80:83]\n"
"s_add_u32 m0, s77, 4096\n"
"s_add_u32 s70, s72, 41472\n"
"v_mfma_f32_16x16x32_bf16 v[80:83], a[164:167], a[20:23], v[80:83]\n"
"ds_read_b128 a[204:207], v225 offset:43008\n"
"ds_read_b128 a[208:211], v224 offset:45056\n"
"v_mfma_f32_16x16x32_bf16 v[112:115], a[160:163], a[24:27], v[112:115]\n"
"buffer_load_dwordx4 v228, s[40:43], s70 offen lds\n"
"v_mfma_f32_16x16x32_bf16 v[112:115], a[164:167], a[28:31], v[112:115]\n"
"ds_read_b128 a[212:215], v225 offset:45056\n"
"v_mfma_f32_16x16x32_bf16 v[144:147], a[160:163], a[32:35], v[144:147]\n"
"s_add_u32 m0, s77, 5120\n"
"s_add_u32 s70, s72, 51712\n"
"v_mfma_f32_16x16x32_bf16 v[144:147], a[164:167], a[36:39], v[144:147]\n"
"ds_read_b128 a[216:219], v234 offset:40960\n"
"ds_read_b128 a[220:223], v235 offset:40960\n"
"v_mfma_f32_16x16x32_bf16 v[176:179], a[160:163], a[40:43], v[176:179]\n"
"buffer_load_dwordx4 v229, s[40:43], s70 offen lds\n"
"v_mfma_f32_16x16x32_bf16 v[176:179], a[164:167], a[44:47], v[176:179]\n"
"s_add_u32 m0, s78, 24576\n"
"ds_read_b128 a[224:227], v234 offset:43008\n"
"v_mfma_f32_16x16x32_bf16 v[20:23], a[168:171], a[0:3], v[20:23]\n"
"ds_read_b128 a[228:231], v235 offset:43008\n"
"v_mfma_f32_16x16x32_bf16 v[20:23], a[172:175], a[4:7], v[20:23]\n"
"buffer_load_dwordx4 v230, s[44:47], s73 offen lds\n"
"v_mfma_f32_16x16x32_bf16 v[52:55], a[168:171], a[8:11], v[52:55]\n"
"ds_read_b128 a[232:235], v234 offset:45056\n"
"s_add_u32 m0, s78, 32512\n"
"v_mfma_f32_16x16x32_bf16 v[52:55], a[172:175], a[12:15], v[52:55]\n"
"ds_read_b128 a[236:239], v235 offset:45056\n"
"v_mfma_f32_16x16x32_bf16 v[84:87], a[168:171], a[16:19], v[84:87]\n"
"buffer_load_dwordx4 v230, s[44:47], s73 offen offset:256 lds\n"
"v_mfma_f32_16x16x32_bf16 v[84:87], a[172:175], a[20:23], v[84:87]\n"
"ds_read_b64_tr_b16 a[128:129], v226 offset:40960\n"
"s_add_u32 s70, s73, 262144\n"
"v_mfma_f32_16x16x32_bf16 v[116:119], a[168:171], a[24:27], v[116:119]\n"
"ds_read_b64_tr_b16 a[130:131], v226 offset:41216\n"
"s_add_u32 m0, s78, 28672\n"
"v_mfma_f32_16x16x32_bf16 v[116:119], a[172:175], a[28:31], v[116:119]\n"
"ds_read_b64_tr_b16 a[132:133], v226 offset:45056\n"
"v_mfma_f32_16x16x32_bf16 v[148:151], a[168:171], a[32:35], v[148:151]\n"
"ds_read_b64_tr_b16 a[134:135], v226 offset:45312\n"
"v_mfma_f32_16x16x32_bf16 v[148:151], a[172:175], a[36:39], v[148:151]\n"
"buffer_load_dwordx4 v230, s[44:47], s70 offen lds\n"
"v_mfma_f32_16x16x32_bf16 v[180:183], a[168:171], a[40:43], v[180:183]\n"
"ds_read_b64_tr_b16 a[136:137], v226 offset:40968\n"
"s_add_u32 m0, s78, 36608\n"
"v_mfma_f32_16x16x32_bf16 v[180:183], a[172:175], a[44:47], v[180:183]\n"
"ds_read_b64_tr_b16 a[138:139], v226 offset:41224\n"
"v_mfma_f32_16x16x32_bf16 v[24:27], a[176:179], a[0:3], v[24:27]\n"
"buffer_load_dwordx4 v230, s[44:47], s70 offen offset:256 lds\n"
"v_mfma_f32_16x16x32_bf16 v[24:27], a[180:183], a[4:7], v[24:27]\n"
"ds_read_b64_tr_b16 a[140:141], v226 offset:45064\n"
"s_add_u32 m0, s67, 4096\n"
"v_mfma_f32_16x16x32_bf16 v[56:59], a[176:179], a[8:11], v[56:59]\n"
"ds_read_b64_tr_b16 a[142:143], v226 offset:45320\n"
"v_mfma_f32_16x16x32_bf16 v[56:59], a[180:183], a[12:15], v[56:59]\n"
"ds_read_b64_tr_b16 a[144:145], v226 offset:41472\n"
"s_add_u32 s64, s64, 4096\n"
"v_mfma_f32_16x16x32_bf16 v[88:91], a[176:179], a[16:19], v[88:91]\n"
"v_mfma_f32_16x16x32_bf16 v[88:91], a[180:183], a[20:23], v[88:91]\n"
"ds_read_b64_tr_b16 a[146:147], v226 offset:41728\n"
"v_mfma_f32_16x16x32_bf16 v[120:123], a[176:179], a[24:27], v[120:123]\n"
"buffer_load_dwordx4 v244, s[48:51], s64 offen nt lds\n"
"s_add_u32 m0, m0, 896\n"
"s_nop 0\n"
"buffer_load_dwordx4 v244, s[48:51], s64 offen offset:128 nt lds\n"
"v_mfma_f32_16x16x32_bf16 v[120:123], a[180:183], a[28:31], v[120:123]\n"
"ds_read_b64_tr_b16 a[148:149], v226 offset:45568\n"
"v_mfma_f32_16x16x32_bf16 v[152:155], a[176:179], a[32:35], v[152:155]\n"
"ds_read_b64_tr_b16 a[150:151], v226 offset:45824\n"
"v_mfma_f32_16x16x32_bf16 v[152:155], a[180:183], a[36:39], v[152:155]\n"
"ds_read_b64_tr_b16 a[152:153], v226 offset:41480\n"
"v_mfma_f32_16x16x32_bf16 v[184:187], a[176:179], a[40:43], v[184:187]\n"
"ds_read_b64_tr_b16 a[154:155], v226 offset:41736\n"
"v_mfma_f32_16x16x32_bf16 v[184:187], a[180:183], a[44:47], v[184:187]\n"
"v_mfma_f32_16x16x32_bf16 v[28:31], a[184:187], a[0:3], v[28:31]\n"
"ds_read_b64_tr_b16 a[156:157], v226 offset:45576\n"
"v_mfma_f32_16x16x32_bf16 v[28:31], a[188:191], a[4:7], v[28:31]\n"
"ds_read_b64_tr_b16 a[158:159], v226 offset:45832\n"
"v_mfma_f32_16x16x32_bf16 v[60:63], a[184:187], a[8:11], v[60:63]\n"
"v_mfma_f32_16x16x32_bf16 v[60:63], a[188:191], a[12:15], v[60:63]\n"
"v_mfma_f32_16x16x32_bf16 v[92:95], a[184:187], a[16:19], v[92:95]\n"
"v_mfma_f32_16x16x32_bf16 v[92:95], a[188:191], a[20:23], v[92:95]\n"
"v_mfma_f32_16x16x32_bf16 v[124:127], a[184:187], a[24:27], v[124:127]\n"
"v_mfma_f32_16x16x32_bf16 v[124:127], a[188:191], a[28:31], v[124:127]\n"
"v_mfma_f32_16x16x32_bf16 v[156:159], a[184:187], a[32:35], v[156:159]\n"
"v_mfma_f32_16x16x32_bf16 v[156:159], a[188:191], a[36:39], v[156:159]\n"
"v_mfma_f32_16x16x32_bf16 v[188:191], a[184:187], a[40:43], v[188:191]\n"
"v_mfma_f32_16x16x32_bf16 v[188:191], a[188:191], a[44:47], v[188:191]\n"
"s_waitcnt vmcnt(9) lgkmcnt(0)\n"
"s_barrier\n"
"v_mfma_f32_16x16x32_bf16 v[0:3], a[128:131], a[192:195], v[0:3]\n"
"ds_read_b64_tr_b16 a[160:161], v226 offset:57344\n"
"v_mfma_f32_16x16x32_bf16 v[0:3], a[132:135], a[196:199], v[0:3]\n"
"s_add_u32 m0, s77, 40960\n"
"s_add_u32 s70, s72, 640\n"
"v_mfma_f32_16x16x32_bf16 v[32:35], a[128:131], a[200:203], v[32:35]\n"
"buffer_load_dwordx4 v228, s[40:43], s70 offen lds\n"
"v_mfma_f32_16x16x32_bf16 v[32:35], a[132:135], a[204:207], v[32:35]\n"
"ds_read_b64_tr_b16 a[162:163], v226 offset:57600\n"
"v_mfma_f32_16x16x32_bf16 v[64:67], a[128:131], a[208:211], v[64:67]\n"
"s_add_u32 m0, s77, 41984\n"
"s_add_u32 s70, s72, 10880\n"
"v_mfma_f32_16x16x32_bf16 v[64:67], a[132:135], a[212:215], v[64:67]\n"
"ds_read_b64_tr_b16 a[164:165], v226 offset:61440\n"
"v_mfma_f32_16x16x32_bf16 v[96:99], a[128:131], a[216:219], v[96:99]\n"
"buffer_load_dwordx4 v229, s[40:43], s70 offen lds\n"
"v_mfma_f32_16x16x32_bf16 v[96:99], a[132:135], a[220:223], v[96:99]\n"
"ds_read_b64_tr_b16 a[166:167], v226 offset:61696\n"
"v_mfma_f32_16x16x32_bf16 v[128:131], a[128:131], a[224:227], v[128:131]\n"
"s_add_u32 m0, s77, 43008\n"
"s_add_u32 s70, s72, 21120\n"
"v_mfma_f32_16x16x32_bf16 v[128:131], a[132:135], a[228:231], v[128:131]\n"
"ds_read_b64_tr_b16 a[168:169], v226 offset:57352\n"
"v_mfma_f32_16x16x32_bf16 v[160:163], a[128:131], a[232:235], v[160:163]\n"
"buffer_load_dwordx4 v228, s[40:43], s70 offen lds\n"
"v_mfma_f32_16x16x32_bf16 v[160:163], a[132:135], a[236:239], v[160:163]\n"
"s_add_u32 m0, s78, 65408\n"
"v_mfma_f32_16x16x32_bf16 v[4:7], a[136:139], a[192:195], v[4:7]\n"
"ds_read_b64_tr_b16 a[170:171], v226 offset:57608\n"
"v_mfma_f32_16x16x32_bf16 v[4:7], a[140:143], a[196:199], v[4:7]\n"
"buffer_load_dwordx4 v230, s[44:47], s73 offen offset:128 lds\n"
"v_mfma_f32_16x16x32_bf16 v[36:39], a[136:139], a[200:203], v[36:39]\n"
"ds_read_b64_tr_b16 a[172:173], v226 offset:61448\n"
"v_mfma_f32_16x16x32_bf16 v[36:39], a[140:143], a[204:207], v[36:39]\n"
"s_add_u32 m0, s78, 73344\n"
"v_mfma_f32_16x16x32_bf16 v[68:71], a[136:139], a[208:211], v[68:71]\n"
"v_mfma_f32_16x16x32_bf16 v[68:71], a[140:143], a[212:215], v[68:71]\n"
"ds_read_b64_tr_b16 a[174:175], v226 offset:61704\n"
"v_mfma_f32_16x16x32_bf16 v[100:103], a[136:139], a[216:219], v[100:103]\n"
"buffer_load_dwordx4 v230, s[44:47], s73 offen offset:384 lds\n"
"v_mfma_f32_16x16x32_bf16 v[100:103], a[140:143], a[220:223], v[100:103]\n"
"ds_read_b64_tr_b16 a[176:177], v226 offset:57856\n"
"s_add_u32 s70, s73, 262144\n"
"v_mfma_f32_16x16x32_bf16 v[132:135], a[136:139], a[224:227], v[132:135]\n"
"v_mfma_f32_16x16x32_bf16 v[132:135], a[140:143], a[228:231], v[132:135]\n"
"ds_read_b64_tr_b16 a[178:179], v226 offset:58112\n"
"s_add_u32 m0, s78, 69504\n"
"v_mfma_f32_16x16x32_bf16 v[164:167], a[136:139], a[232:235], v[164:167]\n"
"v_mfma_f32_16x16x32_bf16 v[164:167], a[140:143], a[236:239], v[164:167]\n"
"buffer_load_dwordx4 v230, s[44:47], s70 offen offset:128 lds\n"
"v_mfma_f32_16x16x32_bf16 v[8:11], a[144:147], a[192:195], v[8:11]\n"
"ds_read_b64_tr_b16 a[180:181], v226 offset:61952\n"
"v_mfma_f32_16x16x32_bf16 v[8:11], a[148:151], a[196:199], v[8:11]\n"
"s_add_u32 m0, s78, 77440\n"
"v_mfma_f32_16x16x32_bf16 v[40:43], a[144:147], a[200:203], v[40:43]\n"
"ds_read_b64_tr_b16 a[182:183], v226 offset:62208\n"
"v_mfma_f32_16x16x32_bf16 v[40:43], a[148:151], a[204:207], v[40:43]\n"
"buffer_load_dwordx4 v230, s[44:47], s70 offen offset:384 lds\n"
"v_mfma_f32_16x16x32_bf16 v[72:75], a[144:147], a[208:211], v[72:75]\n"
"v_mfma_f32_16x16x32_bf16 v[72:75], a[148:151], a[212:215], v[72:75]\n"
"ds_read_b64_tr_b16 a[184:185], v226 offset:57864\n"
"s_add_u32 s73, s73, 524288\n"
"v_mfma_f32_16x16x32_bf16 v[104:107], a[144:147], a[216:219], v[104:107]\n"
"v_mfma_f32_16x16x32_bf16 v[104:107], a[148:151], a[220:223], v[104:107]\n"
"ds_read_b64_tr_b16 a[186:187], v226 offset:58120\n"
"v_mfma_f32_16x16x32_bf16 v[136:139], a[144:147], a[224:227], v[136:139]\n"
"v_mfma_f32_16x16x32_bf16 v[136:139], a[148:151], a[228:231], v[136:139]\n"
"v_mfma_f32_16x16x32_bf16 v[168:171], a[144:147], a[232:235], v[168:171]\n"
"ds_read_b64_tr_b16 a[188:189], v226 offset:61960\n"
"v_mfma_f32_16x16x32_bf16 v[168:171], a[148:151], a[236:239], v[168:171]\n"
"v_mfma_f32_16x16x32_bf16 v[12:15], a[152:155], a[192:195], v[12:15]\n"
"ds_read_b64_tr_b16 a[190:191], v226 offset:62216\n"
"v_mfma_f32_16x16x32_bf16 v[12:15], a[156:159], a[196:199], v[12:15]\n"
"v_mfma_f32_16x16x32_bf16 v[44:47], a[152:155], a[200:203], v[44:47]\n"
"v_mfma_f32_16x16x32_bf16 v[44:47], a[156:159], a[204:207], v[44:47]\n"
"v_mfma_f32_16x16x32_bf16 v[76:79], a[152:155], a[208:211], v[76:79]\n"
"v_mfma_f32_16x16x32_bf16 v[76:79], a[156:159], a[212:215], v[76:79]\n"
"v_mfma_f32_16x16x32_bf16 v[108:111], a[152:155], a[216:219], v[108:111]\n"
"v_mfma_f32_16x16x32_bf16 v[108:111], a[156:159], a[220:223], v[108:111]\n"
"v_mfma_f32_16x16x32_bf16 v[140:143], a[152:155], a[224:227], v[140:143]\n"
"v_mfma_f32_16x16x32_bf16 v[140:143], a[156:159], a[228:231], v[140:143]\n"
"v_mfma_f32_16x16x32_bf16 v[172:175], a[152:155], a[232:235], v[172:175]\n"
"v_mfma_f32_16x16x32_bf16 v[172:175], a[156:159], a[236:239], v[172:175]\n"
"s_waitcnt vmcnt(9) lgkmcnt(0)\n"
"s_barrier\n"
"v_mfma_f32_16x16x32_bf16 v[16:19], a[160:163], a[192:195], v[16:19]\n"
"ds_read_b128 a[0:3], v224 offset:0\n"
"v_mfma_f32_16x16x32_bf16 v[16:19], a[164:167], a[196:199], v[16:19]\n"
"s_add_u32 m0, s77, 44032\n"
"s_add_u32 s70, s72, 31360\n"
"v_mfma_f32_16x16x32_bf16 v[48:51], a[160:163], a[200:203], v[48:51]\n"
"ds_read_b128 a[4:7], v225 offset:0\n"
"ds_read_b128 a[8:11], v224 offset:2048\n"
"v_mfma_f32_16x16x32_bf16 v[48:51], a[164:167], a[204:207], v[48:51]\n"
"buffer_load_dwordx4 v229, s[40:43], s70 offen lds\n"
"v_mfma_f32_16x16x32_bf16 v[80:83], a[160:163], a[208:211], v[80:83]\n"
"s_add_u32 m0, s77, 45056\n"
"s_add_u32 s70, s72, 41600\n"
"v_mfma_f32_16x16x32_bf16 v[80:83], a[164:167], a[212:215], v[80:83]\n"
"ds_read_b128 a[12:15], v225 offset:2048\n"
"ds_read_b128 a[16:19], v224 offset:4096\n"
"v_mfma_f32_16x16x32_bf16 v[112:115], a[160:163], a[216:219], v[112:115]\n"
"buffer_load_dwordx4 v228, s[40:43], s70 offen lds\n"
"v_mfma_f32_16x16x32_bf16 v[112:115], a[164:167], a[220:223], v[112:115]\n"
"s_add_u32 m0, s77, 46080\n"
"s_add_u32 s70, s72, 51840\n"
"v_mfma_f32_16x16x32_bf16 v[144:147], a[160:163], a[224:227], v[144:147]\n"
"ds_read_b128 a[20:23], v225 offset:4096\n"
"ds_read_b128 a[24:27], v234 offset:0\n"
"v_mfma_f32_16x16x32_bf16 v[144:147], a[164:167], a[228:231], v[144:147]\n"
"buffer_load_dwordx4 v229, s[40:43], s70 offen lds\n"
"v_mfma_f32_16x16x32_bf16 v[176:179], a[160:163], a[232:235], v[176:179]\n"
"s_add_u32 m0, s78, 81920\n"
"ds_read_b128 a[28:31], v235 offset:0\n"
"v_mfma_f32_16x16x32_bf16 v[176:179], a[164:167], a[236:239], v[176:179]\n"
"buffer_load_dwordx4 v230, s[44:47], s73 offen lds\n"
"v_mfma_f32_16x16x32_bf16 v[20:23], a[168:171], a[192:195], v[20:23]\n"
"ds_read_b128 a[32:35], v234 offset:2048\n"
"s_add_u32 m0, s78, 89856\n"
"v_mfma_f32_16x16x32_bf16 v[20:23], a[172:175], a[196:199], v[20:23]\n"
"ds_read_b128 a[36:39], v235 offset:2048\n"
"ds_read_b128 a[40:43], v234 offset:4096\n"
"v_mfma_f32_16x16x32_bf16 v[52:55], a[168:171], a[200:203], v[52:55]\n"
"buffer_load_dwordx4 v230, s[44:47], s73 offen offset:256 lds\n"
"v_mfma_f32_16x16x32_bf16 v[52:55], a[172:175], a[204:207], v[52:55]\n"
"s_add_u32 s70, s73, 262144\n"
"ds_read_b128 a[44:47], v235 offset:4096\n"
"v_mfma_f32_16x16x32_bf16 v[84:87], a[168:171], a[208:211], v[84:87]\n"
"s_add_u32 m0, s78, 86016\n"
"ds_read_b64_tr_b16 a[128:129], v226 offset:0\n"
"v_mfma_f32_16x16x32_bf16 v[84:87], a[172:175], a[212:215], v[84:87]\n"
"buffer_load_dwordx4 v230, s[44:47], s70 offen lds\n"
"v_mfma_f32_16x16x32_bf16 v[116:119], a[168:171], a[216:219], v[116:119]\n"
"ds_read_b64_tr_b16 a[130:131], v226 offset:256\n"
"s_add_u32 m0, s78, 93952\n"
"v_mfma_f32_16x16x32_bf16 v[116:119], a[172:175], a[220:223], v[116:119]\n"
"ds_read_b64_tr_b16 a[132:133], v226 offset:4096\n"
"v_mfma_f32_16x16x32_bf16 v[148:151], a[168:171], a[224:227], v[148:151]\n"
"buffer_load_dwordx4 v230, s[44:47], s70 offen offset:256 lds\n"
"v_mfma_f32_16x16x32_bf16 v[148:151], a[172:175], a[228:231], v[148:151]\n"
"ds_read_b64_tr_b16 a[134:135], v226 offset:4352\n"
"s_add_u32 m0, s67, 4992\n"
"v_mfma_f32_16x16x32_bf16 v[180:183], a[168:171], a[232:235], v[180:183]\n"
"ds_read_b64_tr_b16 a[136:137], v226 offset:8\n"
"v_mfma_f32_16x16x32_bf16 v[180:183], a[172:175], a[236:239], v[180:183]\n"
"buffer_load_dwordx4 v245, s[48:51], s64 offen offset:2176 nt lds\n"
"s_sub_u32 m0, m0, 896\n"
"s_nop 0\n"
"buffer_load_dwordx4 v245, s[48:51], s64 offen offset:2048 nt lds\n"
"v_mfma_f32_16x16x32_bf16 v[24:27], a[176:179], a[192:195], v[24:27]\n"
"ds_read_b64_tr_b16 a[138:139], v226 offset:264\n"
"s_add_u32 s64, s64, 12288\n"
"v_mfma_f32_16x16x32_bf16 v[24:27], a[180:183], a[196:199], v[24:27]\n"
"ds_read_b64_tr_b16 a[140:141], v226 offset:4104\n"
"s_and_b32 s64, s64, 0x3ffff\n"
"v_mfma_f32_16x16x32_bf16 v[56:59], a[176:179], a[200:203], v[56:59]\n"
"ds_read_b64_tr_b16 a[142:143], v226 offset:4360\n"
"s_add_u32 s67, s67, 65536\n"
"v_mfma_f32_16x16x32_bf16 v[56:59], a[180:183], a[204:207], v[56:59]\n"
"ds_read_b64_tr_b16 a[144:145], v226 offset:512\n"
"v_mfma_f32_16x16x32_bf16 v[88:91], a[176:179], a[208:211], v[88:91]\n"
"s_cmp_ge_u32 s67, s66\n"
"s_cselect_b32 s80, 163840, 0\n"
"v_mfma_f32_16x16x32_bf16 v[88:91], a[180:183], a[212:215], v[88:91]\n"
"ds_read_b64_tr_b16 a[146:147], v226 offset:768\n"
"s_sub_u32 s67, s67, s80\n"
"v_mfma_f32_16x16x32_bf16 v[120:123], a[176:179], a[216:219], v[120:123]\n"
"ds_read_b64_tr_b16 a[148:149], v226 offset:4608\n"
"v_mfma_f32_16x16x32_bf16 v[120:123], a[180:183], a[220:223], v[120:123]\n"
"v_mfma_f32_16x16x32_bf16 v[152:155], a[176:179], a[224:227], v[152:155]\n"
"ds_read_b64_tr_b16 a[150:151], v226 offset:4864\n"
"v_mfma_f32_16x16x32_bf16 v[152:155], a[180:183], a[228:231], v[152:155]\n"
"ds_read_b64_tr_b16 a[152:153], v226 offset:520\n"
"v_mfma_f32_16x16x32_bf16 v[184:187], a[176:179], a[232:235], v[184:187]\n"
"ds_read_b64_tr_b16 a[154:155], v226 offset:776\n"
"v_mfma_f32_16x16x32_bf16 v[184:187], a[180:183], a[236:239], v[184:187]\n"
"v_mfma_f32_16x16x32_bf16 v[28:31], a[184:187], a[192:195], v[28:31]\n"
"ds_read_b64_tr_b16 a[156:157], v226 offset:4616\n"
"v_mfma_f32_16x16x32_bf16 v[28:31], a[188:191], a[196:199], v[28:31]\n"
"ds_read_b64_tr_b16 a[158:159], v226 offset:4872\n"
"v_mfma_f32_16x16x32_bf16 v[60:63], a[184:187], a[200:203], v[60:63]\n"
"v_mfma_f32_16x16x32_bf16 v[60:63], a[188:191], a[204:207], v[60:63]\n"
"v_mfma_f32_16x16x32_bf16 v[92:95], a[184:187], a[208:211], v[92:95]\n"
"v_mfma_f32_16x16x32_bf16 v[92:95], a[188:191], a[212:215], v[92:95]\n"
"v_mfma_f32_16x16x32_bf16 v[124:127], a[184:187], a[216:219], v[124:127]\n"
"v_mfma_f32_16x16x32_bf16 v[124:127], a[188:191], a[220:223], v[124:127]\n"
"v_mfma_f32_16x16x32_bf16 v[156:159], a[184:187], a[224:227], v[156:159]\n"
"v_mfma_f32_16x16x32_bf16 v[156:159], a[188:191], a[228:231], v[156:159]\n"
"v_mfma_f32_16x16x32_bf16 v[188:191], a[184:187], a[232:235], v[188:191]\n"
"v_mfma_f32_16x16x32_bf16 v[188:191], a[188:191], a[236:239], v[188:191]\n"
"s_waitcnt vmcnt(9) lgkmcnt(0)\n"
"s_barrier\n"
"v_mfma_f32_16x16x32_bf16 v[0:3], a[128:131], a[0:3], v[0:3]\n"
"ds_read_b64_tr_b16 a[160:161], v226 offset:40960\n"
"v_mfma_f32_16x16x32_bf16 v[0:3], a[132:135], a[4:7], v[0:3]\n"
"s_add_u32 m0, s77, 0\n"
"s_add_u32 s70, s72, 768\n"
"v_mfma_f32_16x16x32_bf16 v[32:35], a[128:131], a[8:11], v[32:35]\n"
"buffer_load_dwordx4 v228, s[40:43], s70 offen lds\n"
"v_mfma_f32_16x16x32_bf16 v[32:35], a[132:135], a[12:15], v[32:35]\n"
"ds_read_b64_tr_b16 a[162:163], v226 offset:41216\n"
"v_mfma_f32_16x16x32_bf16 v[64:67], a[128:131], a[16:19], v[64:67]\n"
"s_add_u32 m0, s77, 1024\n"
"s_add_u32 s70, s72, 11008\n"
"v_mfma_f32_16x16x32_bf16 v[64:67], a[132:135], a[20:23], v[64:67]\n"
"ds_read_b64_tr_b16 a[164:165], v226 offset:45056\n"
"v_mfma_f32_16x16x32_bf16 v[96:99], a[128:131], a[24:27], v[96:99]\n"
"buffer_load_dwordx4 v229, s[40:43], s70 offen lds\n"
"v_mfma_f32_16x16x32_bf16 v[96:99], a[132:135], a[28:31], v[96:99]\n"
"ds_read_b64_tr_b16 a[166:167], v226 offset:45312\n"
"v_mfma_f32_16x16x32_bf16 v[128:131], a[128:131], a[32:35], v[128:131]\n"
"s_add_u32 m0, s77, 2048\n"
"s_add_u32 s70, s72, 21248\n"
"v_mfma_f32_16x16x32_bf16 v[128:131], a[132:135], a[36:39], v[128:131]\n"
"ds_read_b64_tr_b16 a[168:169], v226 offset:40968\n"
"v_mfma_f32_16x16x32_bf16 v[160:163], a[128:131], a[40:43], v[160:163]\n"
"buffer_load_dwordx4 v228, s[40:43], s70 offen lds\n"
"v_mfma_f32_16x16x32_bf16 v[160:163], a[132:135], a[44:47], v[160:163]\n"
"s_add_u32 m0, s78, 24448\n"
"v_mfma_f32_16x16x32_bf16 v[4:7], a[136:139], a[0:3], v[4:7]\n"
"ds_read_b64_tr_b16 a[170:171], v226 offset:41224\n"
"v_mfma_f32_16x16x32_bf16 v[4:7], a[140:143], a[4:7], v[4:7]\n"
"buffer_load_dwordx4 v230, s[44:47], s73 offen offset:128 lds\n"
"v_mfma_f32_16x16x32_bf16 v[36:39], a[136:139], a[8:11], v[36:39]\n"
"ds_read_b64_tr_b16 a[172:173], v226 offset:45064\n"
"v_mfma_f32_16x16x32_bf16 v[36:39], a[140:143], a[12:15], v[36:39]\n"
"s_add_u32 m0, s78, 32384\n"
"v_mfma_f32_16x16x32_bf16 v[68:71], a[136:139], a[16:19], v[68:71]\n"
"v_mfma_f32_16x16x32_bf16 v[68:71], a[140:143], a[20:23], v[68:71]\n"
"ds_read_b64_tr_b16 a[174:175], v226 offset:45320\n"
"v_mfma_f32_16x16x32_bf16 v[100:103], a[136:139], a[24:27], v[100:103]\n"
"buffer_load_dwordx4 v230, s[44:47], s73 offen offset:384 lds\n"
"v_mfma_f32_16x16x32_bf16 v[100:103], a[140:143], a[28:31], v[100:103]\n"
"ds_read_b64_tr_b16 a[176:177], v226 offset:41472\n"
"s_add_u32 s70, s73, 262144\n"
"v_mfma_f32_16x16x32_bf16 v[132:135], a[136:139], a[32:35], v[132:135]\n"
"v_mfma_f32_16x16x32_bf16 v[132:135], a[140:143], a[36:39], v[132:135]\n"
"ds_read_b64_tr_b16 a[178:179], v226 offset:41728\n"
"s_add_u32 m0, s78, 28544\n"
"v_mfma_f32_16x16x32_bf16 v[164:167], a[136:139], a[40:43], v[164:167]\n"
"v_mfma_f32_16x16x32_bf16 v[164:167], a[140:143], a[44:47], v[164:167]\n"
"buffer_load_dwordx4 v230, s[44:47], s70 offen offset:128 lds\n"
"v_mfma_f32_16x16x32_bf16 v[8:11], a[144:147], a[0:3], v[8:11]\n"
"ds_read_b64_tr_b16 a[180:181], v226 offset:45568\n"
"v_mfma_f32_16x16x32_bf16 v[8:11], a[148:151], a[4:7], v[8:11]\n"
"s_add_u32 m0, s78, 36480\n"
"v_mfma_f32_16x16x32_bf16 v[40:43], a[144:147], a[8:11], v[40:43]\n"
"ds_read_b64_tr_b16 a[182:183], v226 offset:45824\n"
"v_mfma_f32_16x16x32_bf16 v[40:43], a[148:151], a[12:15], v[40:43]\n"
"buffer_load_dwordx4 v230, s[44:47], s70 offen offset:384 lds\n"
"v_mfma_f32_16x16x32_bf16 v[72:75], a[144:147], a[16:19], v[72:75]\n"
"v_mfma_f32_16x16x32_bf16 v[72:75], a[148:151], a[20:23], v[72:75]\n"
"ds_read_b64_tr_b16 a[184:185], v226 offset:41480\n"
"s_add_u32 s73, s73, 524288\n"
"v_mfma_f32_16x16x32_bf16 v[104:107], a[144:147], a[24:27], v[104:107]\n"
"v_mfma_f32_16x16x32_bf16 v[104:107], a[148:151], a[28:31], v[104:107]\n"
"ds_read_b64_tr_b16 a[186:187], v226 offset:41736\n"
"v_mfma_f32_16x16x32_bf16 v[136:139], a[144:147], a[32:35], v[136:139]\n"
"v_mfma_f32_16x16x32_bf16 v[136:139], a[148:151], a[36:39], v[136:139]\n"
"v_mfma_f32_16x16x32_bf16 v[168:171], a[144:147], a[40:43], v[168:171]\n"
"ds_read_b64_tr_b16 a[188:189], v226 offset:45576\n"
"v_mfma_f32_16x16x32_bf16 v[168:171], a[148:151], a[44:47], v[168:171]\n"
"v_mfma_f32_16x16x32_bf16 v[12:15], a[152:155], a[0:3], v[12:15]\n"
"ds_read_b64_tr_b16 a[190:191], v226 offset:45832\n"
"v_mfma_f32_16x16x32_bf16 v[12:15], a[156:159], a[4:7], v[12:15]\n"
"v_mfma_f32_16x16x32_bf16 v[44:47], a[152:155], a[8:11], v[44:47]\n"
"v_mfma_f32_16x16x32_bf16 v[44:47], a[156:159], a[12:15], v[44:47]\n"
"v_mfma_f32_16x16x32_bf16 v[76:79], a[152:155], a[16:19], v[76:79]\n"
"v_mfma_f32_16x16x32_bf16 v[76:79], a[156:159], a[20:23], v[76:79]\n"
"v_mfma_f32_16x16x32_bf16 v[108:111], a[152:155], a[24:27], v[108:111]\n"
"v_mfma_f32_16x16x32_bf16 v[108:111], a[156:159], a[28:31], v[108:111]\n"
"v_mfma_f32_16x16x32_bf16 v[140:143], a[152:155], a[32:35], v[140:143]\n"
"v_mfma_f32_16x16x32_bf16 v[140:143], a[156:159], a[36:39], v[140:143]\n"
"v_mfma_f32_16x16x32_bf16 v[172:175], a[152:155], a[40:43], v[172:175]\n"
"v_mfma_f32_16x16x32_bf16 v[172:175], a[156:159], a[44:47], v[172:175]\n"
"s_waitcnt vmcnt(9) lgkmcnt(0)\n"
"s_barrier\n"
"v_mfma_f32_16x16x32_bf16 v[16:19], a[160:163], a[0:3], v[16:19]\n"
"ds_read_b128 a[192:195], v224 offset:40960\n"
"v_mfma_f32_16x16x32_bf16 v[16:19], a[164:167], a[4:7], v[16:19]\n"
"s_add_u32 m0, s77, 3072\n"
"s_add_u32 s70, s72, 31488\n"
"v_mfma_f32_16x16x32_bf16 v[48:51], a[160:163], a[8:11], v[48:51]\n"
"ds_read_b128 a[196:199], v225 offset:40960\n"
"ds_read_b128 a[200:203], v224 offset:43008\n"
"v_mfma_f32_16x16x32_bf16 v[48:51], a[164:167], a[12:15], v[48:51]\n"
"buffer_load_dwordx4 v229, s[40:43], s70 offen lds\n"
"v_mfma_f32_16x16x32_bf16 v[80:83], a[160:163], a[16:19], v[80:83]\n"
"s_add_u32 m0, s77, 4096\n"
"s_add_u32 s70, s72, 41728\n"
"v_mfma_f32_16x16x32_bf16 v[80:83], a[164:167], a[20:23], v[80:83]\n"
"ds_read_b128 a[204:207], v225 offset:43008\n"
"ds_read_b128 a[208:211], v224 offset:45056\n"
"v_mfma_f32_16x16x32_bf16 v[112:115], a[160:163], a[24:27], v[112:115]\n"
"buffer_load_dwordx4 v228, s[40:43], s70 offen lds\n"
"v_mfma_f32_16x16x32_bf16 v[112:115], a[164:167], a[28:31], v[112:115]\n"
"ds_read_b128 a[212:215], v225 offset:45056\n"
"v_mfma_f32_16x16x32_bf16 v[144:147], a[160:163], a[32:35], v[144:147]\n"
"s_add_u32 m0, s77, 5120\n"
"s_add_u32 s70, s72, 51968\n"
"v_mfma_f32_16x16x32_bf16 v[144:147], a[164:167], a[36:39], v[144:147]\n"
"ds_read_b128 a[216:219], v234 offset:40960\n"
"ds_read_b128 a[220:223], v235 offset:40960\n"
"v_mfma_f32_16x16x32_bf16 v[176:179], a[160:163], a[40:43], v[176:179]\n"
"buffer_load_dwordx4 v229, s[40:43], s70 offen lds\n"
"v_mfma_f32_16x16x32_bf16 v[176:179], a[164:167], a[44:47], v[176:179]\n"
"s_add_u32 m0, s78, 65536\n"
"ds_read_b128 a[224:227], v234 offset:43008\n"
"v_mfma_f32_16x16x32_bf16 v[20:23], a[168:171], a[0:3], v[20:23]\n"
"ds_read_b128 a[228:231], v235 offset:43008\n"
"v_mfma_f32_16x16x32_bf16 v[20:23], a[172:175], a[4:7], v[20:23]\n"
"buffer_load_dwordx4 v230, s[44:47], s73 offen lds\n"
"v_mfma_f32_16x16x32_bf16 v[52:55], a[168:171], a[8:11], v[52:55]\n"
"ds_read_b128 a[232:235], v234 offset:45056\n"
"s_add_u32 m0, s78, 73472\n"
"v_mfma_f32_16x16x32_bf16 v[52:55], a[172:175], a[12:15], v[52:55]\n"
"ds_read_b128 a[236:239], v235 offset:45056\n"
"v_mfma_f32_16x16x32_bf16 v[84:87], a[168:171], a[16:19], v[84:87]\n"
"ds_read_b64_tr_b16 a[128:129], v226 offset:57344\n"
"v_mfma_f32_16x16x32_bf16 v[84:87], a[172:175], a[20:23], v[84:87]\n"
"buffer_load_dwordx4 v230, s[44:47], s73 offen offset:256 lds\n"
"v_mfma_f32_16x16x32_bf16 v[116:119], a[168:171], a[24:27], v[116:119]\n"
"ds_read_b64_tr_b16 a[130:131], v226 offset:57600\n"
"s_add_u32 s70, s73, 262144\n"
"v_mfma_f32_16x16x32_bf16 v[116:119], a[172:175], a[28:31], v[116:119]\n"
"ds_read_b64_tr_b16 a[132:133], v226 offset:61440\n"
"v_mfma_f32_16x16x32_bf16 v[148:151], a[168:171], a[32:35], v[148:151]\n"
"ds_read_b64_tr_b16 a[134:135], v226 offset:61696\n"
"s_add_u32 m0, s78, 69632\n"
"v_mfma_f32_16x16x32_bf16 v[148:151], a[172:175], a[36:39], v[148:151]\n"
"buffer_load_dwordx4 v230, s[44:47], s70 offen lds\n"
"v_mfma_f32_16x16x32_bf16 v[180:183], a[168:171], a[40:43], v[180:183]\n"
"ds_read_b64_tr_b16 a[136:137], v226 offset:57352\n"
"v_mfma_f32_16x16x32_bf16 v[180:183], a[172:175], a[44:47], v[180:183]\n"
"ds_read_b64_tr_b16 a[138:139], v226 offset:57608\n"
"s_add_u32 m0, s78, 77568\n"
"v_mfma_f32_16x16x32_bf16 v[24:27], a[176:179], a[0:3], v[24:27]\n"
"v_mfma_f32_16x16x32_bf16 v[24:27], a[180:183], a[4:7], v[24:27]\n"
"ds_read_b64_tr_b16 a[140:141], v226 offset:61448\n"
"v_mfma_f32_16x16x32_bf16 v[56:59], a[176:179], a[8:11], v[56:59]\n"
"buffer_load_dwordx4 v230, s[44:47], s70 offen offset:256 lds\n"
"v_mfma_f32_16x16x32_bf16 v[56:59], a[180:183], a[12:15], v[56:59]\n"
"ds_read_b64_tr_b16 a[142:143], v226 offset:61704\n"
"ds_read_b64_tr_b16 a[144:145], v226 offset:57856\n"
"v_mfma_f32_16x16x32_bf16 v[88:91], a[176:179], a[16:19], v[88:91]\n"
"s_add_u32 m0, s91, 8192\n"
"v_mfma_f32_16x16x32_bf16 v[88:91], a[180:183], a[20:23], v[88:91]\n"
"ds_read_b64_tr_b16 a[146:147], v226 offset:58112\n"
"v_mfma_f32_16x16x32_bf16 v[120:123], a[176:179], a[24:27], v[120:123]\n"
"buffer_load_dwordx4 v246, s[52:55], s90 offen nt lds\n"
"s_add_u32 m0, m0, 896\n"
"s_nop 0\n"
"buffer_load_dwordx4 v246, s[52:55], s90 offen offset:128 nt lds\n"
"v_mfma_f32_16x16x32_bf16 v[120:123], a[180:183], a[28:31], v[120:123]\n"
"ds_read_b64_tr_b16 a[148:149], v226 offset:61952\n"
"v_mfma_f32_16x16x32_bf16 v[152:155], a[176:179], a[32:35], v[152:155]\n"
"ds_read_b64_tr_b16 a[150:151], v226 offset:62208\n"
"v_mfma_f32_16x16x32_bf16 v[152:155], a[180:183], a[36:39], v[152:155]\n"
"ds_read_b64_tr_b16 a[152:153], v226 offset:57864\n"
"v_mfma_f32_16x16x32_bf16 v[184:187], a[176:179], a[40:43], v[184:187]\n"
"ds_read_b64_tr_b16 a[154:155], v226 offset:58120\n"
"v_mfma_f32_16x16x32_bf16 v[184:187], a[180:183], a[44:47], v[184:187]\n"
"v_mfma_f32_16x16x32_bf16 v[28:31], a[184:187], a[0:3], v[28:31]\n"
"ds_read_b64_tr_b16 a[156:157], v226 offset:61960\n"
"v_mfma_f32_16x16x32_bf16 v[28:31], a[188:191], a[4:7], v[28:31]\n"
"ds_read_b64_tr_b16 a[158:159], v226 offset:62216\n"
"v_mfma_f32_16x16x32_bf16 v[60:63], a[184:187], a[8:11], v[60:63]\n"
"v_mfma_f32_16x16x32_bf16 v[60:63], a[188:191], a[12:15], v[60:63]\n"
"v_mfma_f32_16x16x32_bf16 v[92:95], a[184:187], a[16:19], v[92:95]\n"
"v_mfma_f32_16x16x32_bf16 v[92:95], a[188:191], a[20:23], v[92:95]\n"
"v_mfma_f32_16x16x32_bf16 v[124:127], a[184:187], a[24:27], v[124:127]\n"
"v_mfma_f32_16x16x32_bf16 v[124:127], a[188:191], a[28:31], v[124:127]\n"
"v_mfma_f32_16x16x32_bf16 v[156:159], a[184:187], a[32:35], v[156:159]\n"
"v_mfma_f32_16x16x32_bf16 v[156:159], a[188:191], a[36:39], v[156:159]\n"
"v_mfma_f32_16x16x32_bf16 v[188:191], a[184:187], a[40:43], v[188:191]\n"
"v_mfma_f32_16x16x32_bf16 v[188:191], a[188:191], a[44:47], v[188:191]\n"
"s_waitcnt vmcnt(9) lgkmcnt(0)\n"
"s_barrier\n"
"v_mfma_f32_16x16x32_bf16 v[0:3], a[128:131], a[192:195], v[0:3]\n"
"ds_read_b64_tr_b16 a[160:161], v226 offset:0\n"
"v_mfma_f32_16x16x32_bf16 v[0:3], a[132:135], a[196:199], v[0:3]\n"
"s_add_u32 m0, s77, 40960\n"
"s_add_u32 s70, s72, 896\n"
"v_mfma_f32_16x16x32_bf16 v[32:35], a[128:131], a[200:203], v[32:35]\n"
"buffer_load_dwordx4 v228, s[40:43], s70 offen lds\n"
"v_mfma_f32_16x16x32_bf16 v[32:35], a[132:135], a[204:207], v[32:35]\n"
"ds_read_b64_tr_b16 a[162:163], v226 offset:256\n"
"v_mfma_f32_16x16x32_bf16 v[64:67], a[128:131], a[208:211], v[64:67]\n"
"s_add_u32 m0, s77, 41984\n"
"s_add_u32 s70, s72, 11136\n"
"v_mfma_f32_16x16x32_bf16 v[64:67], a[132:135], a[212:215], v[64:67]\n"
"ds_read_b64_tr_b16 a[164:165], v226 offset:4096\n"
"v_mfma_f32_16x16x32_bf16 v[96:99], a[128:131], a[216:219], v[96:99]\n"
"buffer_load_dwordx4 v229, s[40:43], s70 offen lds\n"
"v_mfma_f32_16x16x32_bf16 v[96:99], a[132:135], a[220:223], v[96:99]\n"
"ds_read_b64_tr_b16 a[166:167], v226 offset:4352\n"
"v_mfma_f32_16x16x32_bf16 v[128:131], a[128:131], a[224:227], v[128:131]\n"
"s_add_u32 m0, s77, 43008\n"
"s_add_u32 s70, s72, 21376\n"
"v_mfma_f32_16x16x32_bf16 v[128:131], a[132:135], a[228:231], v[128:131]\n"
"ds_read_b64_tr_b16 a[168:169], v226 offset:8\n"
"v_mfma_f32_16x16x32_bf16 v[160:163], a[128:131], a[232:235], v[160:163]\n"
"buffer_load_dwordx4 v228, s[40:43], s70 offen lds\n"
"v_mfma_f32_16x16x32_bf16 v[160:163], a[132:135], a[236:239], v[160:163]\n"
"s_add_u32 m0, s78, 81792\n"
"v_mfma_f32_16x16x32_bf16 v[4:7], a[136:139], a[192:195], v[4:7]\n"
"ds_read_b64_tr_b16 a[170:171], v226 offset:264\n"
"v_mfma_f32_16x16x32_bf16 v[4:7], a[140:143], a[196:199], v[4:7]\n"
"buffer_load_dwordx4 v230, s[44:47], s73 offen offset:128 lds\n"
"v_mfma_f32_16x16x32_bf16 v[36:39], a[136:139], a[200:203], v[36:39]\n"
"ds_read_b64_tr_b16 a[172:173], v226 offset:4104\n"
"v_mfma_f32_16x16x32_bf16 v[36:39], a[140:143], a[204:207], v[36:39]\n"
"s_add_u32 m0, s78, 89728\n"
"v_mfma_f32_16x16x32_bf16 v[68:71], a[136:139], a[208:211], v[68:71]\n"
"v_mfma_f32_16x16x32_bf16 v[68:71], a[140:143], a[212:215], v[68:71]\n"
"ds_read_b64_tr_b16 a[174:175], v226 offset:4360\n"
"v_mfma_f32_16x16x32_bf16 v[100:103], a[136:139], a[216:219], v[100:103]\n"
"buffer_load_dwordx4 v230, s[44:47], s73 offen offset:384 lds\n"
"v_mfma_f32_16x16x32_bf16 v[100:103], a[140:143], a[220:223], v[100:103]\n"
"ds_read_b64_tr_b16 a[176:177], v226 offset:512\n"
"s_add_u32 s70, s73, 262144\n"
"v_mfma_f32_16x16x32_bf16 v[132:135], a[136:139], a[224:227], v[132:135]\n"
"v_mfma_f32_16x16x32_bf16 v[132:135], a[140:143], a[228:231], v[132:135]\n"
"ds_read_b64_tr_b16 a[178:179], v226 offset:768\n"
"s_add_u32 m0, s78, 85888\n"
"v_mfma_f32_16x16x32_bf16 v[164:167], a[136:139], a[232:235], v[164:167]\n"
"v_mfma_f32_16x16x32_bf16 v[164:167], a[140:143], a[236:239], v[164:167]\n"
"buffer_load_dwordx4 v230, s[44:47], s70 offen offset:128 lds\n"
"v_mfma_f32_16x16x32_bf16 v[8:11], a[144:147], a[192:195], v[8:11]\n"
"ds_read_b64_tr_b16 a[180:181], v226 offset:4608\n"
"v_mfma_f32_16x16x32_bf16 v[8:11], a[148:151], a[196:199], v[8:11]\n"
"s_add_u32 m0, s78, 93824\n"
"v_mfma_f32_16x16x32_bf16 v[40:43], a[144:147], a[200:203], v[40:43]\n"
"ds_read_b64_tr_b16 a[182:183], v226 offset:4864\n"
"v_mfma_f32_16x16x32_bf16 v[40:43], a[148:151], a[204:207], v[40:43]\n"
"buffer_load_dwordx4 v230, s[44:47], s70 offen offset:384 lds\n"
"v_mfma_f32_16x16x32_bf16 v[72:75], a[144:147], a[208:211], v[72:75]\n"
"v_mfma_f32_16x16x32_bf16 v[72:75], a[148:151], a[212:215], v[72:75]\n"
"ds_read_b64_tr_b16 a[184:185], v226 offset:520\n"
"s_add_u32 s73, s73, 524288\n"
"v_mfma_f32_16x16x32_bf16 v[104:107], a[144:147], a[216:219], v[104:107]\n"
"v_mfma_f32_16x16x32_bf16 v[104:107], a[148:151], a[220:223], v[104:107]\n"
"ds_read_b64_tr_b16 a[186:187], v226 offset:776\n"
"v_mfma_f32_16x16x32_bf16 v[136:139], a[144:147], a[224:227], v[136:139]\n"
"v_mfma_f32_16x16x32_bf16 v[136:139], a[148:151], a[228:231], v[136:139]\n"
"v_mfma_f32_16x16x32_bf16 v[168:171], a[144:147], a[232:235], v[168:171]\n"
"ds_read_b64_tr_b16 a[188:189], v226 offset:4616\n"
"v_mfma_f32_16x16x32_bf16 v[168:171], a[148:151], a[236:239], v[168:171]\n"
"v_mfma_f32_16x16x32_bf16 v[12:15], a[152:155], a[192:195], v[12:15]\n"
"ds_read_b64_tr_b16 a[190:191], v226 offset:4872\n"
"v_mfma_f32_16x16x32_bf16 v[12:15], a[156:159], a[196:199], v[12:15]\n"
"v_mfma_f32_16x16x32_bf16 v[44:47], a[152:155], a[200:203], v[44:47]\n"
"v_mfma_f32_16x16x32_bf16 v[44:47], a[156:159], a[204:207], v[44:47]\n"
"v_mfma_f32_16x16x32_bf16 v[76:79], a[152:155], a[208:211], v[76:79]\n"
"v_mfma_f32_16x16x32_bf16 v[76:79], a[156:159], a[212:215], v[76:79]\n"
"v_mfma_f32_16x16x32_bf16 v[108:111], a[152:155], a[216:219], v[108:111]\n"
"v_mfma_f32_16x16x32_bf16 v[108:111], a[156:159], a[220:223], v[108:111]\n"
"v_mfma_f32_16x16x32_bf16 v[140:143], a[152:155], a[224:227], v[140:143]\n"
"v_mfma_f32_16x16x32_bf16 v[140:143], a[156:159], a[228:231], v[140:143]\n"
"v_mfma_f32_16x16x32_bf16 v[172:175], a[152:155], a[232:235], v[172:175]\n"
"v_mfma_f32_16x16x32_bf16 v[172:175], a[156:159], a[236:239], v[172:175]\n"
"s_waitcnt vmcnt(9) lgkmcnt(0)\n"
"s_barrier\n"
"v_mfma_f32_16x16x32_bf16 v[16:19], a[160:163], a[192:195], v[16:19]\n"
"ds_read_b128 a[0:3], v224 offset:0\n"
"v_mfma_f32_16x16x32_bf16 v[16:19], a[164:167], a[196:199], v[16:19]\n"
"s_add_u32 m0, s77, 44032\n"
"s_add_u32 s70, s72, 31616\n"
"v_mfma_f32_16x16x32_bf16 v[48:51], a[160:163], a[200:203], v[48:51]\n"
"ds_read_b128 a[4:7], v225 offset:0\n"
"ds_read_b128 a[8:11], v224 offset:2048\n"
"v_mfma_f32_16x16x32_bf16 v[48:51], a[164:167], a[204:207], v[48:51]\n"
"buffer_load_dwordx4 v229, s[40:43], s70 offen lds\n"
"v_mfma_f32_16x16x32_bf16 v[80:83], a[160:163], a[208:211], v[80:83]\n"
"s_add_u32 m0, s77, 45056\n"
"s_add_u32 s70, s72, 41856\n"
"v_mfma_f32_16x16x32_bf16 v[80:83], a[164:167], a[212:215], v[80:83]\n"
"ds_read_b128 a[12:15], v225 offset:2048\n"
"ds_read_b128 a[16:19], v224 offset:4096\n"
"v_mfma_f32_16x16x32_bf16 v[112:115], a[160:163], a[216:219], v[112:115]\n"
"buffer_load_dwordx4 v228, s[40:43], s70 offen lds\n"
"v_mfma_f32_16x16x32_bf16 v[112:115], a[164:167], a[220:223], v[112:115]\n"
"ds_read_b128 a[20:23], v225 offset:4096\n"
"v_mfma_f32_16x16x32_bf16 v[144:147], a[160:163], a[224:227], v[144:147]\n"
"s_add_u32 m0, s77, 46080\n"
"s_add_u32 s70, s72, 52096\n"
"v_mfma_f32_16x16x32_bf16 v[144:147], a[164:167], a[228:231], v[144:147]\n"
"ds_read_b128 a[24:27], v234 offset:0\n"
"ds_read_b128 a[28:31], v235 offset:0\n"
"v_mfma_f32_16x16x32_bf16 v[176:179], a[160:163], a[232:235], v[176:179]\n"
"buffer_load_dwordx4 v229, s[40:43], s70 offen lds\n"
"v_mfma_f32_16x16x32_bf16 v[176:179], a[164:167], a[236:239], v[176:179]\n"
"s_add_u32 m0, s78, 24576\n"
"ds_read_b128 a[32:35], v234 offset:2048\n"
"v_mfma_f32_16x16x32_bf16 v[20:23], a[168:171], a[192:195], v[20:23]\n"
"ds_read_b128 a[36:39], v235 offset:2048\n"
"v_mfma_f32_16x16x32_bf16 v[20:23], a[172:175], a[196:199], v[20:23]\n"
"buffer_load_dwordx4 v230, s[44:47], s73 offen lds\n"
"v_mfma_f32_16x16x32_bf16 v[52:55], a[168:171], a[200:203], v[52:55]\n"
"ds_read_b128 a[40:43], v234 offset:4096\n"
"s_add_u32 m0, s78, 32512\n"
"v_mfma_f32_16x16x32_bf16 v[52:55], a[172:175], a[204:207], v[52:55]\n"
"ds_read_b128 a[44:47], v235 offset:4096\n"
"v_mfma_f32_16x16x32_bf16 v[84:87], a[168:171], a[208:211], v[84:87]\n"
"ds_read_b64_tr_b16 a[128:129], v226 offset:40960\n"
"v_mfma_f32_16x16x32_bf16 v[84:87], a[172:175], a[212:215], v[84:87]\n"
"buffer_load_dwordx4 v230, s[44:47], s73 offen offset:256 lds\n"
"v_mfma_f32_16x16x32_bf16 v[116:119], a[168:171], a[216:219], v[116:119]\n"
"ds_read_b64_tr_b16 a[130:131], v226 offset:41216\n"
"s_add_u32 s70, s73, 262144\n"
"v_mfma_f32_16x16x32_bf16 v[116:119], a[172:175], a[220:223], v[116:119]\n"
"ds_read_b64_tr_b16 a[132:133], v226 offset:45056\n"
"v_mfma_f32_16x16x32_bf16 v[148:151], a[168:171], a[224:227], v[148:151]\n"
"ds_read_b64_tr_b16 a[134:135], v226 offset:45312\n"
"s_add_u32 m0, s78, 28672\n"
"v_mfma_f32_16x16x32_bf16 v[148:151], a[172:175], a[228:231], v[148:151]\n"
"buffer_load_dwordx4 v230, s[44:47], s70 offen lds\n"
"v_mfma_f32_16x16x32_bf16 v[180:183], a[168:171], a[232:235], v[180:183]\n"
"ds_read_b64_tr_b16 a[136:137], v226 offset:40968\n"
"v_mfma_f32_16x16x32_bf16 v[180:183], a[172:175], a[236:239], v[180:183]\n"
"ds_read_b64_tr_b16 a[138:139], v226 offset:41224\n"
"s_add_u32 m0, s78, 36608\n"
"v_mfma_f32_16x16x32_bf16 v[24:27], a[176:179], a[192:195], v[24:27]\n"
"v_mfma_f32_16x16x32_bf16 v[24:27], a[180:183], a[196:199], v[24:27]\n"
"ds_read_b64_tr_b16 a[140:141], v226 offset:45064\n"
"v_mfma_f32_16x16x32_bf16 v[56:59], a[176:179], a[200:203], v[56:59]\n"
"buffer_load_dwordx4 v230, s[44:47], s70 offen offset:256 lds\n"
"v_mfma_f32_16x16x32_bf16 v[56:59], a[180:183], a[204:207], v[56:59]\n"
"ds_read_b64_tr_b16 a[142:143], v226 offset:45320\n"
"ds_read_b64_tr_b16 a[144:145], v226 offset:41472\n"
"v_mfma_f32_16x16x32_bf16 v[88:91], a[176:179], a[208:211], v[88:91]\n"
"s_add_u32 m0, s91, 9088\n"
"v_mfma_f32_16x16x32_bf16 v[88:91], a[180:183], a[212:215], v[88:91]\n"
"ds_read_b64_tr_b16 a[146:147], v226 offset:41728\n"
"v_mfma_f32_16x16x32_bf16 v[120:123], a[176:179], a[216:219], v[120:123]\n"
"buffer_load_dwordx4 v246, s[52:55], s90 offen offset:2176 nt lds\n"
"s_sub_u32 m0, m0, 896\n"
"s_nop 0\n"
"buffer_load_dwordx4 v246, s[52:55], s90 offen offset:2048 nt lds\n"
"v_mfma_f32_16x16x32_bf16 v[120:123], a[180:183], a[220:223], v[120:123]\n"
"ds_read_b64_tr_b16 a[148:149], v226 offset:45568\n"
"v_mfma_f32_16x16x32_bf16 v[152:155], a[176:179], a[224:227], v[152:155]\n"
"ds_read_b64_tr_b16 a[150:151], v226 offset:45824\n"
"v_mfma_f32_16x16x32_bf16 v[152:155], a[180:183], a[228:231], v[152:155]\n"
"ds_read_b64_tr_b16 a[152:153], v226 offset:41480\n"
"v_mfma_f32_16x16x32_bf16 v[184:187], a[176:179], a[232:235], v[184:187]\n"
"ds_read_b64_tr_b16 a[154:155], v226 offset:41736\n"
"v_mfma_f32_16x16x32_bf16 v[184:187], a[180:183], a[236:239], v[184:187]\n"
"v_mfma_f32_16x16x32_bf16 v[28:31], a[184:187], a[192:195], v[28:31]\n"
"ds_read_b64_tr_b16 a[156:157], v226 offset:45576\n"
"v_mfma_f32_16x16x32_bf16 v[28:31], a[188:191], a[196:199], v[28:31]\n"
"ds_read_b64_tr_b16 a[158:159], v226 offset:45832\n"
"v_mfma_f32_16x16x32_bf16 v[60:63], a[184:187], a[200:203], v[60:63]\n"
"v_mfma_f32_16x16x32_bf16 v[60:63], a[188:191], a[204:207], v[60:63]\n"
"v_mfma_f32_16x16x32_bf16 v[92:95], a[184:187], a[208:211], v[92:95]\n"
"v_mfma_f32_16x16x32_bf16 v[92:95], a[188:191], a[212:215], v[92:95]\n"
"v_mfma_f32_16x16x32_bf16 v[124:127], a[184:187], a[216:219], v[124:127]\n"
"v_mfma_f32_16x16x32_bf16 v[124:127], a[188:191], a[220:223], v[124:127]\n"
"v_mfma_f32_16x16x32_bf16 v[156:159], a[184:187], a[224:227], v[156:159]\n"
"v_mfma_f32_16x16x32_bf16 v[156:159], a[188:191], a[228:231], v[156:159]\n"
"v_mfma_f32_16x16x32_bf16 v[188:191], a[184:187], a[232:235], v[188:191]\n"
"v_mfma_f32_16x16x32_bf16 v[188:191], a[188:191], a[236:239], v[188:191]\n"
"s_waitcnt vmcnt(9) lgkmcnt(0)\n"
"s_barrier\n"
"v_mfma_f32_16x16x32_bf16 v[0:3], a[128:131], a[0:3], v[0:3]\n"
"ds_read_b64_tr_b16 a[160:161], v226 offset:57344\n"
"v_mfma_f32_16x16x32_bf16 v[0:3], a[132:135], a[4:7], v[0:3]\n"
"s_add_u32 m0, s77, 0\n"
"s_add_u32 s70, s72, 1024\n"
"v_mfma_f32_16x16x32_bf16 v[32:35], a[128:131], a[8:11], v[32:35]\n"
"buffer_load_dwordx4 v228, s[40:43], s70 offen lds\n"
"v_mfma_f32_16x16x32_bf16 v[32:35], a[132:135], a[12:15], v[32:35]\n"
"ds_read_b64_tr_b16 a[162:163], v226 offset:57600\n"
"v_mfma_f32_16x16x32_bf16 v[64:67], a[128:131], a[16:19], v[64:67]\n"
"s_add_u32 m0, s77, 1024\n"
"s_add_u32 s70, s72, 11264\n"
"v_mfma_f32_16x16x32_bf16 v[64:67], a[132:135], a[20:23], v[64:67]\n"
"ds_read_b64_tr_b16 a[164:165], v226 offset:61440\n"
"v_mfma_f32_16x16x32_bf16 v[96:99], a[128:131], a[24:27], v[96:99]\n"
"buffer_load_dwordx4 v229, s[40:43], s70 offen lds\n"
"v_mfma_f32_16x16x32_bf16 v[96:99], a[132:135], a[28:31], v[96:99]\n"
"ds_read_b64_tr_b16 a[166:167], v226 offset:61696\n"
"v_mfma_f32_16x16x32_bf16 v[128:131], a[128:131], a[32:35], v[128:131]\n"
"s_add_u32 m0, s77, 2048\n"
"s_add_u32 s70, s72, 21504\n"
"v_mfma_f32_16x16x32_bf16 v[128:131], a[132:135], a[36:39], v[128:131]\n"
"ds_read_b64_tr_b16 a[168:169], v226 offset:57352\n"
"v_mfma_f32_16x16x32_bf16 v[160:163], a[128:131], a[40:43], v[160:163]\n"
"buffer_load_dwordx4 v228, s[40:43], s70 offen lds\n"
"v_mfma_f32_16x16x32_bf16 v[160:163], a[132:135], a[44:47], v[160:163]\n"
"s_add_u32 m0, s78, 65408\n"
"v_mfma_f32_16x16x32_bf16 v[4:7], a[136:139], a[0:3], v[4:7]\n"
"ds_read_b64_tr_b16 a[170:171], v226 offset:57608\n"
"v_mfma_f32_16x16x32_bf16 v[4:7], a[140:143], a[4:7], v[4:7]\n"
"buffer_load_dwordx4 v230, s[44:47], s73 offen offset:128 lds\n"
"v_mfma_f32_16x16x32_bf16 v[36:39], a[136:139], a[8:11], v[36:39]\n"
"ds_read_b64_tr_b16 a[172:173], v226 offset:61448\n"
"v_mfma_f32_16x16x32_bf16 v[36:39], a[140:143], a[12:15], v[36:39]\n"
"s_add_u32 m0, s78, 73344\n"
"v_mfma_f32_16x16x32_bf16 v[68:71], a[136:139], a[16:19], v[68:71]\n"
"v_mfma_f32_16x16x32_bf16 v[68:71], a[140:143], a[20:23], v[68:71]\n"
"ds_read_b64_tr_b16 a[174:175], v226 offset:61704\n"
"v_mfma_f32_16x16x32_bf16 v[100:103], a[136:139], a[24:27], v[100:103]\n"
"buffer_load_dwordx4 v230, s[44:47], s73 offen offset:384 lds\n"
"v_mfma_f32_16x16x32_bf16 v[100:103], a[140:143], a[28:31], v[100:103]\n"
"ds_read_b64_tr_b16 a[176:177], v226 offset:57856\n"
"s_add_u32 s70, s73, 262144\n"
"v_mfma_f32_16x16x32_bf16 v[132:135], a[136:139], a[32:35], v[132:135]\n"
"v_mfma_f32_16x16x32_bf16 v[132:135], a[140:143], a[36:39], v[132:135]\n"
"ds_read_b64_tr_b16 a[178:179], v226 offset:58112\n"
"s_add_u32 m0, s78, 69504\n"
"v_mfma_f32_16x16x32_bf16 v[164:167], a[136:139], a[40:43], v[164:167]\n"
"v_mfma_f32_16x16x32_bf16 v[164:167], a[140:143], a[44:47], v[164:167]\n"
"buffer_load_dwordx4 v230, s[44:47], s70 offen offset:128 lds\n"
"v_mfma_f32_16x16x32_bf16 v[8:11], a[144:147], a[0:3], v[8:11]\n"
"ds_read_b64_tr_b16 a[180:181], v226 offset:61952\n"
"v_mfma_f32_16x16x32_bf16 v[8:11], a[148:151], a[4:7], v[8:11]\n"
"s_add_u32 m0, s78, 77440\n"
"v_mfma_f32_16x16x32_bf16 v[40:43], a[144:147], a[8:11], v[40:43]\n"
"ds_read_b64_tr_b16 a[182:183], v226 offset:62208\n"
"v_mfma_f32_16x16x32_bf16 v[40:43], a[148:151], a[12:15], v[40:43]\n"
"buffer_load_dwordx4 v230, s[44:47], s70 offen offset:384 lds\n"
"v_mfma_f32_16x16x32_bf16 v[72:75], a[144:147], a[16:19], v[72:75]\n"
"v_mfma_f32_16x16x32_bf16 v[72:75], a[148:151], a[20:23], v[72:75]\n"
"ds_read_b64_tr_b16 a[184:185], v226 offset:57864\n"
"s_add_u32 s73, s73, 524288\n"
"v_mfma_f32_16x16x32_bf16 v[104:107], a[144:147], a[24:27], v[104:107]\n"
"v_mfma_f32_16x16x32_bf16 v[104:107], a[148:151], a[28:31], v[104:107]\n"
"ds_read_b64_tr_b16 a[186:187], v226 offset:58120\n"
"v_mfma_f32_16x16x32_bf16 v[136:139], a[144:147], a[32:35], v[136:139]\n"
"v_mfma_f32_16x16x32_bf16 v[136:139], a[148:151], a[36:39], v[136:139]\n"
"v_mfma_f32_16x16x32_bf16 v[168:171], a[144:147], a[40:43], v[168:171]\n"
"ds_read_b64_tr_b16 a[188:189], v226 offset:61960\n"
"v_mfma_f32_16x16x32_bf16 v[168:171], a[148:151], a[44:47], v[168:171]\n"
"v_mfma_f32_16x16x32_bf16 v[12:15], a[152:155], a[0:3], v[12:15]\n"
"ds_read_b64_tr_b16 a[190:191], v226 offset:62216\n"
"v_mfma_f32_16x16x32_bf16 v[12:15], a[156:159], a[4:7], v[12:15]\n"
"v_mfma_f32_16x16x32_bf16 v[44:47], a[152:155], a[8:11], v[44:47]\n"
"v_mfma_f32_16x16x32_bf16 v[44:47], a[156:159], a[12:15], v[44:47]\n"
"v_mfma_f32_16x16x32_bf16 v[76:79], a[152:155], a[16:19], v[76:79]\n"
"v_mfma_f32_16x16x32_bf16 v[76:79], a[156:159], a[20:23], v[76:79]\n"
"v_mfma_f32_16x16x32_bf16 v[108:111], a[152:155], a[24:27], v[108:111]\n"
"v_mfma_f32_16x16x32_bf16 v[108:111], a[156:159], a[28:31], v[108:111]\n"
"v_mfma_f32_16x16x32_bf16 v[140:143], a[152:155], a[32:35], v[140:143]\n"
"v_mfma_f32_16x16x32_bf16 v[140:143], a[156:159], a[36:39], v[140:143]\n"
"v_mfma_f32_16x16x32_bf16 v[172:175], a[152:155], a[40:43], v[172:175]\n"
"v_mfma_f32_16x16x32_bf16 v[172:175], a[156:159], a[44:47], v[172:175]\n"
"s_waitcnt vmcnt(9) lgkmcnt(0)\n"
"s_barrier\n"
"v_mfma_f32_16x16x32_bf16 v[16:19], a[160:163], a[0:3], v[16:19]\n"
"ds_read_b128 a[192:195], v224 offset:40960\n"
"v_mfma_f32_16x16x32_bf16 v[16:19], a[164:167], a[4:7], v[16:19]\n"
"s_add_u32 m0, s77, 3072\n"
"s_add_u32 s70, s72, 31744\n"
"v_mfma_f32_16x16x32_bf16 v[48:51], a[160:163], a[8:11], v[48:51]\n"
"ds_read_b128 a[196:199], v225 offset:40960\n"
"ds_read_b128 a[200:203], v224 offset:43008\n"
"v_mfma_f32_16x16x32_bf16 v[48:51], a[164:167], a[12:15], v[48:51]\n"
"buffer_load_dwordx4 v229, s[40:43], s70 offen lds\n"
"v_mfma_f32_16x16x32_bf16 v[80:83], a[160:163], a[16:19], v[80:83]\n"
"s_add_u32 m0, s77, 4096\n"
"s_add_u32 s70, s72, 41984\n"
"v_mfma_f32_16x16x32_bf16 v[80:83], a[164:167], a[20:23], v[80:83]\n"
"ds_read_b128 a[204:207], v225 offset:43008\n"
"ds_read_b128 a[208:211], v224 offset:45056\n"
"v_mfma_f32_16x16x32_bf16 v[112:115], a[160:163], a[24:27], v[112:115]\n"
"buffer_load_dwordx4 v228, s[40:43], s70 offen lds\n"
"v_mfma_f32_16x16x32_bf16 v[112:115], a[164:167], a[28:31], v[112:115]\n"
"ds_read_b128 a[212:215], v225 offset:45056\n"
"v_mfma_f32_16x16x32_bf16 v[144:147], a[160:163], a[32:35], v[144:147]\n"
"s_add_u32 m0, s77, 5120\n"
"s_add_u32 s70, s72, 52224\n"
"v_mfma_f32_16x16x32_bf16 v[144:147], a[164:167], a[36:39], v[144:147]\n"
"ds_read_b128 a[216:219], v234 offset:40960\n"
"ds_read_b128 a[220:223], v235 offset:40960\n"
"v_mfma_f32_16x16x32_bf16 v[176:179], a[160:163], a[40:43], v[176:179]\n"
"buffer_load_dwordx4 v229, s[40:43], s70 offen lds\n"
"v_mfma_f32_16x16x32_bf16 v[176:179], a[164:167], a[44:47], v[176:179]\n"
"s_add_u32 m0, s78, 81920\n"
"ds_read_b128 a[224:227], v234 offset:43008\n"
"v_mfma_f32_16x16x32_bf16 v[20:23], a[168:171], a[0:3], v[20:23]\n"
"ds_read_b128 a[228:231], v235 offset:43008\n"
"v_mfma_f32_16x16x32_bf16 v[20:23], a[172:175], a[4:7], v[20:23]\n"
"buffer_load_dwordx4 v230, s[44:47], s73 offen lds\n"
"v_mfma_f32_16x16x32_bf16 v[52:55], a[168:171], a[8:11], v[52:55]\n"
"ds_read_b128 a[232:235], v234 offset:45056\n"
"s_add_u32 m0, s78, 89856\n"
"v_mfma_f32_16x16x32_bf16 v[52:55], a[172:175], a[12:15], v[52:55]\n"
"ds_read_b128 a[236:239], v235 offset:45056\n"
"v_mfma_f32_16x16x32_bf16 v[84:87], a[168:171], a[16:19], v[84:87]\n"
"buffer_load_dwordx4 v230, s[44:47], s73 offen offset:256 lds\n"
"v_mfma_f32_16x16x32_bf16 v[84:87], a[172:175], a[20:23], v[84:87]\n"
"ds_read_b64_tr_b16 a[128:129], v226 offset:0\n"
"s_add_u32 s70, s73, 262144\n"
"v_mfma_f32_16x16x32_bf16 v[116:119], a[168:171], a[24:27], v[116:119]\n"
"ds_read_b64_tr_b16 a[130:131], v226 offset:256\n"
"s_add_u32 m0, s78, 86016\n"
"v_mfma_f32_16x16x32_bf16 v[116:119], a[172:175], a[28:31], v[116:119]\n"
"ds_read_b64_tr_b16 a[132:133], v226 offset:4096\n"
"v_mfma_f32_16x16x32_bf16 v[148:151], a[168:171], a[32:35], v[148:151]\n"
"ds_read_b64_tr_b16 a[134:135], v226 offset:4352\n"
"v_mfma_f32_16x16x32_bf16 v[148:151], a[172:175], a[36:39], v[148:151]\n"
"buffer_load_dwordx4 v230, s[44:47], s70 offen lds\n"
"v_mfma_f32_16x16x32_bf16 v[180:183], a[168:171], a[40:43], v[180:183]\n"
"ds_read_b64_tr_b16 a[136:137], v226 offset:8\n"
"s_add_u32 m0, s78, 93952\n"
"v_mfma_f32_16x16x32_bf16 v[180:183], a[172:175], a[44:47], v[180:183]\n"
"ds_read_b64_tr_b16 a[138:139], v226 offset:264\n"
"v_mfma_f32_16x16x32_bf16 v[24:27], a[176:179], a[0:3], v[24:27]\n"
"buffer_load_dwordx4 v230, s[44:47], s70 offen offset:256 lds\n"
"v_mfma_f32_16x16x32_bf16 v[24:27], a[180:183], a[4:7], v[24:27]\n"
"ds_read_b64_tr_b16 a[140:141], v226 offset:4104\n"
"s_add_u32 m0, s91, 12288\n"
"v_mfma_f32_16x16x32_bf16 v[56:59], a[176:179], a[8:11], v[56:59]\n"
"ds_read_b64_tr_b16 a[142:143], v226 offset:4360\n"
"v_mfma_f32_16x16x32_bf16 v[56:59], a[180:183], a[12:15], v[56:59]\n"
"ds_read_b64_tr_b16 a[144:145], v226 offset:512\n"
"s_add_u32 s90, s90, 4096\n"
"v_mfma_f32_16x16x32_bf16 v[88:91], a[176:179], a[16:19], v[88:91]\n"
"v_mfma_f32_16x16x32_bf16 v[88:91], a[180:183], a[20:23], v[88:91]\n"
"ds_read_b64_tr_b16 a[146:147], v226 offset:768\n"
"v_mfma_f32_16x16x32_bf16 v[120:123], a[176:179], a[24:27], v[120:123]\n"
"buffer_load_dwordx4 v246, s[52:55], s90 offen nt lds\n"
"s_add_u32 m0, m0, 896\n"
"s_nop 0\n"
"buffer_load_dwordx4 v246, s[52:55], s90 offen offset:128 nt lds\n"
"v_mfma_f32_16x16x32_bf16 v[120:123], a[180:183], a[28:31], v[120:123]\n"
"ds_read_b64_tr_b16 a[148:149], v226 offset:4608\n"
"v_mfma_f32_16x16x32_bf16 v[152:155], a[176:179], a[32:35], v[152:155]\n"
"ds_read_b64_tr_b16 a[150:151], v226 offset:4864\n"
"v_mfma_f32_16x16x32_bf16 v[152:155], a[180:183], a[36:39], v[152:155]\n"
"ds_read_b64_tr_b16 a[152:153], v226 offset:520\n"
"v_mfma_f32_16x16x32_bf16 v[184:187], a[176:179], a[40:43], v[184:187]\n"
"ds_read_b64_tr_b16 a[154:155], v226 offset:776\n"
"v_mfma_f32_16x16x32_bf16 v[184:187], a[180:183], a[44:47], v[184:187]\n"
"v_mfma_f32_16x16x32_bf16 v[28:31], a[184:187], a[0:3], v[28:31]\n"
"ds_read_b64_tr_b16 a[156:157], v226 offset:4616\n"
"v_mfma_f32_16x16x32_bf16 v[28:31], a[188:191], a[4:7], v[28:31]\n"
"ds_read_b64_tr_b16 a[158:159], v226 offset:4872\n"
"v_mfma_f32_16x16x32_bf16 v[60:63], a[184:187], a[8:11], v[60:63]\n"
"v_mfma_f32_16x16x32_bf16 v[60:63], a[188:191], a[12:15], v[60:63]\n"
"v_mfma_f32_16x16x32_bf16 v[92:95], a[184:187], a[16:19], v[92:95]\n"
"v_mfma_f32_16x16x32_bf16 v[92:95], a[188:191], a[20:23], v[92:95]\n"
"v_mfma_f32_16x16x32_bf16 v[124:127], a[184:187], a[24:27], v[124:127]\n"
"v_mfma_f32_16x16x32_bf16 v[124:127], a[188:191], a[28:31], v[124:127]\n"
"v_mfma_f32_16x16x32_bf16 v[156:159], a[184:187], a[32:35], v[156:159]\n"
"v_mfma_f32_16x16x32_bf16 v[156:159], a[188:191], a[36:39], v[156:159]\n"
"v_mfma_f32_16x16x32_bf16 v[188:191], a[184:187], a[40:43], v[188:191]\n"
"v_mfma_f32_16x16x32_bf16 v[188:191], a[188:191], a[44:47], v[188:191]\n"
"s_waitcnt vmcnt(9) lgkmcnt(0)\n"
"s_barrier\n"
"v_mfma_f32_16x16x32_bf16 v[0:3], a[128:131], a[192:195], v[0:3]\n"
"ds_read_b64_tr_b16 a[160:161], v226 offset:40960\n"
"v_mfma_f32_16x16x32_bf16 v[0:3], a[132:135], a[196:199], v[0:3]\n"
"s_add_u32 m0, s77, 40960\n"
"s_add_u32 s70, s72, 1152\n"
"v_mfma_f32_16x16x32_bf16 v[32:35], a[128:131], a[200:203], v[32:35]\n"
"buffer_load_dwordx4 v228, s[40:43], s70 offen lds\n"
"v_mfma_f32_16x16x32_bf16 v[32:35], a[132:135], a[204:207], v[32:35]\n"
"ds_read_b64_tr_b16 a[162:163], v226 offset:41216\n"
"v_mfma_f32_16x16x32_bf16 v[64:67], a[128:131], a[208:211], v[64:67]\n"
"s_add_u32 m0, s77, 41984\n"
"s_add_u32 s70, s72, 11392\n"
"v_mfma_f32_16x16x32_bf16 v[64:67], a[132:135], a[212:215], v[64:67]\n"
"ds_read_b64_tr_b16 a[164:165], v226 offset:45056\n"
"v_mfma_f32_16x16x32_bf16 v[96:99], a[128:131], a[216:219], v[96:99]\n"
"buffer_load_dwordx4 v229, s[40:43], s70 offen lds\n"
"v_mfma_f32_16x16x32_bf16 v[96:99], a[132:135], a[220:223], v[96:99]\n"
"ds_read_b64_tr_b16 a[166:167], v226 offset:45312\n"
"v_mfma_f32_16x16x32_bf16 v[128:131], a[128:131], a[224:227], v[128:131]\n"
"s_add_u32 m0, s77, 43008\n"
"s_add_u32 s70, s72, 21632\n"
"v_mfma_f32_16x16x32_bf16 v[128:131], a[132:135], a[228:231], v[128:131]\n"
"ds_read_b64_tr_b16 a[168:169], v226 offset:40968\n"
"v_mfma_f32_16x16x32_bf16 v[160:163], a[128:131], a[232:235], v[160:163]\n"
"buffer_load_dwordx4 v228, s[40:43], s70 offen lds\n"
"v_mfma_f32_16x16x32_bf16 v[160:163], a[132:135], a[236:239], v[160:163]\n"
"s_add_u32 m0, s78, 24448\n"
"v_mfma_f32_16x16x32_bf16 v[4:7], a[136:139], a[192:195], v[4:7]\n"
"ds_read_b64_tr_b16 a[170:171], v226 offset:41224\n"
"v_mfma_f32_16x16x32_bf16 v[4:7], a[140:143], a[196:199], v[4:7]\n"
"buffer_load_dwordx4 v230, s[44:47], s73 offen offset:128 lds\n"
"v_mfma_f32_16x16x32_bf16 v[36:39], a[136:139], a[200:203], v[36:39]\n"
"ds_read_b64_tr_b16 a[172:173], v226 offset:45064\n"
"v_mfma_f32_16x16x32_bf16 v[36:39], a[140:143], a[204:207], v[36:39]\n"
"s_add_u32 m0, s78, 32384\n"
"v_mfma_f32_16x16x32_bf16 v[68:71], a[136:139], a[208:211], v[68:71]\n"
"v_mfma_f32_16x16x32_bf16 v[68:71], a[140:143], a[212:215], v[68:71]\n"
"ds_read_b64_tr_b16 a[174:175], v226 offset:45320\n"
"v_mfma_f32_16x16x32_bf16 v[100:103], a[136:139], a[216:219], v[100:103]\n"
"buffer_load_dwordx4 v230, s[44:47], s73 offen offset:384 lds\n"
"v_mfma_f32_16x16x32_bf16 v[100:103], a[140:143], a[220:223], v[100:103]\n"
"ds_read_b64_tr_b16 a[176:177], v226 offset:41472\n"
"s_add_u32 s70, s73, 262144\n"
"v_mfma_f32_16x16x32_bf16 v[132:135], a[136:139], a[224:227], v[132:135]\n"
"v_mfma_f32_16x16x32_bf16 v[132:135], a[140:143], a[228:231], v[132:135]\n"
"ds_read_b64_tr_b16 a[178:179], v226 offset:41728\n"
"s_add_u32 m0, s78, 28544\n"
"v_mfma_f32_16x16x32_bf16 v[164:167], a[136:139], a[232:235], v[164:167]\n"
"v_mfma_f32_16x16x32_bf16 v[164:167], a[140:143], a[236:239], v[164:167]\n"
"buffer_load_dwordx4 v230, s[44:47], s70 offen offset:128 lds\n"
"v_mfma_f32_16x16x32_bf16 v[8:11], a[144:147], a[192:195], v[8:11]\n"
"ds_read_b64_tr_b16 a[180:181], v226 offset:45568\n"
"v_mfma_f32_16x16x32_bf16 v[8:11], a[148:151], a[196:199], v[8:11]\n"
"s_add_u32 m0, s78, 36480\n"
"v_mfma_f32_16x16x32_bf16 v[40:43], a[144:147], a[200:203], v[40:43]\n"
"ds_read_b64_tr_b16 a[182:183], v226 offset:45824\n"
"v_mfma_f32_16x16x32_bf16 v[40:43], a[148:151], a[204:207], v[40:43]\n"
"buffer_load_dwordx4 v230, s[44:47], s70 offen offset:384 lds\n"
"v_mfma_f32_16x16x32_bf16 v[72:75], a[144:147], a[208:211], v[72:75]\n"
"v_mfma_f32_16x16x32_bf16 v[72:75], a[148:151], a[212:215], v[72:75]\n"
"ds_read_b64_tr_b16 a[184:185], v226 offset:41480\n"
"s_add_u32 s73, s73, 524288\n"
"v_mfma_f32_16x16x32_bf16 v[104:107], a[144:147], a[216:219], v[104:107]\n"
"v_mfma_f32_16x16x32_bf16 v[104:107], a[148:151], a[220:223], v[104:107]\n"
"ds_read_b64_tr_b16 a[186:187], v226 offset:41736\n"
"v_mfma_f32_16x16x32_bf16 v[136:139], a[144:147], a[224:227], v[136:139]\n"
"v_mfma_f32_16x16x32_bf16 v[136:139], a[148:151], a[228:231], v[136:139]\n"
"v_mfma_f32_16x16x32_bf16 v[168:171], a[144:147], a[232:235], v[168:171]\n"
"ds_read_b64_tr_b16 a[188:189], v226 offset:45576\n"
"v_mfma_f32_16x16x32_bf16 v[168:171], a[148:151], a[236:239], v[168:171]\n"
"v_mfma_f32_16x16x32_bf16 v[12:15], a[152:155], a[192:195], v[12:15]\n"
"ds_read_b64_tr_b16 a[190:191], v226 offset:45832\n"
"v_mfma_f32_16x16x32_bf16 v[12:15], a[156:159], a[196:199], v[12:15]\n"
"v_mfma_f32_16x16x32_bf16 v[44:47], a[152:155], a[200:203], v[44:47]\n"
"v_mfma_f32_16x16x32_bf16 v[44:47], a[156:159], a[204:207], v[44:47]\n"
"v_mfma_f32_16x16x32_bf16 v[76:79], a[152:155], a[208:211], v[76:79]\n"
"v_mfma_f32_16x16x32_bf16 v[76:79], a[156:159], a[212:215], v[76:79]\n"
"v_mfma_f32_16x16x32_bf16 v[108:111], a[152:155], a[216:219], v[108:111]\n"
"v_mfma_f32_16x16x32_bf16 v[108:111], a[156:159], a[220:223], v[108:111]\n"
"v_mfma_f32_16x16x32_bf16 v[140:143], a[152:155], a[224:227], v[140:143]\n"
"v_mfma_f32_16x16x32_bf16 v[140:143], a[156:159], a[228:231], v[140:143]\n"
"v_mfma_f32_16x16x32_bf16 v[172:175], a[152:155], a[232:235], v[172:175]\n"
"v_mfma_f32_16x16x32_bf16 v[172:175], a[156:159], a[236:239], v[172:175]\n"
"s_waitcnt vmcnt(9) lgkmcnt(0)\n"
"s_barrier\n"
"v_mfma_f32_16x16x32_bf16 v[16:19], a[160:163], a[192:195], v[16:19]\n"
"ds_read_b128 a[0:3], v224 offset:0\n"
"v_mfma_f32_16x16x32_bf16 v[16:19], a[164:167], a[196:199], v[16:19]\n"
"s_add_u32 m0, s77, 44032\n"
"s_add_u32 s70, s72, 31872\n"
"v_mfma_f32_16x16x32_bf16 v[48:51], a[160:163], a[200:203], v[48:51]\n"
"buffer_load_dwordx4 v229, s[40:43], s70 offen lds\n"
"v_mfma_f32_16x16x32_bf16 v[48:51], a[164:167], a[204:207], v[48:51]\n"
"ds_read_b128 a[4:7], v225 offset:0\n"
"ds_read_b128 a[8:11], v224 offset:2048\n"
"v_mfma_f32_16x16x32_bf16 v[80:83], a[160:163], a[208:211], v[80:83]\n"
"s_add_u32 m0, s77, 45056\n"
"s_add_u32 s70, s72, 42112\n"
"v_mfma_f32_16x16x32_bf16 v[80:83], a[164:167], a[212:215], v[80:83]\n"
"buffer_load_dwordx4 v228, s[40:43], s70 offen lds\n"
"v_mfma_f32_16x16x32_bf16 v[112:115], a[160:163], a[216:219], v[112:115]\n"
"s_add_u32 m0, s77, 46080\n"
"s_add_u32 s70, s72, 52352\n"
"v_mfma_f32_16x16x32_bf16 v[112:115], a[164:167], a[220:223], v[112:115]\n"
"ds_read_b128 a[12:15], v225 offset:2048\n"
"ds_read_b128 a[16:19], v224 offset:4096\n"
"v_mfma_f32_16x16x32_bf16 v[144:147], a[160:163], a[224:227], v[144:147]\n"
"buffer_load_dwordx4 v229, s[40:43], s70 offen lds\n"
"v_mfma_f32_16x16x32_bf16 v[144:147], a[164:167], a[228:231], v[144:147]\n"
"s_add_u32 m0, s78, 65536\n"
"ds_read_b128 a[20:23], v225 offset:4096\n"
"v_mfma_f32_16x16x32_bf16 v[176:179], a[160:163], a[232:235], v[176:179]\n"
"buffer_load_dwordx4 v230, s[44:47], s73 offen lds\n"
"v_mfma_f32_16x16x32_bf16 v[176:179], a[164:167], a[236:239], v[176:179]\n"
"s_add_u32 m0, s78, 73472\n"
"ds_read_b128 a[24:27], v234 offset:0\n"
"v_mfma_f32_16x16x32_bf16 v[20:23], a[168:171], a[192:195], v[20:23]\n"
"buffer_load_dwordx4 v230, s[44:47], s73 offen offset:256 lds\n"
"v_mfma_f32_16x16x32_bf16 v[20:23], a[172:175], a[196:199], v[20:23]\n"
"s_add_u32 s70, s73, 262144\n"
"ds_read_b128 a[28:31], v235 offset:0\n"
"v_mfma_f32_16x16x32_bf16 v[52:55], a[168:171], a[200:203], v[52:55]\n"
"s_add_u32 m0, s78, 69632\n"
"ds_read_b128 a[32:35], v234 offset:2048\n"
"v_mfma_f32_16x16x32_bf16 v[52:55], a[172:175], a[204:207], v[52:55]\n"
"buffer_load_dwordx4 v230, s[44:47], s70 offen lds\n"
"v_mfma_f32_16x16x32_bf16 v[84:87], a[168:171], a[208:211], v[84:87]\n"
"s_add_u32 m0, s78, 77568\n"
"ds_read_b128 a[36:39], v235 offset:2048\n"
"v_mfma_f32_16x16x32_bf16 v[84:87], a[172:175], a[212:215], v[84:87]\n"
"buffer_load_dwordx4 v230, s[44:47], s70 offen offset:256 lds\n"
"v_mfma_f32_16x16x32_bf16 v[116:119], a[168:171], a[216:219], v[116:119]\n"
"s_add_u32 m0, s91, 13184\n"
"ds_read_b128 a[40:43], v234 offset:4096\n"
"v_mfma_f32_16x16x32_bf16 v[116:119], a[172:175], a[220:223], v[116:119]\n"
"buffer_load_dwordx4 v246, s[52:55], s90 offen offset:2176 nt lds\n"
"s_sub_u32 m0, m0, 896\n"
"s_nop 0\n"
"buffer_load_dwordx4 v246, s[52:55], s90 offen offset:2048 nt lds\n"
"v_mfma_f32_16x16x32_bf16 v[148:151], a[168:171], a[224:227], v[148:151]\n"
"s_add_u32 s90, s90, 12288\n"
"ds_read_b128 a[44:47], v235 offset:4096\n"
"v_mfma_f32_16x16x32_bf16 v[148:151], a[172:175], a[228:231], v[148:151]\n"
"s_and_b32 s90, s90, 0x3ffff\n"
"ds_read_b64_tr_b16 a[128:129], v226 offset:57344\n"
"v_mfma_f32_16x16x32_bf16 v[180:183], a[168:171], a[232:235], v[180:183]\n"
"s_add_u32 s91, s91, 65536\n"
"ds_read_b64_tr_b16 a[130:131], v226 offset:57600\n"
"v_mfma_f32_16x16x32_bf16 v[180:183], a[172:175], a[236:239], v[180:183]\n"
"s_cmp_ge_u32 s91, s66\n"
"s_cselect_b32 s80, 163840, 0\n"
"v_mfma_f32_16x16x32_bf16 v[24:27], a[176:179], a[192:195], v[24:27]\n"
"s_sub_u32 s91, s91, s80\n"
"ds_read_b64_tr_b16 a[132:133], v226 offset:61440\n"
"v_mfma_f32_16x16x32_bf16 v[24:27], a[180:183], a[196:199], v[24:27]\n"
"s_add_u32 s36, %8, 38797312\n"
"s_addc_u32 s37, %9, 0\n"
"v_mfma_f32_16x16x32_bf16 v[56:59], a[176:179], a[200:203], v[56:59]\n"
"s_and_b32 s37, s37, 0xffff\n"
"ds_read_b64_tr_b16 a[134:135], v226 offset:61696\n"
"v_mfma_f32_16x16x32_bf16 v[56:59], a[180:183], a[204:207], v[56:59]\n"
"s_mov_b32 s38, 98304\n"
"s_mov_b32 s39, 0x20000\n"
"v_mfma_f32_16x16x32_bf16 v[88:91], a[176:179], a[208:211], v[88:91]\n"
"ds_read_b64_tr_b16 a[136:137], v226 offset:57352\n"
"s_lshl_b32 s71, %15, 6\n"
"v_mfma_f32_16x16x32_bf16 v[88:91], a[180:183], a[212:215], v[88:91]\n"
"ds_read_b64_tr_b16 a[138:139], v226 offset:57608\n"
"s_add_u32 s71, s71, 4096\n"
"v_mfma_f32_16x16x32_bf16 v[120:123], a[176:179], a[216:219], v[120:123]\n"
"v_mbcnt_lo_u32_b32 v220, -1, 0\n"
"v_mbcnt_hi_u32_b32 v220, -1, v220\n"
"v_or_b32_e32 v220, s60, v220\n"
"v_cmp_eq_u32_e32 vcc, 0, v220\n"
"v_bfrev_b32_e32 v220, 1\n"
"v_mov_b32_e32 v221, s71\n"
"v_cndmask_b32_e32 v220, v220, v221, vcc\n"
"v_mov_b32_e32 v222, s62\n"
"v_mov_b32_e32 v223, s65\n"
"buffer_store_dwordx2 v[222:223], v220, s[36:39], 0 offen sc1\n"
"v_mfma_f32_16x16x32_bf16 v[120:123], a[180:183], a[220:223], v[120:123]\n"
"ds_read_b64_tr_b16 a[140:141], v226 offset:61448\n"
"ds_read_b64_tr_b16 a[142:143], v226 offset:61704\n"
"v_mfma_f32_16x16x32_bf16 v[152:155], a[176:179], a[224:227], v[152:155]\n"
"ds_read_b64_tr_b16 a[144:145], v226 offset:57856\n"
"ds_read_b64_tr_b16 a[146:147], v226 offset:58112\n"
"v_mfma_f32_16x16x32_bf16 v[152:155], a[180:183], a[228:231], v[152:155]\n"
"ds_read_b64_tr_b16 a[148:149], v226 offset:61952\n"
"ds_read_b64_tr_b16 a[150:151], v226 offset:62208\n"
"v_mfma_f32_16x16x32_bf16 v[184:187], a[176:179], a[232:235], v[184:187]\n"
"ds_read_b64_tr_b16 a[152:153], v226 offset:57864\n"
"ds_read_b64_tr_b16 a[154:155], v226 offset:58120\n"
"v_mfma_f32_16x16x32_bf16 v[184:187], a[180:183], a[236:239], v[184:187]\n"
"v_mfma_f32_16x16x32_bf16 v[28:31], a[184:187], a[192:195], v[28:31]\n"
"ds_read_b64_tr_b16 a[156:157], v226 offset:61960\n"
"v_mfma_f32_16x16x32_bf16 v[28:31], a[188:191], a[196:199], v[28:31]\n"
"ds_read_b64_tr_b16 a[158:159], v226 offset:62216\n"
"v_mfma_f32_16x16x32_bf16 v[60:63], a[184:187], a[200:203], v[60:63]\n"
"v_mfma_f32_16x16x32_bf16 v[60:63], a[188:191], a[204:207], v[60:63]\n"
"v_mfma_f32_16x16x32_bf16 v[92:95], a[184:187], a[208:211], v[92:95]\n"
"v_mfma_f32_16x16x32_bf16 v[92:95], a[188:191], a[212:215], v[92:95]\n"
"v_mfma_f32_16x16x32_bf16 v[124:127], a[184:187], a[216:219], v[124:127]\n"
"v_mfma_f32_16x16x32_bf16 v[124:127], a[188:191], a[220:223], v[124:127]\n"
"v_mfma_f32_16x16x32_bf16 v[156:159], a[184:187], a[224:227], v[156:159]\n"
"v_mfma_f32_16x16x32_bf16 v[156:159], a[188:191], a[228:231], v[156:159]\n"
"v_mfma_f32_16x16x32_bf16 v[188:191], a[184:187], a[232:235], v[188:191]\n"
"v_mfma_f32_16x16x32_bf16 v[188:191], a[188:191], a[236:239], v[188:191]\n"
"s_waitcnt vmcnt(10) lgkmcnt(0)\n"
"s_barrier\n"
"v_mfma_f32_16x16x32_bf16 v[0:3], a[128:131], a[0:3], v[0:3]\n"
"ds_read_b64_tr_b16 a[160:161], v226 offset:0\n"
"s_add_u32 m0, s78, 81792\n"
"v_mfma_f32_16x16x32_bf16 v[0:3], a[132:135], a[4:7], v[0:3]\n"
"buffer_load_dwordx4 v230, s[44:47], s73 offen offset:128 lds\n"
"v_mfma_f32_16x16x32_bf16 v[32:35], a[128:131], a[8:11], v[32:35]\n"
"ds_read_b64_tr_b16 a[162:163], v226 offset:256\n"
"v_mfma_f32_16x16x32_bf16 v[32:35], a[132:135], a[12:15], v[32:35]\n"
"s_add_u32 m0, s78, 89728\n"
"v_mfma_f32_16x16x32_bf16 v[64:67], a[128:131], a[16:19], v[64:67]\n"
"ds_read_b64_tr_b16 a[164:165], v226 offset:4096\n"
"v_mfma_f32_16x16x32_bf16 v[64:67], a[132:135], a[20:23], v[64:67]\n"
"buffer_load_dwordx4 v230, s[44:47], s73 offen offset:384 lds\n"
"v_mfma_f32_16x16x32_bf16 v[96:99], a[128:131], a[24:27], v[96:99]\n"
"s_add_u32 s70, s73, 262144\n"
"v_mfma_f32_16x16x32_bf16 v[96:99], a[132:135], a[28:31], v[96:99]\n"
"ds_read_b64_tr_b16 a[166:167], v226 offset:4352\n"
"v_mfma_f32_16x16x32_bf16 v[128:131], a[128:131], a[32:35], v[128:131]\n"
"s_add_u32 m0, s78, 85888\n"
"v_mfma_f32_16x16x32_bf16 v[128:131], a[132:135], a[36:39], v[128:131]\n"
"ds_read_b64_tr_b16 a[168:169], v226 offset:8\n"
"v_mfma_f32_16x16x32_bf16 v[160:163], a[128:131], a[40:43], v[160:163]\n"
"buffer_load_dwordx4 v230, s[44:47], s70 offen offset:128 lds\n"
"v_mfma_f32_16x16x32_bf16 v[160:163], a[132:135], a[44:47], v[160:163]\n"
"s_add_u32 m0, s78, 93824\n"
"v_mfma_f32_16x16x32_bf16 v[4:7], a[136:139], a[0:3], v[4:7]\n"
"ds_read_b64_tr_b16 a[170:171], v226 offset:264\n"
"v_mfma_f32_16x16x32_bf16 v[4:7], a[140:143], a[4:7], v[4:7]\n"
"buffer_load_dwordx4 v230, s[44:47], s70 offen offset:384 lds\n"
"v_mfma_f32_16x16x32_bf16 v[36:39], a[136:139], a[8:11], v[36:39]\n"
"ds_read_b64_tr_b16 a[172:173], v226 offset:4104\n"
"s_add_u32 s73, s73, 524288\n"
"v_mfma_f32_16x16x32_bf16 v[36:39], a[140:143], a[12:15], v[36:39]\n"
"v_mfma_f32_16x16x32_bf16 v[68:71], a[136:139], a[16:19], v[68:71]\n"
"s_mov_b32 s56, %10\n"
"v_mfma_f32_16x16x32_bf16 v[68:71], a[140:143], a[20:23], v[68:71]\n"
"ds_read_b64_tr_b16 a[174:175], v226 offset:4360\n"
"s_and_b32 s57, %11, 0xffff\n"
"v_mfma_f32_16x16x32_bf16 v[100:103], a[136:139], a[24:27], v[100:103]\n"
"v_mfma_f32_16x16x32_bf16 v[100:103], a[140:143], a[28:31], v[100:103]\n"
"ds_read_b64_tr_b16 a[176:177], v226 offset:512\n"
"s_mov_b32 s58, 1310720\n"
"v_mfma_f32_16x16x32_bf16 v[132:135], a[136:139], a[32:35], v[132:135]\n"
"s_mov_b32 s59, 0x20000\n"
"v_mfma_f32_16x16x32_bf16 v[132:135], a[140:143], a[36:39], v[132:135]\n"
"ds_read_b64_tr_b16 a[178:179], v226 offset:768\n"
"v_mfma_f32_16x16x32_bf16 v[164:167], a[136:139], a[40:43], v[164:167]\n"
"s_lshr_b32 s71, %15, 3\n"
"v_mfma_f32_16x16x32_bf16 v[164:167], a[140:143], a[44:47], v[164:167]\n"
"v_mfma_f32_16x16x32_bf16 v[8:11], a[144:147], a[0:3], v[8:11]\n"
"ds_read_b64_tr_b16 a[180:181], v226 offset:4608\n"
"s_lshl_b32 s71, s71, 2\n"
"v_mfma_f32_16x16x32_bf16 v[8:11], a[148:151], a[4:7], v[8:11]\n"
"s_add_u32 s71, s71, s60\n"
"v_mfma_f32_16x16x32_bf16 v[40:43], a[144:147], a[8:11], v[40:43]\n"
"ds_read_b64_tr_b16 a[182:183], v226 offset:4864\n"
"v_mfma_f32_16x16x32_bf16 v[40:43], a[148:151], a[12:15], v[40:43]\n"
"s_and_b32 s70, s71, 0\n"
"v_mfma_f32_16x16x32_bf16 v[72:75], a[144:147], a[16:19], v[72:75]\n"
"s_add_u32 s71, s71, s70\n"
"v_mfma_f32_16x16x32_bf16 v[72:75], a[148:151], a[20:23], v[72:75]\n"
"ds_read_b64_tr_b16 a[184:185], v226 offset:520\n"
"v_mfma_f32_16x16x32_bf16 v[104:107], a[144:147], a[24:27], v[104:107]\n"
"s_mul_i32 s71, s71, 10240\n"
"v_mfma_f32_16x16x32_bf16 v[104:107], a[148:151], a[28:31], v[104:107]\n"
"ds_read_b64_tr_b16 a[186:187], v226 offset:776\n"
"v_mfma_f32_16x16x32_bf16 v[136:139], a[144:147], a[32:35], v[136:139]\n"
"v_lshlrev_b32_e32 v232, 4, v239\n"
"v_mfma_f32_16x16x32_bf16 v[136:139], a[148:151], a[36:39], v[136:139]\n"
"s_mov_b32 s94, s71\n"
"v_mfma_f32_16x16x32_bf16 v[168:171], a[144:147], a[40:43], v[168:171]\n"
"ds_read_b64_tr_b16 a[188:189], v226 offset:4616\n"
"v_mfma_f32_16x16x32_bf16 v[168:171], a[148:151], a[44:47], v[168:171]\n"
"buffer_load_dwordx4 v[248:251], v232, s[56:59], s94 offen \n"
"v_mfma_f32_16x16x32_bf16 v[12:15], a[152:155], a[0:3], v[12:15]\n"
"ds_read_b64_tr_b16 a[190:191], v226 offset:4872\n"
"buffer_load_dwordx4 v[248:251], v232, s[56:59], s94 offen offset:1024 \n"
"v_mfma_f32_16x16x32_bf16 v[12:15], a[156:159], a[4:7], v[12:15]\n"
"v_mfma_f32_16x16x32_bf16 v[44:47], a[152:155], a[8:11], v[44:47]\n"
"buffer_load_dwordx4 v[248:251], v232, s[56:59], s94 offen offset:2048 \n"
"v_mfma_f32_16x16x32_bf16 v[44:47], a[156:159], a[12:15], v[44:47]\n"
"v_mfma_f32_16x16x32_bf16 v[76:79], a[152:155], a[16:19], v[76:79]\n"
"buffer_load_dwordx4 v[248:251], v232, s[56:59], s94 offen offset:3072 \n"
"v_mfma_f32_16x16x32_bf16 v[76:79], a[156:159], a[20:23], v[76:79]\n"
"s_add_u32 s94, s94, 4096\n"
"v_mfma_f32_16x16x32_bf16 v[108:111], a[152:155], a[24:27], v[108:111]\n"
"v_mfma_f32_16x16x32_bf16 v[108:111], a[156:159], a[28:31], v[108:111]\n"
"buffer_load_dwordx4 v[248:251], v232, s[56:59], s94 offen \n"
"v_mfma_f32_16x16x32_bf16 v[140:143], a[152:155], a[32:35], v[140:143]\n"
"v_mfma_f32_16x16x32_bf16 v[140:143], a[156:159], a[36:39], v[140:143]\n"
"v_mfma_f32_16x16x32_bf16 v[172:175], a[152:155], a[40:43], v[172:175]\n"
"v_mfma_f32_16x16x32_bf16 v[172:175], a[156:159], a[44:47], v[172:175]\n"
"s_waitcnt vmcnt(12) lgkmcnt(0)\n"
"s_barrier\n"
"v_mfma_f32_16x16x32_bf16 v[16:19], a[160:163], a[0:3], v[16:19]\n"
"ds_read_b128 a[192:195], v224 offset:40960\n"
"buffer_load_dwordx4 v[248:251], v232, s[56:59], s94 offen offset:1024 \n"
"v_mfma_f32_16x16x32_bf16 v[16:19], a[164:167], a[4:7], v[16:19]\n"
"ds_read_b128 a[196:199], v225 offset:40960\n"
"v_mfma_f32_16x16x32_bf16 v[48:51], a[160:163], a[8:11], v[48:51]\n"
"ds_read_b128 a[200:203], v224 offset:43008\n"
"v_mfma_f32_16x16x32_bf16 v[48:51], a[164:167], a[12:15], v[48:51]\n"
"v_mfma_f32_16x16x32_bf16 v[80:83], a[160:163], a[16:19], v[80:83]\n"
"ds_read_b128 a[204:207], v225 offset:43008\n"
"v_mfma_f32_16x16x32_bf16 v[80:83], a[164:167], a[20:23], v[80:83]\n"
"ds_read_b128 a[208:211], v224 offset:45056\n"
"v_mfma_f32_16x16x32_bf16 v[112:115], a[160:163], a[24:27], v[112:115]\n"
"ds_read_b128 a[212:215], v225 offset:45056\n"
"v_mfma_f32_16x16x32_bf16 v[112:115], a[164:167], a[28:31], v[112:115]\n"
"buffer_load_dwordx4 v[248:251], v232, s[56:59], s94 offen offset:2048 \n"
"v_mfma_f32_16x16x32_bf16 v[144:147], a[160:163], a[32:35], v[144:147]\n"
"ds_read_b128 a[216:219], v234 offset:40960\n"
"v_mfma_f32_16x16x32_bf16 v[144:147], a[164:167], a[36:39], v[144:147]\n"
"ds_read_b128 a[220:223], v235 offset:40960\n"
"v_mfma_f32_16x16x32_bf16 v[176:179], a[160:163], a[40:43], v[176:179]\n"
"v_mfma_f32_16x16x32_bf16 v[176:179], a[164:167], a[44:47], v[176:179]\n"
"ds_read_b128 a[224:227], v234 offset:43008\n"
"v_mfma_f32_16x16x32_bf16 v[20:23], a[168:171], a[0:3], v[20:23]\n"
"ds_read_b128 a[228:231], v235 offset:43008\n"
"v_mfma_f32_16x16x32_bf16 v[20:23], a[172:175], a[4:7], v[20:23]\n"
"ds_read_b128 a[232:235], v234 offset:45056\n"
"v_mfma_f32_16x16x32_bf16 v[52:55], a[168:171], a[8:11], v[52:55]\n"
"v_mfma_f32_16x16x32_bf16 v[52:55], a[172:175], a[12:15], v[52:55]\n"
"ds_read_b128 a[236:239], v235 offset:45056\n"
"buffer_load_dwordx4 v[248:251], v232, s[56:59], s94 offen offset:3072 \n"
"v_mfma_f32_16x16x32_bf16 v[84:87], a[168:171], a[16:19], v[84:87]\n"
"ds_read_b64_tr_b16 a[128:129], v226 offset:40960\n"
"v_mfma_f32_16x16x32_bf16 v[84:87], a[172:175], a[20:23], v[84:87]\n"
"v_mfma_f32_16x16x32_bf16 v[116:119], a[168:171], a[24:27], v[116:119]\n"
"ds_read_b64_tr_b16 a[130:131], v226 offset:41216\n"
"v_mfma_f32_16x16x32_bf16 v[116:119], a[172:175], a[28:31], v[116:119]\n"
"ds_read_b64_tr_b16 a[132:133], v226 offset:45056\n"
"v_mfma_f32_16x16x32_bf16 v[148:151], a[168:171], a[32:35], v[148:151]\n"
"ds_read_b64_tr_b16 a[134:135], v226 offset:45312\n"
"v_mfma_f32_16x16x32_bf16 v[148:151], a[172:175], a[36:39], v[148:151]\n"
"v_mfma_f32_16x16x32_bf16 v[180:183], a[168:171], a[40:43], v[180:183]\n"
"ds_read_b64_tr_b16 a[136:137], v226 offset:40968\n"
"s_add_u32 s94, s94, 4096\n"
"v_mfma_f32_16x16x32_bf16 v[180:183], a[172:175], a[44:47], v[180:183]\n"
"ds_read_b64_tr_b16 a[138:139], v226 offset:41224\n"
"v_mfma_f32_16x16x32_bf16 v[24:27], a[176:179], a[0:3], v[24:27]\n"
"v_mfma_f32_16x16x32_bf16 v[24:27], a[180:183], a[4:7], v[24:27]\n"
"ds_read_b64_tr_b16 a[140:141], v226 offset:45064\n"
"v_mfma_f32_16x16x32_bf16 v[56:59], a[176:179], a[8:11], v[56:59]\n"
"ds_read_b64_tr_b16 a[142:143], v226 offset:45320\n"
"v_mfma_f32_16x16x32_bf16 v[56:59], a[180:183], a[12:15], v[56:59]\n"
"ds_read_b64_tr_b16 a[144:145], v226 offset:41472\n"
"v_mfma_f32_16x16x32_bf16 v[88:91], a[176:179], a[16:19], v[88:91]\n"
"v_mfma_f32_16x16x32_bf16 v[88:91], a[180:183], a[20:23], v[88:91]\n"
"ds_read_b64_tr_b16 a[146:147], v226 offset:41728\n"
"v_mfma_f32_16x16x32_bf16 v[120:123], a[176:179], a[24:27], v[120:123]\n"
"ds_read_b64_tr_b16 a[148:149], v226 offset:45568\n"
"buffer_load_dwordx4 v[248:251], v232, s[56:59], s94 offen \n"
"v_mfma_f32_16x16x32_bf16 v[120:123], a[180:183], a[28:31], v[120:123]\n"
"v_mfma_f32_16x16x32_bf16 v[152:155], a[176:179], a[32:35], v[152:155]\n"
"ds_read_b64_tr_b16 a[150:151], v226 offset:45824\n"
"v_mfma_f32_16x16x32_bf16 v[152:155], a[180:183], a[36:39], v[152:155]\n"
"ds_read_b64_tr_b16 a[152:153], v226 offset:41480\n"
"v_mfma_f32_16x16x32_bf16 v[184:187], a[176:179], a[40:43], v[184:187]\n"
"ds_read_b64_tr_b16 a[154:155], v226 offset:41736\n"
"v_mfma_f32_16x16x32_bf16 v[184:187], a[180:183], a[44:47], v[184:187]\n"
"v_mfma_f32_16x16x32_bf16 v[28:31], a[184:187], a[0:3], v[28:31]\n"
"ds_read_b64_tr_b16 a[156:157], v226 offset:45576\n"
"v_mfma_f32_16x16x32_bf16 v[28:31], a[188:191], a[4:7], v[28:31]\n"
"ds_read_b64_tr_b16 a[158:159], v226 offset:45832\n"
"buffer_load_dwordx4 v[248:251], v232, s[56:59], s94 offen offset:1024 \n"
"v_mfma_f32_16x16x32_bf16 v[60:63], a[184:187], a[8:11], v[60:63]\n"
"v_mfma_f32_16x16x32_bf16 v[60:63], a[188:191], a[12:15], v[60:63]\n"
"v_mfma_f32_16x16x32_bf16 v[92:95], a[184:187], a[16:19], v[92:95]\n"
"v_mfma_f32_16x16x32_bf16 v[92:95], a[188:191], a[20:23], v[92:95]\n"
"v_mfma_f32_16x16x32_bf16 v[124:127], a[184:187], a[24:27], v[124:127]\n"
"v_mfma_f32_16x16x32_bf16 v[124:127], a[188:191], a[28:31], v[124:127]\n"
"v_mfma_f32_16x16x32_bf16 v[156:159], a[184:187], a[32:35], v[156:159]\n"
"v_mfma_f32_16x16x32_bf16 v[156:159], a[188:191], a[36:39], v[156:159]\n"
"v_mfma_f32_16x16x32_bf16 v[188:191], a[184:187], a[40:43], v[188:191]\n"
"v_mfma_f32_16x16x32_bf16 v[188:191], a[188:191], a[44:47], v[188:191]\n"
"s_waitcnt vmcnt(10) lgkmcnt(0)\n"
"s_barrier\n"
"v_mfma_f32_16x16x32_bf16 v[0:3], a[128:131], a[192:195], v[0:3]\n"
"ds_read_b64_tr_b16 a[160:161], v226 offset:57344\n"
"s_mov_b32 m0, s67\n"
"v_mfma_f32_16x16x32_bf16 v[0:3], a[132:135], a[196:199], v[0:3]\n"
"v_mfma_f32_16x16x32_bf16 v[32:35], a[128:131], a[200:203], v[32:35]\n"
"ds_read_b64_tr_b16 a[162:163], v226 offset:57600\n"
"v_mfma_f32_16x16x32_bf16 v[32:35], a[132:135], a[204:207], v[32:35]\n"
"buffer_load_dwordx4 v244, s[48:51], s64 offen nt lds\n"
"s_add_u32 m0, m0, 896\n"
"s_nop 0\n"
"buffer_load_dwordx4 v244, s[48:51], s64 offen offset:128 nt lds\n"
"v_mfma_f32_16x16x32_bf16 v[64:67], a[128:131], a[208:211], v[64:67]\n"
"ds_read_b64_tr_b16 a[164:165], v226 offset:61440\n"
"v_mfma_f32_16x16x32_bf16 v[64:67], a[132:135], a[212:215], v[64:67]\n"
"v_mfma_f32_16x16x32_bf16 v[96:99], a[128:131], a[216:219], v[96:99]\n"
"v_mfma_f32_16x16x32_bf16 v[96:99], a[132:135], a[220:223], v[96:99]\n"
"ds_read_b64_tr_b16 a[166:167], v226 offset:61696\n"
"v_mfma_f32_16x16x32_bf16 v[128:131], a[128:131], a[224:227], v[128:131]\n"
"buffer_load_dwordx4 v245, s[48:51], s64 offen offset:2176 nt lds\n"
"s_sub_u32 m0, m0, 896\n"
"s_nop 0\n"
"buffer_load_dwordx4 v245, s[48:51], s64 offen offset:2048 nt lds\n"
"v_mfma_f32_16x16x32_bf16 v[128:131], a[132:135], a[228:231], v[128:131]\n"
"ds_read_b64_tr_b16 a[168:169], v226 offset:57352\n"
"v_mfma_f32_16x16x32_bf16 v[160:163], a[128:131], a[232:235], v[160:163]\n"
"v_mfma_f32_16x16x32_bf16 v[160:163], a[132:135], a[236:239], v[160:163]\n"
"s_add_u32 m0, m0, 4096\n"
"v_mfma_f32_16x16x32_bf16 v[4:7], a[136:139], a[192:195], v[4:7]\n"
"ds_read_b64_tr_b16 a[170:171], v226 offset:57608\n"
"v_mfma_f32_16x16x32_bf16 v[4:7], a[140:143], a[196:199], v[4:7]\n"
"v_mfma_f32_16x16x32_bf16 v[36:39], a[136:139], a[200:203], v[36:39]\n"
"ds_read_b64_tr_b16 a[172:173], v226 offset:61448\n"
"v_mfma_f32_16x16x32_bf16 v[36:39], a[140:143], a[204:207], v[36:39]\n"
"s_add_u32 s64, s64, 4096\n"
"v_mfma_f32_16x16x32_bf16 v[68:71], a[136:139], a[208:211], v[68:71]\n"
"v_mfma_f32_16x16x32_bf16 v[68:71], a[140:143], a[212:215], v[68:71]\n"
"ds_read_b64_tr_b16 a[174:175], v226 offset:61704\n"
"v_mfma_f32_16x16x32_bf16 v[100:103], a[136:139], a[216:219], v[100:103]\n"
"buffer_load_dwordx4 v244, s[48:51], s64 offen nt lds\n"
"s_add_u32 m0, m0, 896\n"
"s_nop 0\n"
"buffer_load_dwordx4 v244, s[48:51], s64 offen offset:128 nt lds\n"
"v_mfma_f32_16x16x32_bf16 v[100:103], a[140:143], a[220:223], v[100:103]\n"
"ds_read_b64_tr_b16 a[176:177], v226 offset:57856\n"
"v_mfma_f32_16x16x32_bf16 v[132:135], a[136:139], a[224:227], v[132:135]\n"
"v_mfma_f32_16x16x32_bf16 v[132:135], a[140:143], a[228:231], v[132:135]\n"
"ds_read_b64_tr_b16 a[178:179], v226 offset:58112\n"
"v_mfma_f32_16x16x32_bf16 v[164:167], a[136:139], a[232:235], v[164:167]\n"
"buffer_load_dwordx4 v245, s[48:51], s64 offen offset:2176 nt lds\n"
"s_sub_u32 m0, m0, 896\n"
"s_nop 0\n"
"buffer_load_dwordx4 v245, s[48:51], s64 offen offset:2048 nt lds\n"
"v_mfma_f32_16x16x32_bf16 v[164:167], a[140:143], a[236:239], v[164:167]\n"
"v_mfma_f32_16x16x32_bf16 v[8:11], a[144:147], a[192:195], v[8:11]\n"
"ds_read_b64_tr_b16 a[180:181], v226 offset:61952\n"
"v_mfma_f32_16x16x32_bf16 v[8:11], a[148:151], a[196:199], v[8:11]\n"
"v_mfma_f32_16x16x32_bf16 v[40:43], a[144:147], a[200:203], v[40:43]\n"
"ds_read_b64_tr_b16 a[182:183], v226 offset:62208\n"
"s_add_u32 s64, s64, 12288\n"
"v_mfma_f32_16x16x32_bf16 v[40:43], a[148:151], a[204:207], v[40:43]\n"
"v_mfma_f32_16x16x32_bf16 v[72:75], a[144:147], a[208:211], v[72:75]\n"
"v_mfma_f32_16x16x32_bf16 v[72:75], a[148:151], a[212:215], v[72:75]\n"
"ds_read_b64_tr_b16 a[184:185], v226 offset:57864\n"
"v_mfma_f32_16x16x32_bf16 v[104:107], a[144:147], a[216:219], v[104:107]\n"
"s_and_b32 s64, s64, 0x3ffff\n"
"v_mfma_f32_16x16x32_bf16 v[104:107], a[148:151], a[220:223], v[104:107]\n"
"ds_read_b64_tr_b16 a[186:187], v226 offset:58120\n"
"v_mfma_f32_16x16x32_bf16 v[136:139], a[144:147], a[224:227], v[136:139]\n"
"v_mfma_f32_16x16x32_bf16 v[136:139], a[148:151], a[228:231], v[136:139]\n"
"s_add_u32 s67, s67, 65536\n"
"v_mfma_f32_16x16x32_bf16 v[168:171], a[144:147], a[232:235], v[168:171]\n"
"ds_read_b64_tr_b16 a[188:189], v226 offset:61960\n"
"v_mfma_f32_16x16x32_bf16 v[168:171], a[148:151], a[236:239], v[168:171]\n"
"v_mfma_f32_16x16x32_bf16 v[12:15], a[152:155], a[192:195], v[12:15]\n"
"ds_read_b64_tr_b16 a[190:191], v226 offset:62216\n"
"v_mfma_f32_16x16x32_bf16 v[12:15], a[156:159], a[196:199], v[12:15]\n"
"s_cmp_ge_u32 s67, s66\n"
"s_cselect_b32 s80, 163840, 0\n"
"v_mfma_f32_16x16x32_bf16 v[44:47], a[152:155], a[200:203], v[44:47]\n"
"v_mfma_f32_16x16x32_bf16 v[44:47], a[156:159], a[204:207], v[44:47]\n"
"v_mfma_f32_16x16x32_bf16 v[76:79], a[152:155], a[208:211], v[76:79]\n"
"v_mfma_f32_16x16x32_bf16 v[76:79], a[156:159], a[212:215], v[76:79]\n"
"s_sub_u32 s67, s67, s80\n"
"v_mfma_f32_16x16x32_bf16 v[108:111], a[152:155], a[216:219], v[108:111]\n"
"v_mfma_f32_16x16x32_bf16 v[108:111], a[156:159], a[220:223], v[108:111]\n"
"v_mfma_f32_16x16x32_bf16 v[140:143], a[152:155], a[224:227], v[140:143]\n"
"v_mfma_f32_16x16x32_bf16 v[140:143], a[156:159], a[228:231], v[140:143]\n"
"v_mfma_f32_16x16x32_bf16 v[172:175], a[152:155], a[232:235], v[172:175]\n"
"v_mfma_f32_16x16x32_bf16 v[172:175], a[156:159], a[236:239], v[172:175]\n"
"s_waitcnt vmcnt(23) lgkmcnt(0)\n"
"s_barrier\n"
"v_mfma_f32_16x16x32_bf16 v[16:19], a[160:163], a[192:195], v[16:19]\n"
"ds_read_b128 a[96:99], v240 offset:0\n"
"v_mfma_f32_16x16x32_bf16 v[16:19], a[164:167], a[196:199], v[16:19]\n"
"ds_read_b128 a[100:103], v241 offset:0\n"
"v_mfma_f32_16x16x32_bf16 v[20:23], a[168:171], a[192:195], v[20:23]\n"
"ds_read_b128 a[104:107], v240 offset:1024\n"
"v_mfma_f32_16x16x32_bf16 v[20:23], a[172:175], a[196:199], v[20:23]\n"
"ds_read_b128 a[108:111], v241 offset:1024\n"
"v_mfma_f32_16x16x32_bf16 v[24:27], a[176:179], a[192:195], v[24:27]\n"
"v_mfma_f32_16x16x32_bf16 v[24:27], a[180:183], a[196:199], v[24:27]\n"
"ds_read_b128 a[112:115], v240 offset:4096\n"
"v_mfma_f32_16x16x32_bf16 v[28:31], a[184:187], a[192:195], v[28:31]\n"
"ds_read_b128 a[116:119], v241 offset:4096\n"
"v_mfma_f32_16x16x32_bf16 v[28:31], a[188:191], a[196:199], v[28:31]\n"
"ds_read_b128 a[120:123], v240 offset:5120\n"
"v_mfma_f32_16x16x32_bf16 v[48:51], a[160:163], a[200:203], v[48:51]\n"
"ds_read_b128 a[124:127], v241 offset:5120\n"
"v_mfma_f32_16x16x32_bf16 v[48:51], a[164:167], a[204:207], v[48:51]\n"
"v_mfma_f32_16x16x32_bf16 v[52:55], a[168:171], a[200:203], v[52:55]\n"
"v_mfma_f32_16x16x32_bf16 v[52:55], a[172:175], a[204:207], v[52:55]\n"
"s_mov_b32 s70, 0x3e0293ee\n"
"s_mov_b32 s71, 0x3e0293ee\n"
"v_mfma_f32_16x16x32_bf16 v[56:59], a[176:179], a[200:203], v[56:59]\n"
"v_pk_mul_f32 v[0:1], v[0:1], s[70:71]\n"
"v_pk_mul_f32 v[2:3], v[2:3], s[70:71]\n"
"v_pk_mul_f32 v[4:5], v[4:5], s[70:71]\n"
"s_add_u32 m0, s91, 8192\n"
"s_cmp_eq_u32 s69, 131072\n"
"s_cselect_b32 s68, -131072, s81\n"
"v_mfma_f32_16x16x32_bf16 v[56:59], a[180:183], a[204:207], v[56:59]\n"
"v_pk_mul_f32 v[6:7], v[6:7], s[70:71]\n"
"v_pk_mul_f32 v[8:9], v[8:9], s[70:71]\n"
"v_pk_mul_f32 v[10:11], v[10:11], s[70:71]\n"
"s_add_u32 s69, s69, s68\n"
"v_mfma_f32_16x16x32_bf16 v[60:63], a[184:187], a[200:203], v[60:63]\n"
"v_pk_mul_f32 v[12:13], v[12:13], s[70:71]\n"
"v_pk_mul_f32 v[14:15], v[14:15], s[70:71]\n"
"v_mfma_f32_16x16x32_bf16 v[60:63], a[188:191], a[204:207], v[60:63]\n"
"v_pk_mul_f32 v[16:17], v[16:17], s[70:71]\n"
"v_pk_mul_f32 v[18:19], v[18:19], s[70:71]\n"
"v_pk_mul_f32 v[20:21], v[20:21], s[70:71]\n"
"v_add_u32_e32 v240, s68, v240\n"
"v_add_u32_e32 v241, s68, v241\n"
"v_mfma_f32_16x16x32_bf16 v[80:83], a[160:163], a[208:211], v[80:83]\n"
"v_pk_mul_f32 v[22:23], v[22:23], s[70:71]\n"
"v_pk_mul_f32 v[24:25], v[24:25], s[70:71]\n"
"v_pk_mul_f32 v[26:27], v[26:27], s[70:71]\n"
"v_mfma_f32_16x16x32_bf16 v[80:83], a[164:167], a[212:215], v[80:83]\n"
"v_pk_mul_f32 v[28:29], v[28:29], s[70:71]\n"
"v_pk_mul_f32 v[30:31], v[30:31], s[70:71]\n"
"v_mfma_f32_16x16x32_bf16 v[84:87], a[168:171], a[208:211], v[84:87]\n"
"v_cvt_pk_bf16_f32 v192, v0, v1\n"
"v_cvt_pk_bf16_f32 v193, v2, v3\n"
"v_cvt_pk_bf16_f32 v194, v4, v5\n"
"v_mfma_f32_16x16x32_bf16 v[84:87], a[172:175], a[212:215], v[84:87]\n"
"v_cvt_pk_bf16_f32 v195, v6, v7\n"
"v_cvt_pk_bf16_f32 v196, v8, v9\n"
"v_cvt_pk_bf16_f32 v197, v10, v11\n"
"v_mfma_f32_16x16x32_bf16 v[88:91], a[176:179], a[208:211], v[88:91]\n"
"v_cvt_pk_bf16_f32 v198, v12, v13\n"
"v_cvt_pk_bf16_f32 v199, v14, v15\n"
"v_mfma_f32_16x16x32_bf16 v[88:91], a[180:183], a[212:215], v[88:91]\n"
"v_cvt_pk_bf16_f32 v200, v16, v17\n"
"v_cvt_pk_bf16_f32 v201, v18, v19\n"
"v_cvt_pk_bf16_f32 v202, v20, v21\n"
"v_mfma_f32_16x16x32_bf16 v[92:95], a[184:187], a[208:211], v[92:95]\n"
"v_cvt_pk_bf16_f32 v203, v22, v23\n"
"v_cvt_pk_bf16_f32 v204, v24, v25\n"
"v_cvt_pk_bf16_f32 v205, v26, v27\n"
"v_mfma_f32_16x16x32_bf16 v[92:95], a[188:191], a[212:215], v[92:95]\n"
"v_cvt_pk_bf16_f32 v206, v28, v29\n"
"v_cvt_pk_bf16_f32 v207, v30, v31\n"
"v_mfma_f32_16x16x32_bf16 v[112:115], a[160:163], a[216:219], v[112:115]\n"
"v_accvgpr_write_b32 a0, v192\n"
"v_accvgpr_write_b32 a1, v193\n"
"v_accvgpr_write_b32 a2, v194\n"
"buffer_load_dwordx4 v246, s[52:55], s90 offen nt lds\n"
"s_add_u32 m0, m0, 896\n"
"s_nop 0\n"
"buffer_load_dwordx4 v246, s[52:55], s90 offen offset:128 nt lds\n"
"v_mfma_f32_16x16x32_bf16 v[112:115], a[164:167], a[220:223], v[112:115]\n"
"v_accvgpr_write_b32 a3, v195\n"
"v_accvgpr_write_b32 a4, v196\n"
"v_accvgpr_write_b32 a5, v197\n"
"v_mfma_f32_16x16x32_bf16 v[116:119], a[168:171], a[216:219], v[116:119]\n"
"v_accvgpr_write_b32 a6, v198\n"
"v_accvgpr_write_b32 a7, v199\n"
"v_mfma_f32_16x16x32_bf16 v[116:119], a[172:175], a[220:223], v[116:119]\n"
"v_accvgpr_write_b32 a8, v200\n"
"v_accvgpr_write_b32 a9, v201\n"
"v_accvgpr_write_b32 a10, v202\n"
"v_mfma_f32_16x16x32_bf16 v[120:123], a[176:179], a[216:219], v[120:123]\n"
"v_accvgpr_write_b32 a11, v203\n"
"v_accvgpr_write_b32 a12, v204\n"
"v_accvgpr_write_b32 a13, v205\n"
"v_mfma_f32_16x16x32_bf16 v[120:123], a[180:183], a[220:223], v[120:123]\n"
"v_accvgpr_write_b32 a14, v206\n"
"v_accvgpr_write_b32 a15, v207\n"
"v_mfma_f32_16x16x32_bf16 v[124:127], a[184:187], a[216:219], v[124:127]\n"
"v_pk_mul_f32 v[32:33], v[32:33], s[70:71]\n"
"v_pk_mul_f32 v[34:35], v[34:35], s[70:71]\n"
"v_pk_mul_f32 v[36:37], v[36:37], s[70:71]\n"
"v_mfma_f32_16x16x32_bf16 v[124:127], a[188:191], a[220:223], v[124:127]\n"
"v_pk_mul_f32 v[38:39], v[38:39], s[70:71]\n"
"v_pk_mul_f32 v[40:41], v[40:41], s[70:71]\n"
"v_pk_mul_f32 v[42:43], v[42:43], s[70:71]\n"
"v_mfma_f32_16x16x32_bf16 v[144:147], a[160:163], a[224:227], v[144:147]\n"
"v_pk_mul_f32 v[44:45], v[44:45], s[70:71]\n"
"v_pk_mul_f32 v[46:47], v[46:47], s[70:71]\n"
"v_mfma_f32_16x16x32_bf16 v[144:147], a[164:167], a[228:231], v[144:147]\n"
"v_pk_mul_f32 v[48:49], v[48:49], s[70:71]\n"
"v_pk_mul_f32 v[50:51], v[50:51], s[70:71]\n"
"v_pk_mul_f32 v[52:53], v[52:53], s[70:71]\n"
"v_mfma_f32_16x16x32_bf16 v[148:151], a[168:171], a[224:227], v[148:151]\n"
"v_pk_mul_f32 v[54:55], v[54:55], s[70:71]\n"
"v_pk_mul_f32 v[56:57], v[56:57], s[70:71]\n"
"v_pk_mul_f32 v[58:59], v[58:59], s[70:71]\n"
"v_mfma_f32_16x16x32_bf16 v[148:151], a[172:175], a[228:231], v[148:151]\n"
"v_pk_mul_f32 v[60:61], v[60:61], s[70:71]\n"
"v_pk_mul_f32 v[62:63], v[62:63], s[70:71]\n"
"v_mfma_f32_16x16x32_bf16 v[152:155], a[176:179], a[224:227], v[152:155]\n"
"v_cvt_pk_bf16_f32 v224, v32, v33\n"
"v_cvt_pk_bf16_f32 v225, v34, v35\n"
"v_cvt_pk_bf16_f32 v226, v36, v37\n"
"buffer_load_dwordx4 v246, s[52:55], s90 offen offset:2176 nt lds\n"
"s_sub_u32 m0, m0, 896\n"
"s_nop 0\n"
"buffer_load_dwordx4 v246, s[52:55], s90 offen offset:2048 nt lds\n"
"v_mfma_f32_16x16x32_bf16 v[152:155], a[180:183], a[228:231], v[152:155]\n"
"v_cvt_pk_bf16_f32 v227, v38, v39\n"
"v_cvt_pk_bf16_f32 v228, v40, v41\n"
"v_cvt_pk_bf16_f32 v229, v42, v43\n"
"v_mfma_f32_16x16x32_bf16 v[156:159], a[184:187], a[224:227], v[156:159]\n"
"v_cvt_pk_bf16_f32 v230, v44, v45\n"
"v_cvt_pk_bf16_f32 v231, v46, v47\n"
"v_mfma_f32_16x16x32_bf16 v[156:159], a[188:191], a[228:231], v[156:159]\n"
"v_cvt_pk_bf16_f32 v232, v48, v49\n"
"v_cvt_pk_bf16_f32 v233, v50, v51\n"
"v_cvt_pk_bf16_f32 v234, v52, v53\n"
"v_mfma_f32_16x16x32_bf16 v[176:179], a[160:163], a[232:235], v[176:179]\n"
"v_cvt_pk_bf16_f32 v235, v54, v55\n"
"v_cvt_pk_bf16_f32 v236, v56, v57\n"
"v_cvt_pk_bf16_f32 v237, v58, v59\n"
"v_mfma_f32_16x16x32_bf16 v[176:179], a[164:167], a[236:239], v[176:179]\n"
"v_cvt_pk_bf16_f32 v238, v60, v61\n"
"v_cvt_pk_bf16_f32 v239, v62, v63\n"
"v_mfma_f32_16x16x32_bf16 v[180:183], a[168:171], a[232:235], v[180:183]\n"
"v_accvgpr_write_b32 a16, v224\n"
"v_accvgpr_write_b32 a17, v225\n"
"v_accvgpr_write_b32 a18, v226\n"
"v_mfma_f32_16x16x32_bf16 v[180:183], a[172:175], a[236:239], v[180:183]\n"
"v_accvgpr_write_b32 a19, v227\n"
"v_accvgpr_write_b32 a20, v228\n"
"v_accvgpr_write_b32 a21, v229\n"
"v_mfma_f32_16x16x32_bf16 v[184:187], a[176:179], a[232:235], v[184:187]\n"
"v_accvgpr_write_b32 a22, v230\n"
"v_accvgpr_write_b32 a23, v231\n"
"v_mfma_f32_16x16x32_bf16 v[184:187], a[180:183], a[236:239], v[184:187]\n"
"v_accvgpr_write_b32 a24, v232\n"
"v_accvgpr_write_b32 a25, v233\n"
"v_accvgpr_write_b32 a26, v234\n"
"v_mfma_f32_16x16x32_bf16 v[188:191], a[184:187], a[232:235], v[188:191]\n"
"v_accvgpr_write_b32 a27, v235\n"
"v_accvgpr_write_b32 a28, v236\n"
"v_accvgpr_write_b32 a29, v237\n"
"v_mfma_f32_16x16x32_bf16 v[188:191], a[188:191], a[236:239], v[188:191]\n"
"v_accvgpr_write_b32 a30, v238\n"
"v_accvgpr_write_b32 a31, v239\n"
"s_waitcnt lgkmcnt(7)\n"
"v_mfma_f32_16x16x32_bf16 v[0:3], a[96:99], a[0:3], 0\n"
"v_pk_mul_f32 v[64:65], v[64:65], s[70:71]\n"
"v_pk_mul_f32 v[66:67], v[66:67], s[70:71]\n"
"v_pk_mul_f32 v[68:69], v[68:69], s[70:71]\n"
"v_pk_mul_f32 v[70:71], v[70:71], s[70:71]\n"
"v_pk_mul_f32 v[72:73], v[72:73], s[70:71]\n"
"v_pk_mul_f32 v[74:75], v[74:75], s[70:71]\n"
"s_add_u32 m0, s91, 12288\n"
"s_waitcnt lgkmcnt(6)\n"
"v_mfma_f32_16x16x32_bf16 v[0:3], a[100:103], a[4:7], v[0:3]\n"
"v_pk_mul_f32 v[76:77], v[76:77], s[70:71]\n"
"v_pk_mul_f32 v[78:79], v[78:79], s[70:71]\n"
"v_pk_mul_f32 v[80:81], v[80:81], s[70:71]\n"
"v_pk_mul_f32 v[82:83], v[82:83], s[70:71]\n"
"v_pk_mul_f32 v[84:85], v[84:85], s[70:71]\n"
"v_pk_mul_f32 v[86:87], v[86:87], s[70:71]\n"
"s_waitcnt lgkmcnt(5)\n"
"v_mfma_f32_16x16x32_bf16 v[0:3], a[104:107], a[8:11], v[0:3]\n"
"v_pk_mul_f32 v[88:89], v[88:89], s[70:71]\n"
"v_pk_mul_f32 v[90:91], v[90:91], s[70:71]\n"
"v_pk_mul_f32 v[92:93], v[92:93], s[70:71]\n"
"v_pk_mul_f32 v[94:95], v[94:95], s[70:71]\n"
"v_cvt_pk_bf16_f32 v192, v64, v65\n"
"v_cvt_pk_bf16_f32 v193, v66, v67\n"
"ds_read_b64_tr_b16 a[128:129], v242 offset:8192\n"
"ds_read_b64_tr_b16 a[130:131], v242 offset:12288\n"
"s_waitcnt lgkmcnt(6)\n"
"v_mfma_f32_16x16x32_bf16 v[0:3], a[108:111], a[12:15], v[0:3]\n"
"v_cvt_pk_bf16_f32 v194, v68, v69\n"
"v_cvt_pk_bf16_f32 v195, v70, v71\n"
"v_cvt_pk_bf16_f32 v196, v72, v73\n"
"v_cvt_pk_bf16_f32 v197, v74, v75\n"
"v_cvt_pk_bf16_f32 v198, v76, v77\n"
"v_cvt_pk_bf16_f32 v199, v78, v79\n"
"ds_read_b64_tr_b16 a[132:133], v243 offset:8192\n"
"s_waitcnt lgkmcnt(6)\n"
"v_mfma_f32_16x16x32_bf16 v[4:7], a[112:115], a[0:3], 0\n"
"v_cvt_pk_bf16_f32 v200, v80, v81\n"
"v_cvt_pk_bf16_f32 v201, v82, v83\n"
"v_cvt_pk_bf16_f32 v202, v84, v85\n"
"v_cvt_pk_bf16_f32 v203, v86, v87\n"
"v_cvt_pk_bf16_f32 v204, v88, v89\n"
"v_cvt_pk_bf16_f32 v205, v90, v91\n"
"ds_read_b64_tr_b16 a[134:135], v243 offset:12288\n"
"ds_read_b64_tr_b16 a[136:137], v242 offset:8704\n"
"s_waitcnt lgkmcnt(7)\n"
"v_mfma_f32_16x16x32_bf16 v[4:7], a[116:119], a[4:7], v[4:7]\n"
"v_cvt_pk_bf16_f32 v206, v92, v93\n"
"v_cvt_pk_bf16_f32 v207, v94, v95\n"
"v_accvgpr_write_b32 a32, v192\n"
"v_accvgpr_write_b32 a33, v193\n"
"v_accvgpr_write_b32 a34, v194\n"
"v_accvgpr_write_b32 a35, v195\n"
"ds_read_b64_tr_b16 a[138:139], v242 offset:12800\n"
"s_add_u32 s90, s90, 4096\n"
"s_waitcnt lgkmcnt(7)\n"
"v_mfma_f32_16x16x32_bf16 v[4:7], a[120:123], a[8:11], v[4:7]\n"
"v_accvgpr_write_b32 a36, v196\n"
"v_accvgpr_write_b32 a37, v197\n"
"v_accvgpr_write_b32 a38, v198\n"
"v_accvgpr_write_b32 a39, v199\n"
"v_accvgpr_write_b32 a40, v200\n"
"v_accvgpr_write_b32 a41, v201\n"
"ds_read_b64_tr_b16 a[140:141], v243 offset:8704\n"
"s_waitcnt lgkmcnt(7)\n"
"v_mfma_f32_16x16x32_bf16 v[4:7], a[124:127], a[12:15], v[4:7]\n"
"v_accvgpr_write_b32 a42, v202\n"
"v_accvgpr_write_b32 a43, v203\n"
"v_accvgpr_write_b32 a44, v204\n"
"v_accvgpr_write_b32 a45, v205\n"
"v_accvgpr_write_b32 a46, v206\n"
"v_accvgpr_write_b32 a47, v207\n"
"ds_read_b64_tr_b16 a[142:143], v243 offset:12800\n"
"ds_read_b64_tr_b16 a[144:145], v242 offset:9216\n"
"v_mfma_f32_16x16x32_bf16 v[32:35], a[96:99], a[16:19], 0\n"
"v_pk_mul_f32 v[96:97], v[96:97], s[70:71]\n"
"v_pk_mul_f32 v[98:99], v[98:99], s[70:71]\n"
"v_pk_mul_f32 v[100:101], v[100:101], s[70:71]\n"
"v_pk_mul_f32 v[102:103], v[102:103], s[70:71]\n"
"v_pk_mul_f32 v[104:105], v[104:105], s[70:71]\n"
"v_pk_mul_f32 v[106:107], v[106:107], s[70:71]\n"
"ds_read_b64_tr_b16 a[146:147], v242 offset:13312\n"
"v_mfma_f32_16x16x32_bf16 v[32:35], a[100:103], a[20:23], v[32:35]\n"
"v_pk_mul_f32 v[108:109], v[108:109], s[70:71]\n"
"v_pk_mul_f32 v[110:111], v[110:111], s[70:71]\n"
"v_pk_mul_f32 v[112:113], v[112:113], s[70:71]\n"
"v_pk_mul_f32 v[114:115], v[114:115], s[70:71]\n"
"v_pk_mul_f32 v[116:117], v[116:117], s[70:71]\n"
"v_pk_mul_f32 v[118:119], v[118:119], s[70:71]\n"
"ds_read_b64_tr_b16 a[148:149], v243 offset:9216\n"
"v_mfma_f32_16x16x32_bf16 v[32:35], a[104:107], a[24:27], v[32:35]\n"
"v_pk_mul_f32 v[120:121], v[120:121], s[70:71]\n"
"v_pk_mul_f32 v[122:123], v[122:123], s[70:71]\n"
"v_pk_mul_f32 v[124:125], v[124:125], s[70:71]\n"
"v_pk_mul_f32 v[126:127], v[126:127], s[70:71]\n"
"v_cvt_pk_bf16_f32 v224, v96, v97\n"
"v_cvt_pk_bf16_f32 v225, v98, v99\n"
"ds_read_b64_tr_b16 a[150:151], v243 offset:13312\n"
"ds_read_b64_tr_b16 a[152:153], v242 offset:9728\n"
"buffer_load_dwordx4 v246, s[52:55], s90 offen nt lds\n"
"s_add_u32 m0, m0, 896\n"
"s_nop 0\n"
"buffer_load_dwordx4 v246, s[52:55], s90 offen offset:128 nt lds\n"
"v_mfma_f32_16x16x32_bf16 v[32:35], a[108:111], a[28:31], v[32:35]\n"
"v_cvt_pk_bf16_f32 v226, v100, v101\n"
"v_cvt_pk_bf16_f32 v227, v102, v103\n"
"v_cvt_pk_bf16_f32 v228, v104, v105\n"
"v_cvt_pk_bf16_f32 v229, v106, v107\n"
"v_cvt_pk_bf16_f32 v230, v108, v109\n"
"v_cvt_pk_bf16_f32 v231, v110, v111\n"
"ds_read_b64_tr_b16 a[154:155], v242 offset:13824\n"
"v_mfma_f32_16x16x32_bf16 v[36:39], a[112:115], a[16:19], 0\n"
"v_cvt_pk_bf16_f32 v232, v112, v113\n"
"v_cvt_pk_bf16_f32 v233, v114, v115\n"
"v_cvt_pk_bf16_f32 v234, v116, v117\n"
"v_cvt_pk_bf16_f32 v235, v118, v119\n"
"v_cvt_pk_bf16_f32 v236, v120, v121\n"
"v_cvt_pk_bf16_f32 v237, v122, v123\n"
"ds_read_b64_tr_b16 a[156:157], v243 offset:9728\n"
"v_mfma_f32_16x16x32_bf16 v[36:39], a[116:119], a[20:23], v[36:39]\n"
"v_cvt_pk_bf16_f32 v238, v124, v125\n"
"v_cvt_pk_bf16_f32 v239, v126, v127\n"
"v_accvgpr_write_b32 a48, v224\n"
"v_accvgpr_write_b32 a49, v225\n"
"v_accvgpr_write_b32 a50, v226\n"
"v_accvgpr_write_b32 a51, v227\n"
"ds_read_b64_tr_b16 a[158:159], v243 offset:13824\n"
"s_cmp_eq_u32 s95, 131072\n"
"s_cselect_b32 s68, -131072, s81\n"
"v_mfma_f32_16x16x32_bf16 v[36:39], a[120:123], a[24:27], v[36:39]\n"
"v_accvgpr_write_b32 a52, v228\n"
"v_accvgpr_write_b32 a53, v229\n"
"v_accvgpr_write_b32 a54, v230\n"
"v_accvgpr_write_b32 a55, v231\n"
"v_accvgpr_write_b32 a56, v232\n"
"v_accvgpr_write_b32 a57, v233\n"
"s_add_u32 s95, s95, s68\n"
"v_mfma_f32_16x16x32_bf16 v[36:39], a[124:127], a[28:31], v[36:39]\n"
"v_accvgpr_write_b32 a58, v234\n"
"v_accvgpr_write_b32 a59, v235\n"
"v_accvgpr_write_b32 a60, v236\n"
"v_accvgpr_write_b32 a61, v237\n"
"v_accvgpr_write_b32 a62, v238\n"
"v_accvgpr_write_b32 a63, v239\n"
"v_add_u32_e32 v242, s68, v242\n"
"v_add_u32_e32 v243, s68, v243\n"
"v_mfma_f32_16x16x32_bf16 v[64:67], a[96:99], a[32:35], 0\n"
"v_pk_mul_f32 v[128:129], v[128:129], s[70:71]\n"
"v_pk_mul_f32 v[130:131], v[130:131], s[70:71]\n"
"v_pk_mul_f32 v[132:133], v[132:133], s[70:71]\n"
"v_pk_mul_f32 v[134:135], v[134:135], s[70:71]\n"
"v_pk_mul_f32 v[136:137], v[136:137], s[70:71]\n"
"v_pk_mul_f32 v[138:139], v[138:139], s[70:71]\n"
"v_pk_mul_f32 v[140:141], v[140:141], s[70:71]\n"
"s_add_u32 m0, s91, 13184\n"
"ds_read_b128 a[192:195], v240 offset:0\n"
"v_mfma_f32_16x16x32_bf16 v[64:67], a[100:103], a[36:39], v[64:67]\n"
"v_pk_mul_f32 v[142:143], v[142:143], s[70:71]\n"
"v_pk_mul_f32 v[144:145], v[144:145], s[70:71]\n"
"v_pk_mul_f32 v[146:147], v[146:147], s[70:71]\n"
"v_pk_mul_f32 v[148:149], v[148:149], s[70:71]\n"
"v_pk_mul_f32 v[150:151], v[150:151], s[70:71]\n"
"v_pk_mul_f32 v[152:153], v[152:153], s[70:71]\n"
"ds_read_b128 a[196:199], v241 offset:0\n"
"v_mfma_f32_16x16x32_bf16 v[64:67], a[104:107], a[40:43], v[64:67]\n"
"v_pk_mul_f32 v[154:155], v[154:155], s[70:71]\n"
"v_pk_mul_f32 v[156:157], v[156:157], s[70:71]\n"
"v_pk_mul_f32 v[158:159], v[158:159], s[70:71]\n"
"v_cvt_pk_bf16_f32 v192, v128, v129\n"
"v_cvt_pk_bf16_f32 v193, v130, v131\n"
"v_cvt_pk_bf16_f32 v194, v132, v133\n"
"v_cvt_pk_bf16_f32 v195, v134, v135\n"
"buffer_load_dwordx4 v246, s[52:55], s90 offen offset:2176 nt lds\n"
"s_sub_u32 m0, m0, 896\n"
"s_nop 0\n"
"buffer_load_dwordx4 v246, s[52:55], s90 offen offset:2048 nt lds\n"
"ds_read_b128 a[200:203], v240 offset:1024\n"
"v_mfma_f32_16x16x32_bf16 v[64:67], a[108:111], a[44:47], v[64:67]\n"
"v_cvt_pk_bf16_f32 v196, v136, v137\n"
"v_cvt_pk_bf16_f32 v197, v138, v139\n"
"v_cvt_pk_bf16_f32 v198, v140, v141\n"
"v_cvt_pk_bf16_f32 v199, v142, v143\n"
"v_cvt_pk_bf16_f32 v200, v144, v145\n"
"v_cvt_pk_bf16_f32 v201, v146, v147\n"
"v_mfma_f32_16x16x32_bf16 v[68:71], a[112:115], a[32:35], 0\n"
"v_cvt_pk_bf16_f32 v202, v148, v149\n"
"v_cvt_pk_bf16_f32 v203, v150, v151\n"
"v_cvt_pk_bf16_f32 v204, v152, v153\n"
"v_cvt_pk_bf16_f32 v205, v154, v155\n"
"v_cvt_pk_bf16_f32 v206, v156, v157\n"
"v_cvt_pk_bf16_f32 v207, v158, v159\n"
"v_accvgpr_write_b32 a64, v192\n"
"s_add_u32 s90, s90, 12288\n"
"ds_read_b128 a[204:207], v241 offset:1024\n"
"v_mfma_f32_16x16x32_bf16 v[68:71], a[116:119], a[36:39], v[68:71]\n"
"v_accvgpr_write_b32 a65, v193\n"
"v_accvgpr_write_b32 a66, v194\n"
"v_accvgpr_write_b32 a67, v195\n"
"v_accvgpr_write_b32 a68, v196\n"
"v_accvgpr_write_b32 a69, v197\n"
"v_accvgpr_write_b32 a70, v198\n"
"ds_read_b128 a[208:211], v240 offset:4096\n"
"v_mfma_f32_16x16x32_bf16 v[68:71], a[120:123], a[40:43], v[68:71]\n"
"v_accvgpr_write_b32 a71, v199\n"
"v_accvgpr_write_b32 a72, v200\n"
"v_accvgpr_write_b32 a73, v201\n"
"v_accvgpr_write_b32 a74, v202\n"
"v_accvgpr_write_b32 a75, v203\n"
"v_accvgpr_write_b32 a76, v204\n"
"v_accvgpr_write_b32 a77, v205\n"
"s_and_b32 s90, s90, 0x3ffff\n"
"v_mfma_f32_16x16x32_bf16 v[68:71], a[124:127], a[44:47], v[68:71]\n"
"v_accvgpr_write_b32 a78, v206\n"
"v_accvgpr_write_b32 a79, v207\n"
"v_pk_mul_f32 v[160:161], v[160:161], s[70:71]\n"
"v_pk_mul_f32 v[162:163], v[162:163], s[70:71]\n"
"v_pk_mul_f32 v[164:165], v[164:165], s[70:71]\n"
"v_pk_mul_f32 v[166:167], v[166:167], s[70:71]\n"
"ds_read_b128 a[212:215], v241 offset:4096\n"
"v_mfma_f32_16x16x32_bf16 v[96:99], a[96:99], a[48:51], 0\n"
"v_pk_mul_f32 v[168:169], v[168:169], s[70:71]\n"
"v_pk_mul_f32 v[170:171], v[170:171], s[70:71]\n"
"v_pk_mul_f32 v[172:173], v[172:173], s[70:71]\n"
"v_pk_mul_f32 v[174:175], v[174:175], s[70:71]\n"
"v_pk_mul_f32 v[176:177], v[176:177], s[70:71]\n"
"v_pk_mul_f32 v[178:179], v[178:179], s[70:71]\n"
"v_pk_mul_f32 v[180:181], v[180:181], s[70:71]\n"
"ds_read_b128 a[216:219], v240 offset:5120\n"
"v_mfma_f32_16x16x32_bf16 v[96:99], a[100:103], a[52:55], v[96:99]\n"
"v_pk_mul_f32 v[182:183], v[182:183], s[70:71]\n"
"v_pk_mul_f32 v[184:185], v[184:185], s[70:71]\n"
"v_pk_mul_f32 v[186:187], v[186:187], s[70:71]\n"
"v_pk_mul_f32 v[188:189], v[188:189], s[70:71]\n"
"v_pk_mul_f32 v[190:191], v[190:191], s[70:71]\n"
"v_cvt_pk_bf16_f32 v224, v160, v161\n"
"s_add_u32 s91, s91, 65536\n"
"v_mfma_f32_16x16x32_bf16 v[96:99], a[104:107], a[56:59], v[96:99]\n"
"v_cvt_pk_bf16_f32 v225, v162, v163\n"
"v_cvt_pk_bf16_f32 v226, v164, v165\n"
"v_cvt_pk_bf16_f32 v227, v166, v167\n"
"v_cvt_pk_bf16_f32 v228, v168, v169\n"
"v_cvt_pk_bf16_f32 v229, v170, v171\n"
"v_cvt_pk_bf16_f32 v230, v172, v173\n"
"v_cvt_pk_bf16_f32 v231, v174, v175\n"
"ds_read_b128 a[220:223], v241 offset:5120\n"
"v_mfma_f32_16x16x32_bf16 v[96:99], a[108:111], a[60:63], v[96:99]\n"
"v_cvt_pk_bf16_f32 v232, v176, v177\n"
"v_cvt_pk_bf16_f32 v233, v178, v179\n"
"v_cvt_pk_bf16_f32 v234, v180, v181\n"
"v_cvt_pk_bf16_f32 v235, v182, v183\n"
"v_cvt_pk_bf16_f32 v236, v184, v185\n"
"v_cvt_pk_bf16_f32 v237, v186, v187\n"
"s_cmp_ge_u32 s91, s66\n"
"s_cselect_b32 s80, 163840, 0\n"
"s_cmp_eq_u32 s69, 131072\n"
"s_cselect_b32 s68, -131072, s81\n"
"v_mfma_f32_16x16x32_bf16 v[100:103], a[112:115], a[48:51], 0\n"
"v_cvt_pk_bf16_f32 v238, v188, v189\n"
"v_cvt_pk_bf16_f32 v239, v190, v191\n"
"v_accvgpr_write_b32 a80, v224\n"
"v_accvgpr_write_b32 a81, v225\n"
"v_accvgpr_write_b32 a82, v226\n"
"v_accvgpr_write_b32 a83, v227\n"
"v_accvgpr_write_b32 a84, v228\n"
"v_mfma_f32_16x16x32_bf16 v[100:103], a[116:119], a[52:55], v[100:103]\n"
"v_accvgpr_write_b32 a85, v229\n"
"v_accvgpr_write_b32 a86, v230\n"
"v_accvgpr_write_b32 a87, v231\n"
"v_accvgpr_write_b32 a88, v232\n"
"v_accvgpr_write_b32 a89, v233\n"
"v_accvgpr_write_b32 a90, v234\n"
"s_sub_u32 s91, s91, s80\n"
"s_add_u32 s69, s69, s68\n"
"v_mfma_f32_16x16x32_bf16 v[100:103], a[120:123], a[56:59], v[100:103]\n"
"v_accvgpr_write_b32 a91, v235\n"
"v_accvgpr_write_b32 a92, v236\n"
"v_accvgpr_write_b32 a93, v237\n"
"v_accvgpr_write_b32 a94, v238\n"
"v_accvgpr_write_b32 a95, v239\n"
"v_max3_f32 v208, v0, v1, v2\n"
"v_max3_f32 v208, v208, v3, v4\n"
"v_add_u32_e32 v240, s68, v240\n"
"v_add_u32_e32 v241, s68, v241\n"
"v_mfma_f32_16x16x32_bf16 v[100:103], a[124:127], a[60:63], v[100:103]\n"
"v_max3_f32 v208, v208, v5, v6\n"
"v_max_f32_e32 v208, v208, v7\n"
"v_max3_f32 v209, v32, v33, v34\n"
"v_max3_f32 v209, v209, v35, v36\n"
"v_max3_f32 v209, v209, v37, v38\n"
"v_max_f32_e32 v209, v209, v39\n"
"v_mfma_f32_16x16x32_bf16 v[128:131], a[96:99], a[64:67], 0\n"
"v_mov_b32_e32 v214, 0x3f803f80\n"
"v_accvgpr_write_b32 a184, v214\n"
"ds_read_b64_tr_b16 a[224:225], v242 offset:8192\n"
"ds_read_b64_tr_b16 a[226:227], v242 offset:12288\n"
"v_mfma_f32_16x16x32_bf16 v[128:131], a[100:103], a[68:71], v[128:131]\n"
"v_accvgpr_write_b32 a185, v214\n"
"ds_read_b64_tr_b16 a[228:229], v243 offset:8192\n"
"v_mfma_f32_16x16x32_bf16 v[128:131], a[104:107], a[72:75], v[128:131]\n"
"v_accvgpr_write_b32 a186, v214\n"
"ds_read_b64_tr_b16 a[230:231], v243 offset:12288\n"
"v_mfma_f32_16x16x32_bf16 v[128:131], a[108:111], a[76:79], v[128:131]\n"
"v_accvgpr_write_b32 a187, v214\n"
"ds_read_b64_tr_b16 a[232:233], v242 offset:8704\n"
"v_mfma_f32_16x16x32_bf16 v[132:135], a[112:115], a[64:67], 0\n"
"v_max3_f32 v210, v64, v65, v66\n"
"ds_read_b64_tr_b16 a[234:235], v242 offset:12800\n"
"v_mfma_f32_16x16x32_bf16 v[132:135], a[116:119], a[68:71], v[132:135]\n"
"v_max3_f32 v210, v210, v67, v68\n"
"ds_read_b64_tr_b16 a[236:237], v243 offset:8704\n"
"ds_read_b64_tr_b16 a[238:239], v243 offset:12800\n"
"v_mfma_f32_16x16x32_bf16 v[132:135], a[120:123], a[72:75], v[132:135]\n"
"v_max3_f32 v210, v210, v69, v70\n"
"ds_read_b64_tr_b16 a[240:241], v242 offset:9216\n"
"v_mfma_f32_16x16x32_bf16 v[132:135], a[124:127], a[76:79], v[132:135]\n"
"v_max_f32_e32 v210, v210, v71\n"
"ds_read_b64_tr_b16 a[242:243], v242 offset:13312\n"
"v_mfma_f32_16x16x32_bf16 v[160:163], a[96:99], a[80:83], 0\n"
"v_max3_f32 v211, v96, v97, v98\n"
"ds_read_b64_tr_b16 a[244:245], v243 offset:9216\n"
"v_mfma_f32_16x16x32_bf16 v[160:163], a[100:103], a[84:87], v[160:163]\n"
"v_max3_f32 v211, v211, v99, v100\n"
"ds_read_b64_tr_b16 a[246:247], v243 offset:13312\n"
"v_mfma_f32_16x16x32_bf16 v[160:163], a[104:107], a[88:91], v[160:163]\n"
"v_max3_f32 v211, v211, v101, v102\n"
"ds_read_b64_tr_b16 a[248:249], v242 offset:9728\n"
"ds_read_b64_tr_b16 a[250:251], v242 offset:13824\n"
"v_mfma_f32_16x16x32_bf16 v[160:163], a[108:111], a[92:95], v[160:163]\n"
"v_max_f32_e32 v211, v211, v103\n"
"ds_read_b64_tr_b16 a[252:253], v243 offset:9728\n"
"v_mfma_f32_16x16x32_bf16 v[164:167], a[112:115], a[80:83], 0\n"
"v_max3_f32 v212, v128, v129, v130\n"
"ds_read_b64_tr_b16 a[254:255], v243 offset:13824\n"
"v_mfma_f32_16x16x32_bf16 v[164:167], a[116:119], a[84:87], v[164:167]\n"
"v_max3_f32 v212, v212, v131, v132\n"
"s_cmp_eq_u32 s95, 131072\n"
"s_cselect_b32 s68, -131072, s81\n"
"v_mfma_f32_16x16x32_bf16 v[164:167], a[120:123], a[88:91], v[164:167]\n"
"v_max3_f32 v212, v212, v133, v134\n"
"s_add_u32 s95, s95, s68\n"
"v_mfma_f32_16x16x32_bf16 v[164:167], a[124:127], a[92:95], v[164:167]\n"
"v_max_f32_e32 v212, v212, v135\n"
"v_add_u32_e32 v242, s68, v242\n"
"v_add_u32_e32 v243, s68, v243\n"
"v_max3_f32 v213, v160, v161, v162\n"
"s_nop 3\n"
"v_max3_f32 v213, v213, v163, v164\n"
"v_max3_f32 v213, v213, v165, v166\n"
"v_max_f32_e32 v213, v213, v167\n"
"L_setneg%=:\n"
"v_mov_b32_e32 v192, v208\n"
"v_mov_b32_e32 v193, v209\n"
"v_mov_b32_e32 v194, v210\n"
"v_mov_b32_e32 v195, v211\n"
"v_mov_b32_e32 v196, v212\n"
"v_mov_b32_e32 v197, v213\n"
"v_permlane32_swap_b32_e32 v208, v192\n"
"v_permlane32_swap_b32_e32 v209, v193\n"
"v_permlane32_swap_b32_e32 v210, v194\n"
"v_permlane32_swap_b32_e32 v211, v195\n"
"v_permlane32_swap_b32_e32 v212, v196\n"
"v_permlane32_swap_b32_e32 v213, v197\n"
"v_max_f32_e32 v208, v208, v192\n"
"v_max_f32_e32 v209, v209, v193\n"
"v_max_f32_e32 v210, v210, v194\n"
"v_max_f32_e32 v211, v211, v195\n"
"v_max_f32_e32 v212, v212, v196\n"
"v_max_f32_e32 v213, v213, v197\n"
"v_mov_b32_e32 v192, v208\n"
"v_mov_b32_e32 v193, v209\n"
"v_mov_b32_e32 v194, v210\n"
"v_mov_b32_e32 v195, v211\n"
"v_mov_b32_e32 v196, v212\n"
"v_mov_b32_e32 v197, v213\n"
"v_permlane16_swap_b32_e32 v208, v192\n"
"v_permlane16_swap_b32_e32 v209, v193\n"
"v_permlane16_swap_b32_e32 v210, v194\n"
"v_permlane16_swap_b32_e32 v211, v195\n"
"v_permlane16_swap_b32_e32 v212, v196\n"
"v_permlane16_swap_b32_e32 v213, v197\n"
"v_max_f32_e32 v208, v208, v192\n"
"v_max_f32_e32 v209, v209, v193\n"
"v_max_f32_e32 v210, v210, v194\n"
"v_max_f32_e32 v211, v211, v195\n"
"v_max_f32_e32 v212, v212, v196\n"
"v_max_f32_e32 v213, v213, v197\n"
"v_mul_f32_e32 v216, -1.0, v208\n"
"v_mul_f32_e32 v217, -1.0, v208\n"
"v_mul_f32_e32 v218, -1.0, v208\n"
"v_mul_f32_e32 v219, -1.0, v208\n"
"v_mul_f32_e32 v220, -1.0, v209\n"
"v_mul_f32_e32 v221, -1.0, v209\n"
"v_mul_f32_e32 v222, -1.0, v209\n"
"v_mul_f32_e32 v223, -1.0, v209\n"
"v_mul_f32_e32 v224, -1.0, v210\n"
"v_mul_f32_e32 v225, -1.0, v210\n"
"v_mul_f32_e32 v226, -1.0, v210\n"
"v_mul_f32_e32 v227, -1.0, v210\n"
"v_mul_f32_e32 v228, -1.0, v211\n"
"v_mul_f32_e32 v229, -1.0, v211\n"
"v_mul_f32_e32 v230, -1.0, v211\n"
"v_mul_f32_e32 v231, -1.0, v211\n"
"v_mul_f32_e32 v232, -1.0, v212\n"
"v_mul_f32_e32 v233, -1.0, v212\n"
"v_mul_f32_e32 v234, -1.0, v212\n"
"v_mul_f32_e32 v235, -1.0, v212\n"
"v_mul_f32_e32 v236, -1.0, v213\n"
"v_mul_f32_e32 v237, -1.0, v213\n"
"v_mul_f32_e32 v238, -1.0, v213\n"
"v_mul_f32_e32 v239, -1.0, v213\n"
"s_mov_b32 s63, 13\n"
"s_lshl_b32 s77, s60, 8\n"
"s_or_b32 s77, s77, %15\n"
"s_cmp_eq_u32 s77, 255\n"
"s_cselect_b32 s77, 11, 99\n"
"s_memrealtime s[74:75]\n"
"s_waitcnt lgkmcnt(0)\n"
"s_add_u32 s79, s74, 120\n"
"s_lshl_b32 s79, s79, 5\n"
"s_mov_b32 s97, 2602\n"
"s_mul_i32 s80, %15, 37\n"
"s_and_b32 s80, s80, 127\n"
"s_mul_i32 s80, s80, 122\n"
"s_lshr_b32 s80, s80, 2\n"
"s_add_u32 s79, s79, s80\n"
"s_mul_i32 s80, s80, 25\n"
"s_lshr_b32 s80, s80, 10\n"
"s_sub_u32 s97, s97, s80\n"
"s_waitcnt vmcnt(16) lgkmcnt(0)\n"
"s_barrier\n"
"v_mfma_f32_16x16x32_bf16 v[192:195], a[96:99], a[0:3], v[216:219]\n"
"v_mfma_f32_16x16x32_bf16 v[192:195], a[100:103], a[4:7], v[192:195]\n"
"v_mfma_f32_16x16x32_bf16 v[192:195], a[104:107], a[8:11], v[192:195]\n"
"v_mfma_f32_16x16x32_bf16 v[192:195], a[108:111], a[12:15], v[192:195]\n"
"v_mfma_f32_16x16x32_bf16 v[196:199], a[112:115], a[0:3], v[216:219]\n"
"v_mfma_f32_16x16x32_bf16 v[196:199], a[116:119], a[4:7], v[196:199]\n"
"v_mfma_f32_16x16x32_bf16 v[196:199], a[120:123], a[8:11], v[196:199]\n"
"v_mfma_f32_16x16x32_bf16 v[196:199], a[124:127], a[12:15], v[196:199]\n"
"v_mfma_f32_16x16x32_bf16 v[200:203], a[192:195], a[0:3], v[216:219]\n"
"v_mfma_f32_16x16x32_bf16 v[200:203], a[196:199], a[4:7], v[200:203]\n"
"s_nop 1\n"
"v_exp_f32_e32 v192, v192\n"
"v_mfma_f32_16x16x32_bf16 v[200:203], a[200:203], a[8:11], v[200:203]\n"
"v_exp_f32_e32 v193, v193\n"
"v_mfma_f32_16x16x32_bf16 v[200:203], a[204:207], a[12:15], v[200:203]\n"
"v_cvt_pk_bf16_f32 v208, v192, v193\n"
"v_mfma_f32_16x16x32_bf16 v[204:207], a[208:211], a[0:3], v[216:219]\n"
"v_exp_f32_e32 v194, v194\n"
"v_mfma_f32_16x16x32_bf16 v[204:207], a[212:215], a[4:7], v[204:207]\n"
"v_exp_f32_e32 v195, v195\n"
"v_mfma_f32_16x16x32_bf16 v[204:207], a[216:219], a[8:11], v[204:207]\n"
"v_cvt_pk_bf16_f32 v209, v194, v195\n"
"v_mfma_f32_16x16x32_bf16 v[204:207], a[220:223], a[12:15], v[204:207]\n"
"v_exp_f32_e32 v196, v196\n"
"v_exp_f32_e32 v197, v197\n"
"s_nop 0\n"
"v_cvt_pk_bf16_f32 v210, v196, v197\n"
"v_exp_f32_e32 v198, v198\n"
"v_exp_f32_e32 v199, v199\n"
"s_nop 0\n"
"v_cvt_pk_bf16_f32 v211, v198, v199\n"
"v_exp_f32_e32 v200, v200\n"
"v_exp_f32_e32 v201, v201\n"
"s_nop 0\n"
"v_cvt_pk_bf16_f32 v212, v200, v201\n"
"v_exp_f32_e32 v202, v202\n"
"v_exp_f32_e32 v203, v203\n"
"s_nop 0\n"
"v_cvt_pk_bf16_f32 v213, v202, v203\n"
"v_mfma_f32_16x16x32_bf16 v[192:195], a[96:99], a[16:19], v[220:223]\n"
"v_exp_f32_e32 v204, v204\n"
"v_mfma_f32_16x16x32_bf16 v[192:195], a[100:103], a[20:23], v[192:195]\n"
"v_exp_f32_e32 v205, v205\n"
"v_mfma_f32_16x16x32_bf16 v[192:195], a[104:107], a[24:27], v[192:195]\n"
"v_cvt_pk_bf16_f32 v214, v204, v205\n"
"v_mfma_f32_16x16x32_bf16 v[192:195], a[108:111], a[28:31], v[192:195]\n"
"v_exp_f32_e32 v206, v206\n"
"v_mfma_f32_16x16x32_bf16 v[196:199], a[112:115], a[16:19], v[220:223]\n"
"v_exp_f32_e32 v207, v207\n"
"v_mfma_f32_16x16x32_bf16 v[196:199], a[116:119], a[20:23], v[196:199]\n"
"v_cvt_pk_bf16_f32 v215, v206, v207\n"
"v_mfma_f32_16x16x32_bf16 v[196:199], a[120:123], a[24:27], v[196:199]\n"
"v_mfma_f32_16x16x32_bf16 v[196:199], a[124:127], a[28:31], v[196:199]\n"
"v_mfma_f32_16x16x32_bf16 v[200:203], a[192:195], a[16:19], v[220:223]\n"
"v_mfma_f32_16x16x32_bf16 v[200:203], a[196:199], a[20:23], v[200:203]\n"
"v_exp_f32_e32 v192, v192\n"
"v_mfma_f32_16x16x32_bf16 v[200:203], a[200:203], a[24:27], v[200:203]\n"
"v_exp_f32_e32 v193, v193\n"
"v_mfma_f32_16x16x32_bf16 v[200:203], a[204:207], a[28:31], v[200:203]\n"
"v_cvt_pk_bf16_f32 v248, v192, v193\n"
"v_mfma_f32_16x16x32_bf16 v[204:207], a[208:211], a[16:19], v[220:223]\n"
"v_exp_f32_e32 v194, v194\n"
"v_mfma_f32_16x16x32_bf16 v[204:207], a[212:215], a[20:23], v[204:207]\n"
"v_exp_f32_e32 v195, v195\n"
"v_mfma_f32_16x16x32_bf16 v[204:207], a[216:219], a[24:27], v[204:207]\n"
"v_cvt_pk_bf16_f32 v249, v194, v195\n"
"v_mfma_f32_16x16x32_bf16 v[204:207], a[220:223], a[28:31], v[204:207]\n"
"v_mfma_f32_16x16x32_bf16 v[0:3], a[128:131], v[208:211], 0\n"
"v_exp_f32_e32 v196, v196\n"
"v_mfma_f32_16x16x32_bf16 v[0:3], a[224:227], v[212:215], v[0:3]\n"
"v_exp_f32_e32 v197, v197\n"
"v_mfma_f32_16x16x32_bf16 v[4:7], a[132:135], v[208:211], 0\n"
"v_cvt_pk_bf16_f32 v250, v196, v197\n"
"v_mov_b32_e32 v160, 0\n"
"v_mfma_f32_16x16x32_bf16 v[4:7], a[228:231], v[212:215], v[4:7]\n"
"v_exp_f32_e32 v198, v198\n"
"v_mfma_f32_16x16x32_bf16 v[8:11], a[136:139], v[208:211], 0\n"
"v_exp_f32_e32 v199, v199\n"
"v_mfma_f32_16x16x32_bf16 v[8:11], a[232:235], v[212:215], v[8:11]\n"
"v_cvt_pk_bf16_f32 v251, v198, v199\n"
"v_mov_b32_e32 v161, 0\n"
"v_mfma_f32_16x16x32_bf16 v[12:15], a[140:143], v[208:211], 0\n"
"v_mov_b32_e32 v162, 0\n"
"v_mfma_f32_16x16x32_bf16 v[12:15], a[236:239], v[212:215], v[12:15]\n"
"v_mov_b32_e32 v163, 0\n"
"v_mfma_f32_16x16x32_bf16 v[16:19], a[144:147], v[208:211], 0\n"
"v_mfma_f32_16x16x32_bf16 v[16:19], a[240:243], v[212:215], v[16:19]\n"
"v_exp_f32_e32 v200, v200\n"
"v_mfma_f32_16x16x32_bf16 v[20:23], a[148:151], v[208:211], 0\n"
"v_exp_f32_e32 v201, v201\n"
"v_mfma_f32_16x16x32_bf16 v[20:23], a[244:247], v[212:215], v[20:23]\n"
"v_cvt_pk_bf16_f32 v252, v200, v201\n"
"v_mov_b32_e32 v164, 0\n"
"v_mfma_f32_16x16x32_bf16 v[24:27], a[152:155], v[208:211], 0\n"
"v_exp_f32_e32 v202, v202\n"
"v_mfma_f32_16x16x32_bf16 v[24:27], a[248:251], v[212:215], v[24:27]\n"
"v_exp_f32_e32 v203, v203\n"
"v_mfma_f32_16x16x32_bf16 v[28:31], a[156:159], v[208:211], 0\n"
"v_cvt_pk_bf16_f32 v253, v202, v203\n"
"v_mov_b32_e32 v165, 0\n"
"v_mfma_f32_16x16x32_bf16 v[28:31], a[252:255], v[212:215], v[28:31]\n"
"v_mov_b32_e32 v166, 0\n"
"v_mfma_f32_16x16x32_bf16 a[160:163], a[184:187], v[208:211], 0\n"
"v_mov_b32_e32 v167, 0\n"
"v_mfma_f32_16x16x32_bf16 a[160:163], a[184:187], v[212:215], a[160:163]\n"
"v_mfma_f32_16x16x32_bf16 v[192:195], a[96:99], a[32:35], v[224:227]\n"
"v_exp_f32_e32 v204, v204\n"
"v_mov_b32_e32 v168, 0\n"
"v_mfma_f32_16x16x32_bf16 v[192:195], a[100:103], a[36:39], v[192:195]\n"
"v_exp_f32_e32 v205, v205\n"
"v_mfma_f32_16x16x32_bf16 v[192:195], a[104:107], a[40:43], v[192:195]\n"
"v_cvt_pk_bf16_f32 v254, v204, v205\n"
"s_mov_b32 m0, s67\n"
"v_mfma_f32_16x16x32_bf16 v[192:195], a[108:111], a[44:47], v[192:195]\n"
"v_exp_f32_e32 v206, v206\n"
"v_mfma_f32_16x16x32_bf16 v[196:199], a[112:115], a[32:35], v[224:227]\n"
"v_exp_f32_e32 v207, v207\n"
"v_mfma_f32_16x16x32_bf16 v[196:199], a[116:119], a[36:39], v[196:199]\n"
"v_cvt_pk_bf16_f32 v255, v206, v207\n"
"v_mov_b32_e32 v169, 0\n"
"v_mfma_f32_16x16x32_bf16 v[196:199], a[120:123], a[40:43], v[196:199]\n"
"buffer_load_dwordx4 v244, s[48:51], s64 offen nt lds\n"
"s_add_u32 m0, m0, 896\n"
"s_nop 0\n"
"buffer_load_dwordx4 v244, s[48:51], s64 offen offset:128 nt lds\n"
"v_mfma_f32_16x16x32_bf16 v[196:199], a[124:127], a[44:47], v[196:199]\n"
"v_mov_b32_e32 v170, 0\n"
"v_mov_b32_e32 v171, 0\n"
"v_mfma_f32_16x16x32_bf16 v[200:203], a[192:195], a[32:35], v[224:227]\n"
"v_mfma_f32_16x16x32_bf16 v[200:203], a[196:199], a[36:39], v[200:203]\n"
"v_exp_f32_e32 v192, v192\n"
"v_mfma_f32_16x16x32_bf16 v[200:203], a[200:203], a[40:43], v[200:203]\n"
"v_exp_f32_e32 v193, v193\n"
"v_mfma_f32_16x16x32_bf16 v[200:203], a[204:207], a[44:47], v[200:203]\n"
"v_cvt_pk_bf16_f32 v208, v192, v193\n"
"buffer_load_dwordx4 v245, s[48:51], s64 offen offset:2176 nt lds\n"
"s_sub_u32 m0, m0, 896\n"
"s_nop 0\n"
"buffer_load_dwordx4 v245, s[48:51], s64 offen offset:2048 nt lds\n"
"v_mfma_f32_16x16x32_bf16 v[204:207], a[208:211], a[32:35], v[224:227]\n"
"v_exp_f32_e32 v194, v194\n"
"v_mfma_f32_16x16x32_bf16 v[204:207], a[212:215], a[36:39], v[204:207]\n"
"v_exp_f32_e32 v195, v195\n"
"v_mfma_f32_16x16x32_bf16 v[204:207], a[216:219], a[40:43], v[204:207]\n"
"v_cvt_pk_bf16_f32 v209, v194, v195\n"
"v_mov_b32_e32 v172, 0\n"
"v_mfma_f32_16x16x32_bf16 v[204:207], a[220:223], a[44:47], v[204:207]\n"
"v_mov_b32_e32 v173, 0\n"
"v_mov_b32_e32 v174, 0\n"
"v_mfma_f32_16x16x32_bf16 v[32:35], a[128:131], v[248:251], 0\n"
"v_exp_f32_e32 v196, v196\n"
"v_mfma_f32_16x16x32_bf16 v[32:35], a[224:227], v[252:255], v[32:35]\n"
"v_exp_f32_e32 v197, v197\n"
"v_mfma_f32_16x16x32_bf16 v[36:39], a[132:135], v[248:251], 0\n"
"v_cvt_pk_bf16_f32 v210, v196, v197\n"
"s_add_u32 m0, m0, 4096\n"
"v_mfma_f32_16x16x32_bf16 v[36:39], a[228:231], v[252:255], v[36:39]\n"
"v_exp_f32_e32 v198, v198\n"
"v_mfma_f32_16x16x32_bf16 v[40:43], a[136:139], v[248:251], 0\n"
"v_exp_f32_e32 v199, v199\n"
"v_mfma_f32_16x16x32_bf16 v[40:43], a[232:235], v[252:255], v[40:43]\n"
"v_cvt_pk_bf16_f32 v211, v198, v199\n"
"v_mov_b32_e32 v175, 0\n"
"v_mfma_f32_16x16x32_bf16 v[44:47], a[140:143], v[248:251], 0\n"
"v_mov_b32_e32 v176, 0\n"
"s_add_u32 s64, s64, 4096\n"
"v_mfma_f32_16x16x32_bf16 v[44:47], a[236:239], v[252:255], v[44:47]\n"
"v_mov_b32_e32 v177, 0\n"
"v_mfma_f32_16x16x32_bf16 v[48:51], a[144:147], v[248:251], 0\n"
"v_mov_b32_e32 v178, 0\n"
"v_mfma_f32_16x16x32_bf16 v[48:51], a[240:243], v[252:255], v[48:51]\n"
"v_exp_f32_e32 v200, v200\n"
"v_mfma_f32_16x16x32_bf16 v[52:55], a[148:151], v[248:251], 0\n"
"v_exp_f32_e32 v201, v201\n"
"v_mfma_f32_16x16x32_bf16 v[52:55], a[244:247], v[252:255], v[52:55]\n"
"v_cvt_pk_bf16_f32 v212, v200, v201\n"
"buffer_load_dwordx4 v244, s[48:51], s64 offen nt lds\n"
"s_add_u32 m0, m0, 896\n"
"s_nop 0\n"
"buffer_load_dwordx4 v244, s[48:51], s64 offen offset:128 nt lds\n"
"v_mfma_f32_16x16x32_bf16 v[56:59], a[152:155], v[248:251], 0\n"
"v_exp_f32_e32 v202, v202\n"
"v_mfma_f32_16x16x32_bf16 v[56:59], a[248:251], v[252:255], v[56:59]\n"
"v_exp_f32_e32 v203, v203\n"
"v_mfma_f32_16x16x32_bf16 v[60:63], a[156:159], v[248:251], 0\n"
"v_cvt_pk_bf16_f32 v213, v202, v203\n"
"v_mov_b32_e32 v179, 0\n"
"v_mfma_f32_16x16x32_bf16 v[60:63], a[252:255], v[252:255], v[60:63]\n"
"v_mov_b32_e32 v180, 0\n"
"v_mov_b32_e32 v181, 0\n"
"v_mfma_f32_16x16x32_bf16 a[164:167], a[184:187], v[248:251], 0\n"
"buffer_load_dwordx4 v245, s[48:51], s64 offen offset:2176 nt lds\n"
"s_sub_u32 m0, m0, 896\n"
"s_nop 0\n"
"buffer_load_dwordx4 v245, s[48:51], s64 offen offset:2048 nt lds\n"
"v_mfma_f32_16x16x32_bf16 a[164:167], a[184:187], v[252:255], a[164:167]\n"
"v_mov_b32_e32 v182, 0\n"
"v_mfma_f32_16x16x32_bf16 v[192:195], a[96:99], a[48:51], v[228:231]\n"
"v_exp_f32_e32 v204, v204\n"
"v_mfma_f32_16x16x32_bf16 v[192:195], a[100:103], a[52:55], v[192:195]\n"
"v_exp_f32_e32 v205, v205\n"
"v_mfma_f32_16x16x32_bf16 v[192:195], a[104:107], a[56:59], v[192:195]\n"
"v_cvt_pk_bf16_f32 v214, v204, v205\n"
"v_mov_b32_e32 v183, 0\n"
"v_mfma_f32_16x16x32_bf16 v[192:195], a[108:111], a[60:63], v[192:195]\n"
"v_exp_f32_e32 v206, v206\n"
"v_mfma_f32_16x16x32_bf16 v[196:199], a[112:115], a[48:51], v[228:231]\n"
"v_exp_f32_e32 v207, v207\n"
"v_mfma_f32_16x16x32_bf16 v[196:199], a[116:119], a[52:55], v[196:199]\n"
"v_cvt_pk_bf16_f32 v215, v206, v207\n"
"s_add_u32 s64, s64, 12288\n"
"v_mfma_f32_16x16x32_bf16 v[196:199], a[120:123], a[56:59], v[196:199]\n"
"v_mov_b32_e32 v184, 0\n"
"v_mov_b32_e32 v185, 0\n"
"v_mfma_f32_16x16x32_bf16 v[196:199], a[124:127], a[60:63], v[196:199]\n"
"v_mfma_f32_16x16x32_bf16 v[200:203], a[192:195], a[48:51], v[228:231]\n"
"s_and_b32 s64, s64, 0x3ffff\n"
"v_mov_b32_e32 v186, 0\n"
"v_mfma_f32_16x16x32_bf16 v[200:203], a[196:199], a[52:55], v[200:203]\n"
"v_exp_f32_e32 v192, v192\n"
"v_mfma_f32_16x16x32_bf16 v[200:203], a[200:203], a[56:59], v[200:203]\n"
"v_exp_f32_e32 v193, v193\n"
"v_mfma_f32_16x16x32_bf16 v[200:203], a[204:207], a[60:63], v[200:203]\n"
"v_cvt_pk_bf16_f32 v248, v192, v193\n"
"v_mov_b32_e32 v187, 0\n"
"v_mfma_f32_16x16x32_bf16 v[204:207], a[208:211], a[48:51], v[228:231]\n"
"v_exp_f32_e32 v194, v194\n"
"v_mfma_f32_16x16x32_bf16 v[204:207], a[212:215], a[52:55], v[204:207]\n"
"v_exp_f32_e32 v195, v195\n"
"v_mfma_f32_16x16x32_bf16 v[204:207], a[216:219], a[56:59], v[204:207]\n"
"v_cvt_pk_bf16_f32 v249, v194, v195\n"
"v_mov_b32_e32 v188, 0\n"
"v_mfma_f32_16x16x32_bf16 v[204:207], a[220:223], a[60:63], v[204:207]\n"
"s_add_u32 s67, s67, 65536\n"
"v_mov_b32_e32 v189, 0\n"
"v_mfma_f32_16x16x32_bf16 v[64:67], a[128:131], v[208:211], 0\n"
"v_exp_f32_e32 v196, v196\n"
"v_mfma_f32_16x16x32_bf16 v[64:67], a[224:227], v[212:215], v[64:67]\n"
"v_exp_f32_e32 v197, v197\n"
"v_mfma_f32_16x16x32_bf16 v[68:71], a[132:135], v[208:211], 0\n"
"v_cvt_pk_bf16_f32 v250, v196, v197\n"
"v_mov_b32_e32 v190, 0\n"
"v_mfma_f32_16x16x32_bf16 v[68:71], a[228:231], v[212:215], v[68:71]\n"
"v_exp_f32_e32 v198, v198\n"
"v_mfma_f32_16x16x32_bf16 v[72:75], a[136:139], v[208:211], 0\n"
"v_exp_f32_e32 v199, v199\n"
"v_mfma_f32_16x16x32_bf16 v[72:75], a[232:235], v[212:215], v[72:75]\n"
"v_cvt_pk_bf16_f32 v251, v198, v199\n"
"v_mov_b32_e32 v191, 0\n"
"v_mfma_f32_16x16x32_bf16 v[76:79], a[140:143], v[208:211], 0\n"
"s_cmp_ge_u32 s67, s66\n"
"s_cselect_b32 s80, 163840, 0\n"
"v_mfma_f32_16x16x32_bf16 v[76:79], a[236:239], v[212:215], v[76:79]\n"
"v_accvgpr_write_b32 a180, 0\n"
"v_mfma_f32_16x16x32_bf16 v[80:83], a[144:147], v[208:211], 0\n"
"s_sub_u32 s67, s67, s80\n"
"v_mfma_f32_16x16x32_bf16 v[80:83], a[240:243], v[212:215], v[80:83]\n"
"v_exp_f32_e32 v200, v200\n"
"v_mfma_f32_16x16x32_bf16 v[84:87], a[148:151], v[208:211], 0\n"
"v_exp_f32_e32 v201, v201\n"
"v_mfma_f32_16x16x32_bf16 v[84:87], a[244:247], v[212:215], v[84:87]\n"
"v_cvt_pk_bf16_f32 v252, v200, v201\n"
"v_accvgpr_write_b32 a181, 0\n"
"v_mfma_f32_16x16x32_bf16 v[88:91], a[152:155], v[208:211], 0\n"
"v_exp_f32_e32 v202, v202\n"
"v_mfma_f32_16x16x32_bf16 v[88:91], a[248:251], v[212:215], v[88:91]\n"
"v_exp_f32_e32 v203, v203\n"
"v_mfma_f32_16x16x32_bf16 v[92:95], a[156:159], v[208:211], 0\n"
"v_cvt_pk_bf16_f32 v253, v202, v203\n"
"s_memrealtime s[74:75]\n"
"v_mfma_f32_16x16x32_bf16 v[92:95], a[252:255], v[212:215], v[92:95]\n"
"v_accvgpr_write_b32 a182, 0\n"
"v_accvgpr_write_b32 a183, 0\n"
"v_mfma_f32_16x16x32_bf16 a[168:171], a[184:187], v[208:211], 0\n"
"s_waitcnt lgkmcnt(0)\n"
"v_mfma_f32_16x16x32_bf16 a[168:171], a[184:187], v[212:215], a[168:171]\n"
"s_waitcnt vmcnt(16)\n"
"s_lshl_b32 s74, s74, 5\n"
"s_sub_u32 s74, s79, s74\n"
"s_min_i32 s75, s74, -1920\n"
"s_add_u32 s75, s75, 1920\n"
"s_sub_u32 s79, s79, s75\n"
"s_add_u32 s79, s79, s97\n"
"s_cmp_lt_i32 s74, 128\n"
"s_cbranch_scc1 L_pe1%=\n"
"L_pl1%=:\n"
"s_sleep 1\n"
"s_sub_u32 s74, s74, 120\n"
"s_cmp_gt_i32 s74, 100\n"
"s_cbranch_scc1 L_pl1%=\n"
"L_pe1%=:\n"
"s_barrier\n"
"v_mfma_f32_16x16x32_bf16 v[192:195], a[96:99], a[64:67], v[232:235]\n"
"v_exp_f32_e32 v204, v204\n"
"v_mfma_f32_16x16x32_bf16 v[192:195], a[100:103], a[68:71], v[192:195]\n"
"v_exp_f32_e32 v205, v205\n"
"v_mfma_f32_16x16x32_bf16 v[192:195], a[104:107], a[72:75], v[192:195]\n"
"v_cvt_pk_bf16_f32 v254, v204, v205\n"
"s_add_u32 m0, s91, 8192\n"
"v_mfma_f32_16x16x32_bf16 v[192:195], a[108:111], a[76:79], v[192:195]\n"
"v_exp_f32_e32 v206, v206\n"
"v_mfma_f32_16x16x32_bf16 v[196:199], a[112:115], a[64:67], v[232:235]\n"
"v_exp_f32_e32 v207, v207\n"
"v_mfma_f32_16x16x32_bf16 v[196:199], a[116:119], a[68:71], v[196:199]\n"
"v_cvt_pk_bf16_f32 v255, v206, v207\n"
"buffer_load_dwordx4 v246, s[52:55], s90 offen nt lds\n"
"s_add_u32 m0, m0, 896\n"
"s_nop 0\n"
"buffer_load_dwordx4 v246, s[52:55], s90 offen offset:128 nt lds\n"
"v_mfma_f32_16x16x32_bf16 v[196:199], a[120:123], a[72:75], v[196:199]\n"
"buffer_load_dwordx4 v246, s[52:55], s90 offen offset:2176 nt lds\n"
"s_sub_u32 m0, m0, 896\n"
"s_nop 0\n"
"buffer_load_dwordx4 v246, s[52:55], s90 offen offset:2048 nt lds\n"
"v_mfma_f32_16x16x32_bf16 v[196:199], a[124:127], a[76:79], v[196:199]\n"
"v_mfma_f32_16x16x32_bf16 v[200:203], a[192:195], a[64:67], v[232:235]\n"
"s_add_u32 m0, m0, 4096\n"
"v_mfma_f32_16x16x32_bf16 v[200:203], a[196:199], a[68:71], v[200:203]\n"
"v_exp_f32_e32 v192, v192\n"
"v_mfma_f32_16x16x32_bf16 v[200:203], a[200:203], a[72:75], v[200:203]\n"
"v_exp_f32_e32 v193, v193\n"
"v_mfma_f32_16x16x32_bf16 v[200:203], a[204:207], a[76:79], v[200:203]\n"
"v_cvt_pk_bf16_f32 v208, v192, v193\n"
"s_add_u32 s90, s90, 4096\n"
"v_mfma_f32_16x16x32_bf16 v[204:207], a[208:211], a[64:67], v[232:235]\n"
"v_exp_f32_e32 v194, v194\n"
"v_mfma_f32_16x16x32_bf16 v[204:207], a[212:215], a[68:71], v[204:207]\n"
"v_exp_f32_e32 v195, v195\n"
"v_mfma_f32_16x16x32_bf16 v[204:207], a[216:219], a[72:75], v[204:207]\n"
"v_cvt_pk_bf16_f32 v209, v194, v195\n"
"v_mfma_f32_16x16x32_bf16 v[204:207], a[220:223], a[76:79], v[204:207]\n"
"buffer_load_dwordx4 v246, s[52:55], s90 offen nt lds\n"
"s_add_u32 m0, m0, 896\n"
"s_nop 0\n"
"buffer_load_dwordx4 v246, s[52:55], s90 offen offset:128 nt lds\n"
"v_mfma_f32_16x16x32_bf16 v[96:99], a[128:131], v[248:251], 0\n"
"v_exp_f32_e32 v196, v196\n"
"v_mfma_f32_16x16x32_bf16 v[96:99], a[224:227], v[252:255], v[96:99]\n"
"v_exp_f32_e32 v197, v197\n"
"v_mfma_f32_16x16x32_bf16 v[100:103], a[132:135], v[248:251], 0\n"
"v_cvt_pk_bf16_f32 v210, v196, v197\n"
"buffer_load_dwordx4 v246, s[52:55], s90 offen offset:2176 nt lds\n"
"s_sub_u32 m0, m0, 896\n"
"s_nop 0\n"
"buffer_load_dwordx4 v246, s[52:55], s90 offen offset:2048 nt lds\n"
"v_mfma_f32_16x16x32_bf16 v[100:103], a[228:231], v[252:255], v[100:103]\n"
"v_exp_f32_e32 v198, v198\n"
"v_mfma_f32_16x16x32_bf16 v[104:107], a[136:139], v[248:251], 0\n"
"v_exp_f32_e32 v199, v199\n"
"v_mfma_f32_16x16x32_bf16 v[104:107], a[232:235], v[252:255], v[104:107]\n"
"v_cvt_pk_bf16_f32 v211, v198, v199\n"
"s_add_u32 s90, s90, 12288\n"
"v_mfma_f32_16x16x32_bf16 v[108:111], a[140:143], v[248:251], 0\n"
"v_mfma_f32_16x16x32_bf16 v[108:111], a[236:239], v[252:255], v[108:111]\n"
"s_and_b32 s90, s90, 0x3ffff\n"
"v_mfma_f32_16x16x32_bf16 v[112:115], a[144:147], v[248:251], 0\n"
"v_mfma_f32_16x16x32_bf16 v[112:115], a[240:243], v[252:255], v[112:115]\n"
"v_exp_f32_e32 v200, v200\n"
"v_mfma_f32_16x16x32_bf16 v[116:119], a[148:151], v[248:251], 0\n"
"v_exp_f32_e32 v201, v201\n"
"v_mfma_f32_16x16x32_bf16 v[116:119], a[244:247], v[252:255], v[116:119]\n"
"v_cvt_pk_bf16_f32 v212, v200, v201\n"
"s_add_u32 s91, s91, 65536\n"
"v_mfma_f32_16x16x32_bf16 v[120:123], a[152:155], v[248:251], 0\n"
"v_exp_f32_e32 v202, v202\n"
"v_mfma_f32_16x16x32_bf16 v[120:123], a[248:251], v[252:255], v[120:123]\n"
"v_exp_f32_e32 v203, v203\n"
"v_mfma_f32_16x16x32_bf16 v[124:127], a[156:159], v[248:251], 0\n"
"v_cvt_pk_bf16_f32 v213, v202, v203\n"
"v_mfma_f32_16x16x32_bf16 v[124:127], a[252:255], v[252:255], v[124:127]\n"
"s_cmp_ge_u32 s91, s66\n"
"s_cselect_b32 s80, 163840, 0\n"
"v_mfma_f32_16x16x32_bf16 a[172:175], a[184:187], v[248:251], 0\n"
"s_sub_u32 s91, s91, s80\n"
"v_mfma_f32_16x16x32_bf16 a[172:175], a[184:187], v[252:255], a[172:175]\n"
"v_mfma_f32_16x16x32_bf16 v[192:195], a[96:99], a[80:83], v[236:239]\n"
"v_exp_f32_e32 v204, v204\n"
"v_mfma_f32_16x16x32_bf16 v[192:195], a[100:103], a[84:87], v[192:195]\n"
"v_exp_f32_e32 v205, v205\n"
"v_mfma_f32_16x16x32_bf16 v[192:195], a[104:107], a[88:91], v[192:195]\n"
"v_cvt_pk_bf16_f32 v214, v204, v205\n"
"ds_read_b128 a[96:99], v240 offset:0\n"
"v_mfma_f32_16x16x32_bf16 v[192:195], a[108:111], a[92:95], v[192:195]\n"
"v_exp_f32_e32 v206, v206\n"
"v_mfma_f32_16x16x32_bf16 v[196:199], a[112:115], a[80:83], v[236:239]\n"
"v_exp_f32_e32 v207, v207\n"
"v_mfma_f32_16x16x32_bf16 v[196:199], a[116:119], a[84:87], v[196:199]\n"
"v_cvt_pk_bf16_f32 v215, v206, v207\n"
"ds_read_b128 a[100:103], v241 offset:0\n"
"v_mfma_f32_16x16x32_bf16 v[196:199], a[120:123], a[88:91], v[196:199]\n"
"ds_read_b128 a[104:107], v240 offset:1024\n"
"ds_read_b128 a[108:111], v241 offset:1024\n"
"v_mfma_f32_16x16x32_bf16 v[196:199], a[124:127], a[92:95], v[196:199]\n"
"ds_read_b128 a[112:115], v240 offset:4096\n"
"ds_read_b128 a[116:119], v241 offset:4096\n"
"v_mfma_f32_16x16x32_bf16 v[200:203], a[192:195], a[80:83], v[236:239]\n"
"ds_read_b128 a[120:123], v240 offset:5120\n"
"v_mfma_f32_16x16x32_bf16 v[200:203], a[196:199], a[84:87], v[200:203]\n"
"v_exp_f32_e32 v192, v192\n"
"v_mfma_f32_16x16x32_bf16 v[200:203], a[200:203], a[88:91], v[200:203]\n"
"v_exp_f32_e32 v193, v193\n"
"v_mfma_f32_16x16x32_bf16 v[200:203], a[204:207], a[92:95], v[200:203]\n"
"v_cvt_pk_bf16_f32 v248, v192, v193\n"
"ds_read_b128 a[124:127], v241 offset:5120\n"
"v_mfma_f32_16x16x32_bf16 v[204:207], a[208:211], a[80:83], v[236:239]\n"
"v_exp_f32_e32 v194, v194\n"
"v_mfma_f32_16x16x32_bf16 v[204:207], a[212:215], a[84:87], v[204:207]\n"
"v_exp_f32_e32 v195, v195\n"
"v_mfma_f32_16x16x32_bf16 v[204:207], a[216:219], a[88:91], v[204:207]\n"
"v_cvt_pk_bf16_f32 v249, v194, v195\n"
"s_cmp_eq_u32 s69, 131072\n"
"s_cselect_b32 s68, -131072, s81\n"
"v_mfma_f32_16x16x32_bf16 v[204:207], a[220:223], a[92:95], v[204:207]\n"
"s_add_u32 s69, s69, s68\n"
"v_add_u32_e32 v240, s68, v240\n"
"v_add_u32_e32 v241, s68, v241\n"
"v_mfma_f32_16x16x32_bf16 v[128:131], a[128:131], v[208:211], 0\n"
"v_exp_f32_e32 v196, v196\n"
"v_mfma_f32_16x16x32_bf16 v[128:131], a[224:227], v[212:215], v[128:131]\n"
"v_exp_f32_e32 v197, v197\n"
"v_mfma_f32_16x16x32_bf16 v[132:135], a[132:135], v[208:211], 0\n"
"v_cvt_pk_bf16_f32 v250, v196, v197\n"
"ds_read_b128 a[192:195], v240 offset:0\n"
"v_mfma_f32_16x16x32_bf16 v[132:135], a[228:231], v[212:215], v[132:135]\n"
"v_exp_f32_e32 v198, v198\n"
"v_mfma_f32_16x16x32_bf16 v[136:139], a[136:139], v[208:211], 0\n"
"v_exp_f32_e32 v199, v199\n"
"v_mfma_f32_16x16x32_bf16 v[136:139], a[232:235], v[212:215], v[136:139]\n"
"v_cvt_pk_bf16_f32 v251, v198, v199\n"
"ds_read_b128 a[196:199], v241 offset:0\n"
"v_mfma_f32_16x16x32_bf16 v[140:143], a[140:143], v[208:211], 0\n"
"ds_read_b128 a[200:203], v240 offset:1024\n"
"ds_read_b128 a[204:207], v241 offset:1024\n"
"v_mfma_f32_16x16x32_bf16 v[140:143], a[236:239], v[212:215], v[140:143]\n"
"ds_read_b128 a[208:211], v240 offset:4096\n"
"ds_read_b128 a[212:215], v241 offset:4096\n"
"v_mfma_f32_16x16x32_bf16 v[144:147], a[144:147], v[208:211], 0\n"
"ds_read_b128 a[216:219], v240 offset:5120\n"
"ds_read_b128 a[220:223], v241 offset:5120\n"
"v_mfma_f32_16x16x32_bf16 v[144:147], a[240:243], v[212:215], v[144:147]\n"
"v_exp_f32_e32 v200, v200\n"
"v_mfma_f32_16x16x32_bf16 v[148:151], a[148:151], v[208:211], 0\n"
"v_exp_f32_e32 v201, v201\n"
"v_mfma_f32_16x16x32_bf16 v[148:151], a[244:247], v[212:215], v[148:151]\n"
"v_cvt_pk_bf16_f32 v252, v200, v201\n"
"v_mfma_f32_16x16x32_bf16 v[152:155], a[152:155], v[208:211], 0\n"
"v_exp_f32_e32 v202, v202\n"
"v_mfma_f32_16x16x32_bf16 v[152:155], a[248:251], v[212:215], v[152:155]\n"
"v_exp_f32_e32 v203, v203\n"
"v_mfma_f32_16x16x32_bf16 v[156:159], a[156:159], v[208:211], 0\n"
"v_cvt_pk_bf16_f32 v253, v202, v203\n"
"s_memrealtime s[74:75]\n"
"v_mfma_f32_16x16x32_bf16 v[156:159], a[252:255], v[212:215], v[156:159]\n"
"s_cmp_eq_u32 s69, 131072\n"
"s_cselect_b32 s68, -131072, s81\n"
"v_mfma_f32_16x16x32_bf16 a[176:179], a[184:187], v[208:211], 0\n"
"s_add_u32 s69, s69, s68\n"
"v_mfma_f32_16x16x32_bf16 a[176:179], a[184:187], v[212:215], a[176:179]\n"
"v_add_u32_e32 v240, s68, v240\n"
"v_add_u32_e32 v241, s68, v241\n"
"L_att%=:\n"
"s_waitcnt vmcnt(16) lgkmcnt(0)\n"
"s_lshl_b32 s74, s74, 5\n"
"s_sub_u32 s74, s79, s74\n"
"s_cmp_lt_i32 s74, -4000\n"
"s_addc_u32 s94, s94, 0\n"
"s_min_i32 s75, s74, -1920\n"
"s_add_u32 s75, s75, 1920\n"
"s_sub_u32 s79, s79, s75\n"
"s_add_u32 s79, s79, s97\n"
"s_add_u32 s79, s79, s97\n"
"s_cmp_lt_i32 s74, 128\n"
"s_cbranch_scc1 L_pe2%=\n"
"L_pl2%=:\n"
"s_sleep 1\n"
"s_sub_u32 s74, s74, 120\n"
"s_cmp_gt_i32 s74, 100\n"
"s_cbranch_scc1 L_pl2%=\n"
"L_pe2%=:\n"
"s_barrier\n"
"v_mfma_f32_16x16x32_bf16 v[192:195], a[96:99], a[0:3], v[216:219]\n"
"v_exp_f32_e32 v204, v204\n"
"v_mfma_f32_16x16x32_bf16 v[192:195], a[100:103], a[4:7], v[192:195]\n"
"v_exp_f32_e32 v205, v205\n"
"v_mfma_f32_16x16x32_bf16 v[192:195], a[104:107], a[8:11], v[192:195]\n"
"v_cvt_pk_bf16_f32 v254, v204, v205\n"
"v_mfma_f32_16x16x32_bf16 v[192:195], a[108:111], a[12:15], v[192:195]\n"
"v_exp_f32_e32 v206, v206\n"
"v_mfma_f32_16x16x32_bf16 v[196:199], a[112:115], a[0:3], v[216:219]\n"
"v_exp_f32_e32 v207, v207\n"
"v_mfma_f32_16x16x32_bf16 v[196:199], a[116:119], a[4:7], v[196:199]\n"
"v_cvt_pk_bf16_f32 v255, v206, v207\n"
"v_mfma_f32_16x16x32_bf16 v[196:199], a[120:123], a[8:11], v[196:199]\n"
"s_cmp_eq_u32 s95, 131072\n"
"s_cselect_b32 s68, -131072, s81\n"
"v_mfma_f32_16x16x32_bf16 v[196:199], a[124:127], a[12:15], v[196:199]\n"
"s_add_u32 s95, s95, s68\n"
"v_mfma_f32_16x16x32_bf16 v[200:203], a[192:195], a[0:3], v[216:219]\n"
"v_mfma_f32_16x16x32_bf16 v[200:203], a[196:199], a[4:7], v[200:203]\n"
"v_exp_f32_e32 v192, v192\n"
"v_mfma_f32_16x16x32_bf16 v[200:203], a[200:203], a[8:11], v[200:203]\n"
"v_exp_f32_e32 v193, v193\n"
"v_mfma_f32_16x16x32_bf16 v[200:203], a[204:207], a[12:15], v[200:203]\n"
"v_cvt_pk_bf16_f32 v208, v192, v193\n"
"v_mfma_f32_16x16x32_bf16 v[204:207], a[208:211], a[0:3], v[216:219]\n"
"v_exp_f32_e32 v194, v194\n"
"v_mfma_f32_16x16x32_bf16 v[204:207], a[212:215], a[4:7], v[204:207]\n"
"v_exp_f32_e32 v195, v195\n"
"v_mfma_f32_16x16x32_bf16 v[204:207], a[216:219], a[8:11], v[204:207]\n"
"v_cvt_pk_bf16_f32 v209, v194, v195\n"
"v_mfma_f32_16x16x32_bf16 v[204:207], a[220:223], a[12:15], v[204:207]\n"
"v_mfma_f32_16x16x32_bf16 v[160:163], a[128:131], v[248:251], v[160:163]\n"
"v_exp_f32_e32 v196, v196\n"
"v_mfma_f32_16x16x32_bf16 v[160:163], a[224:227], v[252:255], v[160:163]\n"
"v_exp_f32_e32 v197, v197\n"
"ds_read_b64_tr_b16 a[128:129], v242 offset:8192\n"
"v_mfma_f32_16x16x32_bf16 v[164:167], a[132:135], v[248:251], v[164:167]\n"
"v_cvt_pk_bf16_f32 v210, v196, v197\n"
"ds_read_b64_tr_b16 a[130:131], v242 offset:12288\n"
"v_mfma_f32_16x16x32_bf16 v[164:167], a[228:231], v[252:255], v[164:167]\n"
"v_exp_f32_e32 v198, v198\n"
"ds_read_b64_tr_b16 a[132:133], v243 offset:8192\n"
"v_mfma_f32_16x16x32_bf16 v[168:171], a[136:139], v[248:251], v[168:171]\n"
"v_exp_f32_e32 v199, v199\n"
"ds_read_b64_tr_b16 a[134:135], v243 offset:12288\n"
"v_mfma_f32_16x16x32_bf16 v[168:171], a[232:235], v[252:255], v[168:171]\n"
"v_cvt_pk_bf16_f32 v211, v198, v199\n"
"ds_read_b64_tr_b16 a[136:137], v242 offset:8704\n"
"v_mfma_f32_16x16x32_bf16 v[172:175], a[140:143], v[248:251], v[172:175]\n"
"ds_read_b64_tr_b16 a[138:139], v242 offset:12800\n"
"v_mfma_f32_16x16x32_bf16 v[172:175], a[236:239], v[252:255], v[172:175]\n"
"ds_read_b64_tr_b16 a[140:141], v243 offset:8704\n"
"v_mfma_f32_16x16x32_bf16 v[176:179], a[144:147], v[248:251], v[176:179]\n"
"ds_read_b64_tr_b16 a[142:143], v243 offset:12800\n"
"v_mfma_f32_16x16x32_bf16 v[176:179], a[240:243], v[252:255], v[176:179]\n"
"v_exp_f32_e32 v200, v200\n"
"ds_read_b64_tr_b16 a[144:145], v242 offset:9216\n"
"v_mfma_f32_16x16x32_bf16 v[180:183], a[148:151], v[248:251], v[180:183]\n"
"v_exp_f32_e32 v201, v201\n"
"ds_read_b64_tr_b16 a[146:147], v242 offset:13312\n"
"v_mfma_f32_16x16x32_bf16 v[180:183], a[244:247], v[252:255], v[180:183]\n"
"v_cvt_pk_bf16_f32 v212, v200, v201\n"
"ds_read_b64_tr_b16 a[148:149], v243 offset:9216\n"
"v_mfma_f32_16x16x32_bf16 v[184:187], a[152:155], v[248:251], v[184:187]\n"
"v_exp_f32_e32 v202, v202\n"
"ds_read_b64_tr_b16 a[150:151], v243 offset:13312\n"
"v_mfma_f32_16x16x32_bf16 v[184:187], a[248:251], v[252:255], v[184:187]\n"
"v_exp_f32_e32 v203, v203\n"
"ds_read_b64_tr_b16 a[152:153], v242 offset:9728\n"
"v_mfma_f32_16x16x32_bf16 v[188:191], a[156:159], v[248:251], v[188:191]\n"
"v_cvt_pk_bf16_f32 v213, v202, v203\n"
"ds_read_b64_tr_b16 a[154:155], v242 offset:13824\n"
"v_mfma_f32_16x16x32_bf16 v[188:191], a[252:255], v[252:255], v[188:191]\n"
"ds_read_b64_tr_b16 a[156:157], v243 offset:9728\n"
"v_mfma_f32_16x16x32_bf16 a[180:183], a[184:187], v[248:251], a[180:183]\n"
"ds_read_b64_tr_b16 a[158:159], v243 offset:13824\n"
"v_mfma_f32_16x16x32_bf16 a[180:183], a[184:187], v[252:255], a[180:183]\n"
"v_add_u32_e32 v242, s68, v242\n"
"v_add_u32_e32 v243, s68, v243\n"
"v_mfma_f32_16x16x32_bf16 v[192:195], a[96:99], a[16:19], v[220:223]\n"
"v_exp_f32_e32 v204, v204\n"
"ds_read_b64_tr_b16 a[224:225], v242 offset:8192\n"
"v_mfma_f32_16x16x32_bf16 v[192:195], a[100:103], a[20:23], v[192:195]\n"
"v_exp_f32_e32 v205, v205\n"
"ds_read_b64_tr_b16 a[226:227], v242 offset:12288\n"
"v_mfma_f32_16x16x32_bf16 v[192:195], a[104:107], a[24:27], v[192:195]\n"
"v_cvt_pk_bf16_f32 v214, v204, v205\n"
"ds_read_b64_tr_b16 a[228:229], v243 offset:8192\n"
"v_mfma_f32_16x16x32_bf16 v[192:195], a[108:111], a[28:31], v[192:195]\n"
"v_exp_f32_e32 v206, v206\n"
"ds_read_b64_tr_b16 a[230:231], v243 offset:12288\n"
"v_mfma_f32_16x16x32_bf16 v[196:199], a[112:115], a[16:19], v[220:223]\n"
"v_exp_f32_e32 v207, v207\n"
"ds_read_b64_tr_b16 a[232:233], v242 offset:8704\n"
"v_mfma_f32_16x16x32_bf16 v[196:199], a[116:119], a[20:23], v[196:199]\n"
"v_cvt_pk_bf16_f32 v215, v206, v207\n"
"ds_read_b64_tr_b16 a[234:235], v242 offset:12800\n"
"v_mfma_f32_16x16x32_bf16 v[196:199], a[120:123], a[24:27], v[196:199]\n"
"ds_read_b64_tr_b16 a[236:237], v243 offset:8704\n"
"v_mfma_f32_16x16x32_bf16 v[196:199], a[124:127], a[28:31], v[196:199]\n"
"ds_read_b64_tr_b16 a[238:239], v243 offset:12800\n"
"v_mfma_f32_16x16x32_bf16 v[200:203], a[192:195], a[16:19], v[220:223]\n"
"ds_read_b64_tr_b16 a[240:241], v242 offset:9216\n"
"v_mfma_f32_16x16x32_bf16 v[200:203], a[196:199], a[20:23], v[200:203]\n"
"v_exp_f32_e32 v192, v192\n"
"ds_read_b64_tr_b16 a[242:243], v242 offset:13312\n"
"v_mfma_f32_16x16x32_bf16 v[200:203], a[200:203], a[24:27], v[200:203]\n"
"v_exp_f32_e32 v193, v193\n"
"ds_read_b64_tr_b16 a[244:245], v243 offset:9216\n"
"v_mfma_f32_16x16x32_bf16 v[200:203], a[204:207], a[28:31], v[200:203]\n"
"v_cvt_pk_bf16_f32 v248, v192, v193\n"
"ds_read_b64_tr_b16 a[246:247], v243 offset:13312\n"
"v_mfma_f32_16x16x32_bf16 v[204:207], a[208:211], a[16:19], v[220:223]\n"
"v_exp_f32_e32 v194, v194\n"
"ds_read_b64_tr_b16 a[248:249], v242 offset:9728\n"
"v_mfma_f32_16x16x32_bf16 v[204:207], a[212:215], a[20:23], v[204:207]\n"
"v_exp_f32_e32 v195, v195\n"
"ds_read_b64_tr_b16 a[250:251], v242 offset:13824\n"
"v_mfma_f32_16x16x32_bf16 v[204:207], a[216:219], a[24:27], v[204:207]\n"
"v_cvt_pk_bf16_f32 v249, v194, v195\n"
"ds_read_b64_tr_b16 a[252:253], v243 offset:9728\n"
"v_mfma_f32_16x16x32_bf16 v[204:207], a[220:223], a[28:31], v[204:207]\n"
"ds_read_b64_tr_b16 a[254:255], v243 offset:13824\n"
"s_waitcnt lgkmcnt(8)\n"
"v_mfma_f32_16x16x32_bf16 v[0:3], a[128:131], v[208:211], v[0:3]\n"
"v_exp_f32_e32 v196, v196\n"
"v_mfma_f32_16x16x32_bf16 v[0:3], a[224:227], v[212:215], v[0:3]\n"
"v_exp_f32_e32 v197, v197\n"
"v_mfma_f32_16x16x32_bf16 v[4:7], a[132:135], v[208:211], v[4:7]\n"
"v_cvt_pk_bf16_f32 v250, v196, v197\n"
"v_mfma_f32_16x16x32_bf16 v[4:7], a[228:231], v[212:215], v[4:7]\n"
"v_exp_f32_e32 v198, v198\n"
"v_mfma_f32_16x16x32_bf16 v[8:11], a[136:139], v[208:211], v[8:11]\n"
"v_exp_f32_e32 v199, v199\n"
"v_mfma_f32_16x16x32_bf16 v[8:11], a[232:235], v[212:215], v[8:11]\n"
"v_cvt_pk_bf16_f32 v251, v198, v199\n"
"v_mfma_f32_16x16x32_bf16 v[12:15], a[140:143], v[208:211], v[12:15]\n"
"s_cmp_eq_u32 s95, 131072\n"
"s_cselect_b32 s68, -131072, s81\n"
"v_mfma_f32_16x16x32_bf16 v[12:15], a[236:239], v[212:215], v[12:15]\n"
"s_waitcnt lgkmcnt(0)\n"
"s_add_u32 s95, s95, s68\n"
"v_mfma_f32_16x16x32_bf16 v[16:19], a[144:147], v[208:211], v[16:19]\n"
"v_add_u32_e32 v242, s68, v242\n"
"v_add_u32_e32 v243, s68, v243\n"
"v_mfma_f32_16x16x32_bf16 v[16:19], a[240:243], v[212:215], v[16:19]\n"
"v_exp_f32_e32 v200, v200\n"
"v_mfma_f32_16x16x32_bf16 v[20:23], a[148:151], v[208:211], v[20:23]\n"
"v_exp_f32_e32 v201, v201\n"
"v_mfma_f32_16x16x32_bf16 v[20:23], a[244:247], v[212:215], v[20:23]\n"
"v_cvt_pk_bf16_f32 v252, v200, v201\n"
"v_mfma_f32_16x16x32_bf16 v[24:27], a[152:155], v[208:211], v[24:27]\n"
"v_exp_f32_e32 v202, v202\n"
"v_mfma_f32_16x16x32_bf16 v[24:27], a[248:251], v[212:215], v[24:27]\n"
"v_exp_f32_e32 v203, v203\n"
"v_mfma_f32_16x16x32_bf16 v[28:31], a[156:159], v[208:211], v[28:31]\n"
"v_cvt_pk_bf16_f32 v253, v202, v203\n"
"v_mfma_f32_16x16x32_bf16 v[28:31], a[252:255], v[212:215], v[28:31]\n"
"v_mfma_f32_16x16x32_bf16 a[160:163], a[184:187], v[208:211], a[160:163]\n"
"v_mfma_f32_16x16x32_bf16 a[160:163], a[184:187], v[212:215], a[160:163]\n"
"v_mfma_f32_16x16x32_bf16 v[192:195], a[96:99], a[32:35], v[224:227]\n"
"v_exp_f32_e32 v204, v204\n"
"v_mfma_f32_16x16x32_bf16 v[192:195], a[100:103], a[36:39], v[192:195]\n"
"v_exp_f32_e32 v205, v205\n"
"v_mfma_f32_16x16x32_bf16 v[192:195], a[104:107], a[40:43], v[192:195]\n"
"v_cvt_pk_bf16_f32 v254, v204, v205\n"
"s_mov_b32 m0, s67\n"
"v_mfma_f32_16x16x32_bf16 v[192:195], a[108:111], a[44:47], v[192:195]\n"
"v_exp_f32_e32 v206, v206\n"
"v_mfma_f32_16x16x32_bf16 v[196:199], a[112:115], a[32:35], v[224:227]\n"
"v_exp_f32_e32 v207, v207\n"
"v_mfma_f32_16x16x32_bf16 v[196:199], a[116:119], a[36:39], v[196:199]\n"
"v_cvt_pk_bf16_f32 v255, v206, v207\n"
"v_mfma_f32_16x16x32_bf16 v[196:199], a[120:123], a[40:43], v[196:199]\n"
"buffer_load_dwordx4 v244, s[48:51], s64 offen nt lds\n"
"s_add_u32 m0, m0, 896\n"
"s_nop 0\n"
"buffer_load_dwordx4 v244, s[48:51], s64 offen offset:128 nt lds\n"
"v_mfma_f32_16x16x32_bf16 v[196:199], a[124:127], a[44:47], v[196:199]\n"
"v_mfma_f32_16x16x32_bf16 v[200:203], a[192:195], a[32:35], v[224:227]\n"
"v_mfma_f32_16x16x32_bf16 v[200:203], a[196:199], a[36:39], v[200:203]\n"
"v_exp_f32_e32 v192, v192\n"
"v_mfma_f32_16x16x32_bf16 v[200:203], a[200:203], a[40:43], v[200:203]\n"
"v_exp_f32_e32 v193, v193\n"
"v_mfma_f32_16x16x32_bf16 v[200:203], a[204:207], a[44:47], v[200:203]\n"
"v_cvt_pk_bf16_f32 v208, v192, v193\n"
"buffer_load_dwordx4 v245, s[48:51], s64 offen offset:2176 nt lds\n"
"s_sub_u32 m0, m0, 896\n"
"s_nop 0\n"
"buffer_load_dwordx4 v245, s[48:51], s64 offen offset:2048 nt lds\n"
"v_mfma_f32_16x16x32_bf16 v[204:207], a[208:211], a[32:35], v[224:227]\n"
"v_exp_f32_e32 v194, v194\n"
"v_mfma_f32_16x16x32_bf16 v[204:207], a[212:215], a[36:39], v[204:207]\n"
"v_exp_f32_e32 v195, v195\n"
"v_mfma_f32_16x16x32_bf16 v[204:207], a[216:219], a[40:43], v[204:207]\n"
"v_cvt_pk_bf16_f32 v209, v194, v195\n"
"v_mfma_f32_16x16x32_bf16 v[204:207], a[220:223], a[44:47], v[204:207]\n"
"v_mfma_f32_16x16x32_bf16 v[32:35], a[128:131], v[248:251], v[32:35]\n"
"v_exp_f32_e32 v196, v196\n"
"v_mfma_f32_16x16x32_bf16 v[32:35], a[224:227], v[252:255], v[32:35]\n"
"v_exp_f32_e32 v197, v197\n"
"v_mfma_f32_16x16x32_bf16 v[36:39], a[132:135], v[248:251], v[36:39]\n"
"v_cvt_pk_bf16_f32 v210, v196, v197\n"
"s_add_u32 m0, m0, 4096\n"
"v_mfma_f32_16x16x32_bf16 v[36:39], a[228:231], v[252:255], v[36:39]\n"
"v_exp_f32_e32 v198, v198\n"
"v_mfma_f32_16x16x32_bf16 v[40:43], a[136:139], v[248:251], v[40:43]\n"
"v_exp_f32_e32 v199, v199\n"
"v_mfma_f32_16x16x32_bf16 v[40:43], a[232:235], v[252:255], v[40:43]\n"
"v_cvt_pk_bf16_f32 v211, v198, v199\n"
"s_add_u32 s64, s64, 4096\n"
"v_mfma_f32_16x16x32_bf16 v[44:47], a[140:143], v[248:251], v[44:47]\n"
"v_mfma_f32_16x16x32_bf16 v[44:47], a[236:239], v[252:255], v[44:47]\n"
"v_mfma_f32_16x16x32_bf16 v[48:51], a[144:147], v[248:251], v[48:51]\n"
"v_mfma_f32_16x16x32_bf16 v[48:51], a[240:243], v[252:255], v[48:51]\n"
"v_exp_f32_e32 v200, v200\n"
"v_mfma_f32_16x16x32_bf16 v[52:55], a[148:151], v[248:251], v[52:55]\n"
"v_exp_f32_e32 v201, v201\n"
"v_mfma_f32_16x16x32_bf16 v[52:55], a[244:247], v[252:255], v[52:55]\n"
"v_cvt_pk_bf16_f32 v212, v200, v201\n"
"buffer_load_dwordx4 v244, s[48:51], s64 offen nt lds\n"
"s_add_u32 m0, m0, 896\n"
"s_nop 0\n"
"buffer_load_dwordx4 v244, s[48:51], s64 offen offset:128 nt lds\n"
"v_mfma_f32_16x16x32_bf16 v[56:59], a[152:155], v[248:251], v[56:59]\n"
"v_exp_f32_e32 v202, v202\n"
"v_mfma_f32_16x16x32_bf16 v[56:59], a[248:251], v[252:255], v[56:59]\n"
"v_exp_f32_e32 v203, v203\n"
"v_mfma_f32_16x16x32_bf16 v[60:63], a[156:159], v[248:251], v[60:63]\n"
"v_cvt_pk_bf16_f32 v213, v202, v203\n"
"v_mfma_f32_16x16x32_bf16 v[60:63], a[252:255], v[252:255], v[60:63]\n"
"v_mfma_f32_16x16x32_bf16 a[164:167], a[184:187], v[248:251], a[164:167]\n"
"buffer_load_dwordx4 v245, s[48:51], s64 offen offset:2176 nt lds\n"
"s_sub_u32 m0, m0, 896\n"
"s_nop 0\n"
"buffer_load_dwordx4 v245, s[48:51], s64 offen offset:2048 nt lds\n"
"v_mfma_f32_16x16x32_bf16 a[164:167], a[184:187], v[252:255], a[164:167]\n"
"v_mfma_f32_16x16x32_bf16 v[192:195], a[96:99], a[48:51], v[228:231]\n"
"v_exp_f32_e32 v204, v204\n"
"v_mfma_f32_16x16x32_bf16 v[192:195], a[100:103], a[52:55], v[192:195]\n"
"v_exp_f32_e32 v205, v205\n"
"v_mfma_f32_16x16x32_bf16 v[192:195], a[104:107], a[56:59], v[192:195]\n"
"v_cvt_pk_bf16_f32 v214, v204, v205\n"
"v_mfma_f32_16x16x32_bf16 v[192:195], a[108:111], a[60:63], v[192:195]\n"
"v_exp_f32_e32 v206, v206\n"
"v_mfma_f32_16x16x32_bf16 v[196:199], a[112:115], a[48:51], v[228:231]\n"
"v_exp_f32_e32 v207, v207\n"
"v_mfma_f32_16x16x32_bf16 v[196:199], a[116:119], a[52:55], v[196:199]\n"
"v_cvt_pk_bf16_f32 v215, v206, v207\n"
"s_add_u32 s64, s64, 12288\n"
"v_mfma_f32_16x16x32_bf16 v[196:199], a[120:123], a[56:59], v[196:199]\n"
"v_mfma_f32_16x16x32_bf16 v[196:199], a[124:127], a[60:63], v[196:199]\n"
"v_mfma_f32_16x16x32_bf16 v[200:203], a[192:195], a[48:51], v[228:231]\n"
"s_and_b32 s64, s64, 0x3ffff\n"
"v_mfma_f32_16x16x32_bf16 v[200:203], a[196:199], a[52:55], v[200:203]\n"
"v_exp_f32_e32 v192, v192\n"
"v_mfma_f32_16x16x32_bf16 v[200:203], a[200:203], a[56:59], v[200:203]\n"
"v_exp_f32_e32 v193, v193\n"
"v_mfma_f32_16x16x32_bf16 v[200:203], a[204:207], a[60:63], v[200:203]\n"
"v_cvt_pk_bf16_f32 v248, v192, v193\n"
"v_mfma_f32_16x16x32_bf16 v[204:207], a[208:211], a[48:51], v[228:231]\n"
"v_exp_f32_e32 v194, v194\n"
"v_mfma_f32_16x16x32_bf16 v[204:207], a[212:215], a[52:55], v[204:207]\n"
"v_exp_f32_e32 v195, v195\n"
"v_mfma_f32_16x16x32_bf16 v[204:207], a[216:219], a[56:59], v[204:207]\n"
"v_cvt_pk_bf16_f32 v249, v194, v195\n"
"s_add_u32 s67, s67, 65536\n"
"v_mfma_f32_16x16x32_bf16 v[204:207], a[220:223], a[60:63], v[204:207]\n"
"v_mfma_f32_16x16x32_bf16 v[64:67], a[128:131], v[208:211], v[64:67]\n"
"v_exp_f32_e32 v196, v196\n"
"v_mfma_f32_16x16x32_bf16 v[64:67], a[224:227], v[212:215], v[64:67]\n"
"v_exp_f32_e32 v197, v197\n"
"v_mfma_f32_16x16x32_bf16 v[68:71], a[132:135], v[208:211], v[68:71]\n"
"v_cvt_pk_bf16_f32 v250, v196, v197\n"
"v_mfma_f32_16x16x32_bf16 v[68:71], a[228:231], v[212:215], v[68:71]\n"
"v_exp_f32_e32 v198, v198\n"
"v_mfma_f32_16x16x32_bf16 v[72:75], a[136:139], v[208:211], v[72:75]\n"
"v_exp_f32_e32 v199, v199\n"
"v_mfma_f32_16x16x32_bf16 v[72:75], a[232:235], v[212:215], v[72:75]\n"
"v_cvt_pk_bf16_f32 v251, v198, v199\n"
"v_mfma_f32_16x16x32_bf16 v[76:79], a[140:143], v[208:211], v[76:79]\n"
"s_cmp_ge_u32 s67, s66\n"
"s_cselect_b32 s80, 163840, 0\n"
"v_mfma_f32_16x16x32_bf16 v[76:79], a[236:239], v[212:215], v[76:79]\n"
"v_mfma_f32_16x16x32_bf16 v[80:83], a[144:147], v[208:211], v[80:83]\n"
"s_sub_u32 s67, s67, s80\n"
"v_mfma_f32_16x16x32_bf16 v[80:83], a[240:243], v[212:215], v[80:83]\n"
"v_exp_f32_e32 v200, v200\n"
"v_mfma_f32_16x16x32_bf16 v[84:87], a[148:151], v[208:211], v[84:87]\n"
"v_exp_f32_e32 v201, v201\n"
"v_mfma_f32_16x16x32_bf16 v[84:87], a[244:247], v[212:215], v[84:87]\n"
"v_cvt_pk_bf16_f32 v252, v200, v201\n"
"v_mfma_f32_16x16x32_bf16 v[88:91], a[152:155], v[208:211], v[88:91]\n"
"v_exp_f32_e32 v202, v202\n"
"v_mfma_f32_16x16x32_bf16 v[88:91], a[248:251], v[212:215], v[88:91]\n"
"v_exp_f32_e32 v203, v203\n"
"v_mfma_f32_16x16x32_bf16 v[92:95], a[156:159], v[208:211], v[92:95]\n"
"v_cvt_pk_bf16_f32 v253, v202, v203\n"
"s_memrealtime s[74:75]\n"
"v_mfma_f32_16x16x32_bf16 v[92:95], a[252:255], v[212:215], v[92:95]\n"
"v_mfma_f32_16x16x32_bf16 a[168:171], a[184:187], v[208:211], a[168:171]\n"
"s_waitcnt lgkmcnt(0)\n"
"v_mfma_f32_16x16x32_bf16 a[168:171], a[184:187], v[212:215], a[168:171]\n"
"s_waitcnt vmcnt(16)\n"
"s_lshl_b32 s74, s74, 5\n"
"s_sub_u32 s74, s79, s74\n"
"s_min_i32 s75, s74, -1920\n"
"s_add_u32 s75, s75, 1920\n"
"s_sub_u32 s79, s79, s75\n"
"s_add_u32 s79, s79, s97\n"
"s_cmp_lt_i32 s74, 128\n"
"s_cbranch_scc1 L_pe3%=\n"
"L_pl3%=:\n"
"s_sleep 1\n"
"s_sub_u32 s74, s74, 120\n"
"s_cmp_gt_i32 s74, 100\n"
"s_cbranch_scc1 L_pl3%=\n"
"L_pe3%=:\n"
"s_barrier\n"
"v_mfma_f32_16x16x32_bf16 v[192:195], a[96:99], a[64:67], v[232:235]\n"
"v_exp_f32_e32 v204, v204\n"
"v_mfma_f32_16x16x32_bf16 v[192:195], a[100:103], a[68:71], v[192:195]\n"
"v_exp_f32_e32 v205, v205\n"
"v_mfma_f32_16x16x32_bf16 v[192:195], a[104:107], a[72:75], v[192:195]\n"
"v_cvt_pk_bf16_f32 v254, v204, v205\n"
"s_add_u32 m0, s91, 8192\n"
"v_mfma_f32_16x16x32_bf16 v[192:195], a[108:111], a[76:79], v[192:195]\n"
"v_exp_f32_e32 v206, v206\n"
"v_mfma_f32_16x16x32_bf16 v[196:199], a[112:115], a[64:67], v[232:235]\n"
"v_exp_f32_e32 v207, v207\n"
"v_mfma_f32_16x16x32_bf16 v[196:199], a[116:119], a[68:71], v[196:199]\n"
"v_cvt_pk_bf16_f32 v255, v206, v207\n"
"buffer_load_dwordx4 v246, s[52:55], s90 offen nt lds\n"
"s_add_u32 m0, m0, 896\n"
"s_nop 0\n"
"buffer_load_dwordx4 v246, s[52:55], s90 offen offset:128 nt lds\n"
"v_mfma_f32_16x16x32_bf16 v[196:199], a[120:123], a[72:75], v[196:199]\n"
"buffer_load_dwordx4 v246, s[52:55], s90 offen offset:2176 nt lds\n"
"s_sub_u32 m0, m0, 896\n"
"s_nop 0\n"
"buffer_load_dwordx4 v246, s[52:55], s90 offen offset:2048 nt lds\n"
"v_mfma_f32_16x16x32_bf16 v[196:199], a[124:127], a[76:79], v[196:199]\n"
"v_mfma_f32_16x16x32_bf16 v[200:203], a[192:195], a[64:67], v[232:235]\n"
"s_add_u32 m0, m0, 4096\n"
"v_mfma_f32_16x16x32_bf16 v[200:203], a[196:199], a[68:71], v[200:203]\n"
"v_exp_f32_e32 v192, v192\n"
"v_mfma_f32_16x16x32_bf16 v[200:203], a[200:203], a[72:75], v[200:203]\n"
"v_exp_f32_e32 v193, v193\n"
"v_mfma_f32_16x16x32_bf16 v[200:203], a[204:207], a[76:79], v[200:203]\n"
"v_cvt_pk_bf16_f32 v208, v192, v193\n"
"s_add_u32 s90, s90, 4096\n"
"v_mfma_f32_16x16x32_bf16 v[204:207], a[208:211], a[64:67], v[232:235]\n"
"v_exp_f32_e32 v194, v194\n"
"v_mfma_f32_16x16x32_bf16 v[204:207], a[212:215], a[68:71], v[204:207]\n"
"v_exp_f32_e32 v195, v195\n"
"v_mfma_f32_16x16x32_bf16 v[204:207], a[216:219], a[72:75], v[204:207]\n"
"v_cvt_pk_bf16_f32 v209, v194, v195\n"
"v_mfma_f32_16x16x32_bf16 v[204:207], a[220:223], a[76:79], v[204:207]\n"
"buffer_load_dwordx4 v246, s[52:55], s90 offen nt lds\n"
"s_add_u32 m0, m0, 896\n"
"s_nop 0\n"
"buffer_load_dwordx4 v246, s[52:55], s90 offen offset:128 nt lds\n"
"v_mfma_f32_16x16x32_bf16 v[96:99], a[128:131], v[248:251], v[96:99]\n"
"v_exp_f32_e32 v196, v196\n"
"v_mfma_f32_16x16x32_bf16 v[96:99], a[224:227], v[252:255], v[96:99]\n"
"v_exp_f32_e32 v197, v197\n"
"v_mfma_f32_16x16x32_bf16 v[100:103], a[132:135], v[248:251], v[100:103]\n"
"v_cvt_pk_bf16_f32 v210, v196, v197\n"
"buffer_load_dwordx4 v246, s[52:55], s90 offen offset:2176 nt lds\n"
"s_sub_u32 m0, m0, 896\n"
"s_nop 0\n"
"buffer_load_dwordx4 v246, s[52:55], s90 offen offset:2048 nt lds\n"
"v_mfma_f32_16x16x32_bf16 v[100:103], a[228:231], v[252:255], v[100:103]\n"
"v_exp_f32_e32 v198, v198\n"
"v_mfma_f32_16x16x32_bf16 v[104:107], a[136:139], v[248:251], v[104:107]\n"
"v_exp_f32_e32 v199, v199\n"
"v_mfma_f32_16x16x32_bf16 v[104:107], a[232:235], v[252:255], v[104:107]\n"
"v_cvt_pk_bf16_f32 v211, v198, v199\n"
"s_add_u32 s90, s90, 12288\n"
"v_mfma_f32_16x16x32_bf16 v[108:111], a[140:143], v[248:251], v[108:111]\n"
"v_mfma_f32_16x16x32_bf16 v[108:111], a[236:239], v[252:255], v[108:111]\n"
"s_and_b32 s90, s90, 0x3ffff\n"
"v_mfma_f32_16x16x32_bf16 v[112:115], a[144:147], v[248:251], v[112:115]\n"
"v_mfma_f32_16x16x32_bf16 v[112:115], a[240:243], v[252:255], v[112:115]\n"
"v_exp_f32_e32 v200, v200\n"
"v_mfma_f32_16x16x32_bf16 v[116:119], a[148:151], v[248:251], v[116:119]\n"
"v_exp_f32_e32 v201, v201\n"
"v_mfma_f32_16x16x32_bf16 v[116:119], a[244:247], v[252:255], v[116:119]\n"
"v_cvt_pk_bf16_f32 v212, v200, v201\n"
"s_add_u32 s91, s91, 65536\n"
"v_mfma_f32_16x16x32_bf16 v[120:123], a[152:155], v[248:251], v[120:123]\n"
"v_exp_f32_e32 v202, v202\n"
"v_mfma_f32_16x16x32_bf16 v[120:123], a[248:251], v[252:255], v[120:123]\n"
"v_exp_f32_e32 v203, v203\n"
"v_mfma_f32_16x16x32_bf16 v[124:127], a[156:159], v[248:251], v[124:127]\n"
"v_cvt_pk_bf16_f32 v213, v202, v203\n"
"v_mfma_f32_16x16x32_bf16 v[124:127], a[252:255], v[252:255], v[124:127]\n"
"s_cmp_ge_u32 s91, s66\n"
"s_cselect_b32 s80, 163840, 0\n"
"v_mfma_f32_16x16x32_bf16 a[172:175], a[184:187], v[248:251], a[172:175]\n"
"s_sub_u32 s91, s91, s80\n"
"v_mfma_f32_16x16x32_bf16 a[172:175], a[184:187], v[252:255], a[172:175]\n"
"v_mfma_f32_16x16x32_bf16 v[192:195], a[96:99], a[80:83], v[236:239]\n"
"v_exp_f32_e32 v204, v204\n"
"v_mfma_f32_16x16x32_bf16 v[192:195], a[100:103], a[84:87], v[192:195]\n"
"v_exp_f32_e32 v205, v205\n"
"v_mfma_f32_16x16x32_bf16 v[192:195], a[104:107], a[88:91], v[192:195]\n"
"v_cvt_pk_bf16_f32 v214, v204, v205\n"
"ds_read_b128 a[96:99], v240 offset:0\n"
"v_mfma_f32_16x16x32_bf16 v[192:195], a[108:111], a[92:95], v[192:195]\n"
"v_exp_f32_e32 v206, v206\n"
"v_mfma_f32_16x16x32_bf16 v[196:199], a[112:115], a[80:83], v[236:239]\n"
"v_exp_f32_e32 v207, v207\n"
"v_mfma_f32_16x16x32_bf16 v[196:199], a[116:119], a[84:87], v[196:199]\n"
"v_cvt_pk_bf16_f32 v215, v206, v207\n"
"ds_read_b128 a[100:103], v241 offset:0\n"
"v_mfma_f32_16x16x32_bf16 v[196:199], a[120:123], a[88:91], v[196:199]\n"
"ds_read_b128 a[104:107], v240 offset:1024\n"
"ds_read_b128 a[108:111], v241 offset:1024\n"
"v_mfma_f32_16x16x32_bf16 v[196:199], a[124:127], a[92:95], v[196:199]\n"
"ds_read_b128 a[112:115], v240 offset:4096\n"
"ds_read_b128 a[116:119], v241 offset:4096\n"
"v_mfma_f32_16x16x32_bf16 v[200:203], a[192:195], a[80:83], v[236:239]\n"
"ds_read_b128 a[120:123], v240 offset:5120\n"
"v_mfma_f32_16x16x32_bf16 v[200:203], a[196:199], a[84:87], v[200:203]\n"
"v_exp_f32_e32 v192, v192\n"
"v_mfma_f32_16x16x32_bf16 v[200:203], a[200:203], a[88:91], v[200:203]\n"
"v_exp_f32_e32 v193, v193\n"
"v_mfma_f32_16x16x32_bf16 v[200:203], a[204:207], a[92:95], v[200:203]\n"
"v_cvt_pk_bf16_f32 v248, v192, v193\n"
"ds_read_b128 a[124:127], v241 offset:5120\n"
"v_mfma_f32_16x16x32_bf16 v[204:207], a[208:211], a[80:83], v[236:239]\n"
"v_exp_f32_e32 v194, v194\n"
"v_mfma_f32_16x16x32_bf16 v[204:207], a[212:215], a[84:87], v[204:207]\n"
"v_exp_f32_e32 v195, v195\n"
"v_mfma_f32_16x16x32_bf16 v[204:207], a[216:219], a[88:91], v[204:207]\n"
"v_cvt_pk_bf16_f32 v249, v194, v195\n"
"s_cmp_eq_u32 s69, 131072\n"
"s_cselect_b32 s68, -131072, s81\n"
"v_mfma_f32_16x16x32_bf16 v[204:207], a[220:223], a[92:95], v[204:207]\n"
"s_add_u32 s69, s69, s68\n"
"v_add_u32_e32 v240, s68, v240\n"
"v_add_u32_e32 v241, s68, v241\n"
"v_mfma_f32_16x16x32_bf16 v[128:131], a[128:131], v[208:211], v[128:131]\n"
"v_exp_f32_e32 v196, v196\n"
"v_mfma_f32_16x16x32_bf16 v[128:131], a[224:227], v[212:215], v[128:131]\n"
"v_exp_f32_e32 v197, v197\n"
"v_mfma_f32_16x16x32_bf16 v[132:135], a[132:135], v[208:211], v[132:135]\n"
"v_cvt_pk_bf16_f32 v250, v196, v197\n"
"ds_read_b128 a[192:195], v240 offset:0\n"
"v_mfma_f32_16x16x32_bf16 v[132:135], a[228:231], v[212:215], v[132:135]\n"
"v_exp_f32_e32 v198, v198\n"
"v_mfma_f32_16x16x32_bf16 v[136:139], a[136:139], v[208:211], v[136:139]\n"
"v_exp_f32_e32 v199, v199\n"
"v_mfma_f32_16x16x32_bf16 v[136:139], a[232:235], v[212:215], v[136:139]\n"
"v_cvt_pk_bf16_f32 v251, v198, v199\n"
"ds_read_b128 a[196:199], v241 offset:0\n"
"v_mfma_f32_16x16x32_bf16 v[140:143], a[140:143], v[208:211], v[140:143]\n"
"ds_read_b128 a[200:203], v240 offset:1024\n"
"ds_read_b128 a[204:207], v241 offset:1024\n"
"v_mfma_f32_16x16x32_bf16 v[140:143], a[236:239], v[212:215], v[140:143]\n"
"ds_read_b128 a[208:211], v240 offset:4096\n"
"ds_read_b128 a[212:215], v241 offset:4096\n"
"v_mfma_f32_16x16x32_bf16 v[144:147], a[144:147], v[208:211], v[144:147]\n"
"ds_read_b128 a[216:219], v240 offset:5120\n"
"ds_read_b128 a[220:223], v241 offset:5120\n"
"v_mfma_f32_16x16x32_bf16 v[144:147], a[240:243], v[212:215], v[144:147]\n"
"v_exp_f32_e32 v200, v200\n"
"v_mfma_f32_16x16x32_bf16 v[148:151], a[148:151], v[208:211], v[148:151]\n"
"v_exp_f32_e32 v201, v201\n"
"v_mfma_f32_16x16x32_bf16 v[148:151], a[244:247], v[212:215], v[148:151]\n"
"v_cvt_pk_bf16_f32 v252, v200, v201\n"
"v_mfma_f32_16x16x32_bf16 v[152:155], a[152:155], v[208:211], v[152:155]\n"
"v_exp_f32_e32 v202, v202\n"
"v_mfma_f32_16x16x32_bf16 v[152:155], a[248:251], v[212:215], v[152:155]\n"
"v_exp_f32_e32 v203, v203\n"
"v_mfma_f32_16x16x32_bf16 v[156:159], a[156:159], v[208:211], v[156:159]\n"
"v_cvt_pk_bf16_f32 v253, v202, v203\n"
"s_memrealtime s[74:75]\n"
"v_mfma_f32_16x16x32_bf16 v[156:159], a[252:255], v[212:215], v[156:159]\n"
"s_cmp_eq_u32 s69, 131072\n"
"s_cselect_b32 s68, -131072, s81\n"
"v_mfma_f32_16x16x32_bf16 a[176:179], a[184:187], v[208:211], a[176:179]\n"
"s_add_u32 s69, s69, s68\n"
"v_mfma_f32_16x16x32_bf16 a[176:179], a[184:187], v[212:215], a[176:179]\n"
"v_add_u32_e32 v240, s68, v240\n"
"v_add_u32_e32 v241, s68, v241\n"
"s_cmp_ge_u32 s63, 12\n"
"s_cselect_b32 s94, 0, s94\n"
"s_cmp_ge_u32 s94, 2\n"
"s_cselect_b32 s97, 2901, s97\n"
"s_cmp_eq_u32 s63, s77\n"
"s_cbranch_scc1 L_side%=\n"
"L_bumpd%=:\n"
"s_sub_u32 s63, s63, 1\n"
"s_cmp_lg_u32 s63, 0\n"
"s_cbranch_scc1 L_att%=\n"
"s_waitcnt vmcnt(16) lgkmcnt(0)\n"
"s_barrier\n"
"v_mfma_f32_16x16x32_bf16 v[192:195], a[96:99], a[0:3], v[216:219]\n"
"v_exp_f32_e32 v204, v204\n"
"v_mfma_f32_16x16x32_bf16 v[192:195], a[100:103], a[4:7], v[192:195]\n"
"v_exp_f32_e32 v205, v205\n"
"v_mfma_f32_16x16x32_bf16 v[192:195], a[104:107], a[8:11], v[192:195]\n"
"v_cvt_pk_bf16_f32 v254, v204, v205\n"
"v_mfma_f32_16x16x32_bf16 v[192:195], a[108:111], a[12:15], v[192:195]\n"
"v_exp_f32_e32 v206, v206\n"
"v_mfma_f32_16x16x32_bf16 v[196:199], a[112:115], a[0:3], v[216:219]\n"
"v_exp_f32_e32 v207, v207\n"
"v_mfma_f32_16x16x32_bf16 v[196:199], a[116:119], a[4:7], v[196:199]\n"
"v_cvt_pk_bf16_f32 v255, v206, v207\n"
"v_mfma_f32_16x16x32_bf16 v[196:199], a[120:123], a[8:11], v[196:199]\n"
"s_cmp_eq_u32 s95, 131072\n"
"s_cselect_b32 s68, -131072, s81\n"
"v_mfma_f32_16x16x32_bf16 v[196:199], a[124:127], a[12:15], v[196:199]\n"
"s_add_u32 s95, s95, s68\n"
"v_mfma_f32_16x16x32_bf16 v[200:203], a[192:195], a[0:3], v[216:219]\n"
"v_mfma_f32_16x16x32_bf16 v[200:203], a[196:199], a[4:7], v[200:203]\n"
"v_exp_f32_e32 v192, v192\n"
"v_mfma_f32_16x16x32_bf16 v[200:203], a[200:203], a[8:11], v[200:203]\n"
"v_exp_f32_e32 v193, v193\n"
"v_mfma_f32_16x16x32_bf16 v[200:203], a[204:207], a[12:15], v[200:203]\n"
"v_cvt_pk_bf16_f32 v208, v192, v193\n"
"v_mfma_f32_16x16x32_bf16 v[204:207], a[208:211], a[0:3], v[216:219]\n"
"v_exp_f32_e32 v194, v194\n"
"v_mfma_f32_16x16x32_bf16 v[204:207], a[212:215], a[4:7], v[204:207]\n"
"v_exp_f32_e32 v195, v195\n"
"v_mfma_f32_16x16x32_bf16 v[204:207], a[216:219], a[8:11], v[204:207]\n"
"v_cvt_pk_bf16_f32 v209, v194, v195\n"
"v_mfma_f32_16x16x32_bf16 v[204:207], a[220:223], a[12:15], v[204:207]\n"
"v_mfma_f32_16x16x32_bf16 v[160:163], a[128:131], v[248:251], v[160:163]\n"
"v_exp_f32_e32 v196, v196\n"
"v_mfma_f32_16x16x32_bf16 v[160:163], a[224:227], v[252:255], v[160:163]\n"
"v_exp_f32_e32 v197, v197\n"
"ds_read_b64_tr_b16 a[128:129], v242 offset:8192\n"
"v_mfma_f32_16x16x32_bf16 v[164:167], a[132:135], v[248:251], v[164:167]\n"
"v_cvt_pk_bf16_f32 v210, v196, v197\n"
"ds_read_b64_tr_b16 a[130:131], v242 offset:12288\n"
"v_mfma_f32_16x16x32_bf16 v[164:167], a[228:231], v[252:255], v[164:167]\n"
"v_exp_f32_e32 v198, v198\n"
"ds_read_b64_tr_b16 a[132:133], v243 offset:8192\n"
"v_mfma_f32_16x16x32_bf16 v[168:171], a[136:139], v[248:251], v[168:171]\n"
"v_exp_f32_e32 v199, v199\n"
"ds_read_b64_tr_b16 a[134:135], v243 offset:12288\n"
"v_mfma_f32_16x16x32_bf16 v[168:171], a[232:235], v[252:255], v[168:171]\n"
"v_cvt_pk_bf16_f32 v211, v198, v199\n"
"ds_read_b64_tr_b16 a[136:137], v242 offset:8704\n"
"v_mfma_f32_16x16x32_bf16 v[172:175], a[140:143], v[248:251], v[172:175]\n"
"ds_read_b64_tr_b16 a[138:139], v242 offset:12800\n"
"v_mfma_f32_16x16x32_bf16 v[172:175], a[236:239], v[252:255], v[172:175]\n"
"ds_read_b64_tr_b16 a[140:141], v243 offset:8704\n"
"v_mfma_f32_16x16x32_bf16 v[176:179], a[144:147], v[248:251], v[176:179]\n"
"ds_read_b64_tr_b16 a[142:143], v243 offset:12800\n"
"v_mfma_f32_16x16x32_bf16 v[176:179], a[240:243], v[252:255], v[176:179]\n"
"v_exp_f32_e32 v200, v200\n"
"ds_read_b64_tr_b16 a[144:145], v242 offset:9216\n"
"v_mfma_f32_16x16x32_bf16 v[180:183], a[148:151], v[248:251], v[180:183]\n"
"v_exp_f32_e32 v201, v201\n"
"ds_read_b64_tr_b16 a[146:147], v242 offset:13312\n"
"v_mfma_f32_16x16x32_bf16 v[180:183], a[244:247], v[252:255], v[180:183]\n"
"v_cvt_pk_bf16_f32 v212, v200, v201\n"
"ds_read_b64_tr_b16 a[148:149], v243 offset:9216\n"
"v_mfma_f32_16x16x32_bf16 v[184:187], a[152:155], v[248:251], v[184:187]\n"
"v_exp_f32_e32 v202, v202\n"
"ds_read_b64_tr_b16 a[150:151], v243 offset:13312\n"
"v_mfma_f32_16x16x32_bf16 v[184:187], a[248:251], v[252:255], v[184:187]\n"
"v_exp_f32_e32 v203, v203\n"
"ds_read_b64_tr_b16 a[152:153], v242 offset:9728\n"
"v_mfma_f32_16x16x32_bf16 v[188:191], a[156:159], v[248:251], v[188:191]\n"
"v_cvt_pk_bf16_f32 v213, v202, v203\n"
"ds_read_b64_tr_b16 a[154:155], v242 offset:13824\n"
"v_mfma_f32_16x16x32_bf16 v[188:191], a[252:255], v[252:255], v[188:191]\n"
"ds_read_b64_tr_b16 a[156:157], v243 offset:9728\n"
"v_mfma_f32_16x16x32_bf16 a[180:183], a[184:187], v[248:251], a[180:183]\n"
"ds_read_b64_tr_b16 a[158:159], v243 offset:13824\n"
"v_mfma_f32_16x16x32_bf16 a[180:183], a[184:187], v[252:255], a[180:183]\n"
"v_add_u32_e32 v242, s68, v242\n"
"v_add_u32_e32 v243, s68, v243\n"
"v_mfma_f32_16x16x32_bf16 v[192:195], a[96:99], a[16:19], v[220:223]\n"
"v_exp_f32_e32 v204, v204\n"
"ds_read_b64_tr_b16 a[224:225], v242 offset:8192\n"
"v_mfma_f32_16x16x32_bf16 v[192:195], a[100:103], a[20:23], v[192:195]\n"
"v_exp_f32_e32 v205, v205\n"
"ds_read_b64_tr_b16 a[226:227], v242 offset:12288\n"
"v_mfma_f32_16x16x32_bf16 v[192:195], a[104:107], a[24:27], v[192:195]\n"
"v_cvt_pk_bf16_f32 v214, v204, v205\n"
"ds_read_b64_tr_b16 a[228:229], v243 offset:8192\n"
"v_mfma_f32_16x16x32_bf16 v[192:195], a[108:111], a[28:31], v[192:195]\n"
"v_exp_f32_e32 v206, v206\n"
"ds_read_b64_tr_b16 a[230:231], v243 offset:12288\n"
"v_mfma_f32_16x16x32_bf16 v[196:199], a[112:115], a[16:19], v[220:223]\n"
"v_exp_f32_e32 v207, v207\n"
"ds_read_b64_tr_b16 a[232:233], v242 offset:8704\n"
"v_mfma_f32_16x16x32_bf16 v[196:199], a[116:119], a[20:23], v[196:199]\n"
"v_cvt_pk_bf16_f32 v215, v206, v207\n"
"ds_read_b64_tr_b16 a[234:235], v242 offset:12800\n"
"v_mfma_f32_16x16x32_bf16 v[196:199], a[120:123], a[24:27], v[196:199]\n"
"ds_read_b64_tr_b16 a[236:237], v243 offset:8704\n"
"v_mfma_f32_16x16x32_bf16 v[196:199], a[124:127], a[28:31], v[196:199]\n"
"ds_read_b64_tr_b16 a[238:239], v243 offset:12800\n"
"v_mfma_f32_16x16x32_bf16 v[200:203], a[192:195], a[16:19], v[220:223]\n"
"ds_read_b64_tr_b16 a[240:241], v242 offset:9216\n"
"v_mfma_f32_16x16x32_bf16 v[200:203], a[196:199], a[20:23], v[200:203]\n"
"v_exp_f32_e32 v192, v192\n"
"ds_read_b64_tr_b16 a[242:243], v242 offset:13312\n"
"v_mfma_f32_16x16x32_bf16 v[200:203], a[200:203], a[24:27], v[200:203]\n"
"v_exp_f32_e32 v193, v193\n"
"ds_read_b64_tr_b16 a[244:245], v243 offset:9216\n"
"v_mfma_f32_16x16x32_bf16 v[200:203], a[204:207], a[28:31], v[200:203]\n"
"v_cvt_pk_bf16_f32 v248, v192, v193\n"
"ds_read_b64_tr_b16 a[246:247], v243 offset:13312\n"
"v_mfma_f32_16x16x32_bf16 v[204:207], a[208:211], a[16:19], v[220:223]\n"
"v_exp_f32_e32 v194, v194\n"
"ds_read_b64_tr_b16 a[248:249], v242 offset:9728\n"
"v_mfma_f32_16x16x32_bf16 v[204:207], a[212:215], a[20:23], v[204:207]\n"
"v_exp_f32_e32 v195, v195\n"
"ds_read_b64_tr_b16 a[250:251], v242 offset:13824\n"
"v_mfma_f32_16x16x32_bf16 v[204:207], a[216:219], a[24:27], v[204:207]\n"
"v_cvt_pk_bf16_f32 v249, v194, v195\n"
"ds_read_b64_tr_b16 a[252:253], v243 offset:9728\n"
"v_mfma_f32_16x16x32_bf16 v[204:207], a[220:223], a[28:31], v[204:207]\n"
"ds_read_b64_tr_b16 a[254:255], v243 offset:13824\n"
"s_waitcnt lgkmcnt(8)\n"
"v_mfma_f32_16x16x32_bf16 v[0:3], a[128:131], v[208:211], v[0:3]\n"
"v_exp_f32_e32 v196, v196\n"
"v_mfma_f32_16x16x32_bf16 v[0:3], a[224:227], v[212:215], v[0:3]\n"
"v_exp_f32_e32 v197, v197\n"
"v_mfma_f32_16x16x32_bf16 v[4:7], a[132:135], v[208:211], v[4:7]\n"
"v_cvt_pk_bf16_f32 v250, v196, v197\n"
"v_mfma_f32_16x16x32_bf16 v[4:7], a[228:231], v[212:215], v[4:7]\n"
"v_exp_f32_e32 v198, v198\n"
"v_mfma_f32_16x16x32_bf16 v[8:11], a[136:139], v[208:211], v[8:11]\n"
"v_exp_f32_e32 v199, v199\n"
"v_mfma_f32_16x16x32_bf16 v[8:11], a[232:235], v[212:215], v[8:11]\n"
"v_cvt_pk_bf16_f32 v251, v198, v199\n"
"v_mfma_f32_16x16x32_bf16 v[12:15], a[140:143], v[208:211], v[12:15]\n"
"s_cmp_eq_u32 s95, 131072\n"
"s_cselect_b32 s68, -131072, s81\n"
"v_mfma_f32_16x16x32_bf16 v[12:15], a[236:239], v[212:215], v[12:15]\n"
"s_waitcnt lgkmcnt(0)\n"
"s_add_u32 s95, s95, s68\n"
"v_mfma_f32_16x16x32_bf16 v[16:19], a[144:147], v[208:211], v[16:19]\n"
"v_add_u32_e32 v242, s68, v242\n"
"v_add_u32_e32 v243, s68, v243\n"
"v_mfma_f32_16x16x32_bf16 v[16:19], a[240:243], v[212:215], v[16:19]\n"
"v_exp_f32_e32 v200, v200\n"
"v_mfma_f32_16x16x32_bf16 v[20:23], a[148:151], v[208:211], v[20:23]\n"
"v_exp_f32_e32 v201, v201\n"
"v_mfma_f32_16x16x32_bf16 v[20:23], a[244:247], v[212:215], v[20:23]\n"
"v_cvt_pk_bf16_f32 v252, v200, v201\n"
"v_mfma_f32_16x16x32_bf16 v[24:27], a[152:155], v[208:211], v[24:27]\n"
"v_exp_f32_e32 v202, v202\n"
"v_mfma_f32_16x16x32_bf16 v[24:27], a[248:251], v[212:215], v[24:27]\n"
"v_exp_f32_e32 v203, v203\n"
"v_mfma_f32_16x16x32_bf16 v[28:31], a[156:159], v[208:211], v[28:31]\n"
"v_cvt_pk_bf16_f32 v253, v202, v203\n"
"v_mfma_f32_16x16x32_bf16 v[28:31], a[252:255], v[212:215], v[28:31]\n"
"v_mfma_f32_16x16x32_bf16 a[160:163], a[184:187], v[208:211], a[160:163]\n"
"v_mfma_f32_16x16x32_bf16 a[160:163], a[184:187], v[212:215], a[160:163]\n"
"v_mfma_f32_16x16x32_bf16 v[192:195], a[96:99], a[32:35], v[224:227]\n"
"v_exp_f32_e32 v204, v204\n"
"v_mfma_f32_16x16x32_bf16 v[192:195], a[100:103], a[36:39], v[192:195]\n"
"v_exp_f32_e32 v205, v205\n"
"v_mfma_f32_16x16x32_bf16 v[192:195], a[104:107], a[40:43], v[192:195]\n"
"v_cvt_pk_bf16_f32 v254, v204, v205\n"
"s_bfe_u32 s70, %15, 0x10004\n"
"v_mfma_f32_16x16x32_bf16 v[192:195], a[108:111], a[44:47], v[192:195]\n"
"v_exp_f32_e32 v206, v206\n"
"v_mfma_f32_16x16x32_bf16 v[196:199], a[112:115], a[32:35], v[224:227]\n"
"v_exp_f32_e32 v207, v207\n"
"v_mfma_f32_16x16x32_bf16 v[196:199], a[116:119], a[36:39], v[196:199]\n"
"v_cvt_pk_bf16_f32 v255, v206, v207\n"
"s_mul_i32 s70, s70, 655360\n"
"v_mfma_f32_16x16x32_bf16 v[196:199], a[120:123], a[40:43], v[196:199]\n"
"s_bfe_u32 s71, %15, 0x10003\n"
"s_mul_i32 s73, s71, 327680\n"
"v_mfma_f32_16x16x32_bf16 v[196:199], a[124:127], a[44:47], v[196:199]\n"
"s_add_u32 s73, s73, s70\n"
"s_bfe_u32 s70, %15, 0x20003\n"
"v_mfma_f32_16x16x32_bf16 v[200:203], a[192:195], a[32:35], v[224:227]\n"
"s_mul_i32 s70, s70, 320\n"
"v_mfma_f32_16x16x32_bf16 v[200:203], a[196:199], a[36:39], v[200:203]\n"
"v_exp_f32_e32 v192, v192\n"
"v_mfma_f32_16x16x32_bf16 v[200:203], a[200:203], a[40:43], v[200:203]\n"
"v_exp_f32_e32 v193, v193\n"
"v_mfma_f32_16x16x32_bf16 v[200:203], a[204:207], a[44:47], v[200:203]\n"
"v_cvt_pk_bf16_f32 v208, v192, v193\n"
"s_mul_i32 s71, s60, 20480\n"
"v_mfma_f32_16x16x32_bf16 v[204:207], a[208:211], a[32:35], v[224:227]\n"
"v_exp_f32_e32 v194, v194\n"
"v_mfma_f32_16x16x32_bf16 v[204:207], a[212:215], a[36:39], v[204:207]\n"
"v_exp_f32_e32 v195, v195\n"
"v_mfma_f32_16x16x32_bf16 v[204:207], a[216:219], a[40:43], v[204:207]\n"
"v_cvt_pk_bf16_f32 v209, v194, v195\n"
"s_add_u32 s70, s70, s71\n"
"v_mfma_f32_16x16x32_bf16 v[204:207], a[220:223], a[44:47], v[204:207]\n"
"s_add_u32 s40, %10, s70\n"
"s_addc_u32 s41, %11, 0\n"
"v_mfma_f32_16x16x32_bf16 v[32:35], a[128:131], v[248:251], v[32:35]\n"
"v_exp_f32_e32 v196, v196\n"
"v_mfma_f32_16x16x32_bf16 v[32:35], a[224:227], v[252:255], v[32:35]\n"
"v_exp_f32_e32 v197, v197\n"
"v_mfma_f32_16x16x32_bf16 v[36:39], a[132:135], v[248:251], v[36:39]\n"
"v_cvt_pk_bf16_f32 v210, v196, v197\n"
"s_and_b32 s41, s41, 0xffff\n"
"v_mfma_f32_16x16x32_bf16 v[36:39], a[228:231], v[252:255], v[36:39]\n"
"v_exp_f32_e32 v198, v198\n"
"v_mfma_f32_16x16x32_bf16 v[40:43], a[136:139], v[248:251], v[40:43]\n"
"v_exp_f32_e32 v199, v199\n"
"v_mfma_f32_16x16x32_bf16 v[40:43], a[232:235], v[252:255], v[40:43]\n"
"v_cvt_pk_bf16_f32 v211, v198, v199\n"
"s_mov_b32 s42, 1248320\n"
"v_mfma_f32_16x16x32_bf16 v[44:47], a[140:143], v[248:251], v[44:47]\n"
"s_mov_b32 s43, 0x20000\n"
"s_mul_i32 s78, s60, 5120\n"
"v_mfma_f32_16x16x32_bf16 v[44:47], a[236:239], v[252:255], v[44:47]\n"
"s_mul_i32 s70, %16, 49152\n"
"v_mfma_f32_16x16x32_bf16 v[48:51], a[144:147], v[248:251], v[48:51]\n"
"s_add_u32 s56, %8, s70\n"
"s_addc_u32 s57, %9, 0\n"
"v_mfma_f32_16x16x32_bf16 v[48:51], a[240:243], v[252:255], v[48:51]\n"
"v_exp_f32_e32 v200, v200\n"
"v_mfma_f32_16x16x32_bf16 v[52:55], a[148:151], v[248:251], v[52:55]\n"
"v_exp_f32_e32 v201, v201\n"
"v_mfma_f32_16x16x32_bf16 v[52:55], a[244:247], v[252:255], v[52:55]\n"
"v_cvt_pk_bf16_f32 v212, v200, v201\n"
"s_and_b32 s57, s57, 0xffff\n"
"v_mfma_f32_16x16x32_bf16 v[56:59], a[152:155], v[248:251], v[56:59]\n"
"v_exp_f32_e32 v202, v202\n"
"v_mfma_f32_16x16x32_bf16 v[56:59], a[248:251], v[252:255], v[56:59]\n"
"v_exp_f32_e32 v203, v203\n"
"v_mfma_f32_16x16x32_bf16 v[60:63], a[156:159], v[248:251], v[60:63]\n"
"v_cvt_pk_bf16_f32 v213, v202, v203\n"
"s_mov_b32 s58, 49152\n"
"v_mfma_f32_16x16x32_bf16 v[60:63], a[252:255], v[252:255], v[60:63]\n"
"s_mov_b32 s59, 0x20000\n"
"v_lshrrev_b32_e32 v244, 8, v246\n"
"v_mfma_f32_16x16x32_bf16 a[164:167], a[184:187], v[248:251], a[164:167]\n"
"v_lshl_add_u32 v244, v244, 10, v246\n"
"v_bfe_u32 v245, v246, 6, 1\n"
"v_mfma_f32_16x16x32_bf16 a[164:167], a[184:187], v[252:255], a[164:167]\n"
"s_movk_i32 s70, 9920\n"
"v_mad_u32_u24 v245, v245, s70, v244\n"
"v_mfma_f32_16x16x32_bf16 v[192:195], a[96:99], a[48:51], v[228:231]\n"
"v_exp_f32_e32 v204, v204\n"
"v_mfma_f32_16x16x32_bf16 v[192:195], a[100:103], a[52:55], v[192:195]\n"
"v_exp_f32_e32 v205, v205\n"
"v_mfma_f32_16x16x32_bf16 v[192:195], a[104:107], a[56:59], v[192:195]\n"
"v_cvt_pk_bf16_f32 v214, v204, v205\n"
"v_mbcnt_lo_u32_b32 v246, -1, 0\n"
"v_mbcnt_hi_u32_b32 v246, -1, v246\n"
"v_mfma_f32_16x16x32_bf16 v[192:195], a[108:111], a[60:63], v[192:195]\n"
"v_exp_f32_e32 v206, v206\n"
"v_mfma_f32_16x16x32_bf16 v[196:199], a[112:115], a[48:51], v[228:231]\n"
"v_exp_f32_e32 v207, v207\n"
"v_mfma_f32_16x16x32_bf16 v[196:199], a[116:119], a[52:55], v[196:199]\n"
"v_cvt_pk_bf16_f32 v215, v206, v207\n"
"v_lshlrev_b32_e32 v246, 4, v246\n"
"v_mfma_f32_16x16x32_bf16 v[196:199], a[120:123], a[56:59], v[196:199]\n"
"s_and_b32 s80, s60, 1\n"
"s_mul_i32 s80, s80, 6144\n"
"v_mfma_f32_16x16x32_bf16 v[196:199], a[124:127], a[60:63], v[196:199]\n"
"s_lshr_b32 s70, s60, 1\n"
"s_mul_i32 s70, s70, 3072\n"
"v_mfma_f32_16x16x32_bf16 v[200:203], a[192:195], a[48:51], v[228:231]\n"
"s_sub_u32 s67, s80, s70\n"
"v_mfma_f32_16x16x32_bf16 v[200:203], a[196:199], a[52:55], v[200:203]\n"
"v_exp_f32_e32 v192, v192\n"
"v_mfma_f32_16x16x32_bf16 v[200:203], a[200:203], a[56:59], v[200:203]\n"
"v_exp_f32_e32 v193, v193\n"
"v_mfma_f32_16x16x32_bf16 v[200:203], a[204:207], a[60:63], v[200:203]\n"
"v_cvt_pk_bf16_f32 v248, v192, v193\n"
"s_add_u32 s67, s67, 3072\n"
"v_mfma_f32_16x16x32_bf16 v[204:207], a[208:211], a[48:51], v[228:231]\n"
"v_exp_f32_e32 v194, v194\n"
"v_mfma_f32_16x16x32_bf16 v[204:207], a[212:215], a[52:55], v[204:207]\n"
"v_exp_f32_e32 v195, v195\n"
"v_mfma_f32_16x16x32_bf16 v[204:207], a[216:219], a[56:59], v[204:207]\n"
"v_cvt_pk_bf16_f32 v249, v194, v195\n"
"s_add_u32 s80, s80, s70\n"
"v_mfma_f32_16x16x32_bf16 v[204:207], a[220:223], a[60:63], v[204:207]\n"
"s_add_u32 m0, s78, 0\n"
"s_nop 0\n"
"buffer_load_dwordx4 v244, s[40:43], s73 offen lds\n"
"v_mfma_f32_16x16x32_bf16 v[64:67], a[128:131], v[208:211], v[64:67]\n"
"v_exp_f32_e32 v196, v196\n"
"v_mfma_f32_16x16x32_bf16 v[64:67], a[224:227], v[212:215], v[64:67]\n"
"v_exp_f32_e32 v197, v197\n"
"v_mfma_f32_16x16x32_bf16 v[68:71], a[132:135], v[208:211], v[68:71]\n"
"v_cvt_pk_bf16_f32 v250, v196, v197\n"
"s_add_u32 m0, s78, 896\n"
"v_mfma_f32_16x16x32_bf16 v[68:71], a[228:231], v[212:215], v[68:71]\n"
"v_exp_f32_e32 v198, v198\n"
"v_mfma_f32_16x16x32_bf16 v[72:75], a[136:139], v[208:211], v[72:75]\n"
"v_exp_f32_e32 v199, v199\n"
"v_mfma_f32_16x16x32_bf16 v[72:75], a[232:235], v[212:215], v[72:75]\n"
"v_cvt_pk_bf16_f32 v251, v198, v199\n"
"v_mfma_f32_16x16x32_bf16 v[76:79], a[140:143], v[208:211], v[76:79]\n"
"buffer_load_dwordx4 v244, s[40:43], s73 offen offset:128 lds\n"
"v_mfma_f32_16x16x32_bf16 v[76:79], a[236:239], v[212:215], v[76:79]\n"
"s_add_u32 m0, s78, 1792\n"
"v_mfma_f32_16x16x32_bf16 v[80:83], a[144:147], v[208:211], v[80:83]\n"
"buffer_load_dwordx4 v245, s[40:43], s73 offen offset:256 lds\n"
"v_mfma_f32_16x16x32_bf16 v[80:83], a[240:243], v[212:215], v[80:83]\n"
"v_exp_f32_e32 v200, v200\n"
"v_mfma_f32_16x16x32_bf16 v[84:87], a[148:151], v[208:211], v[84:87]\n"
"v_exp_f32_e32 v201, v201\n"
"v_mfma_f32_16x16x32_bf16 v[84:87], a[244:247], v[212:215], v[84:87]\n"
"v_cvt_pk_bf16_f32 v252, v200, v201\n"
"s_add_u32 m0, s78, 3008\n"
"s_add_u32 s64, s73, 10240\n"
"v_mfma_f32_16x16x32_bf16 v[88:91], a[152:155], v[208:211], v[88:91]\n"
"v_exp_f32_e32 v202, v202\n"
"buffer_load_dwordx4 v244, s[40:43], s64 offen offset:64 lds\n"
"v_mfma_f32_16x16x32_bf16 v[88:91], a[248:251], v[212:215], v[88:91]\n"
"v_exp_f32_e32 v203, v203\n"
"s_add_u32 m0, s78, 3904\n"
"s_nop 0\n"
"buffer_load_dwordx4 v244, s[40:43], s64 offen offset:192 lds\n"
"s_add_u32 s73, s73, 81920\n"
"s_cmp_eq_u32 s73, 1310720\n"
"s_cselect_b32 s73, 0, s73\n"
"v_mfma_f32_16x16x32_bf16 v[92:95], a[156:159], v[208:211], v[92:95]\n"
"v_cvt_pk_bf16_f32 v253, v202, v203\n"
"s_memrealtime s[74:75]\n"
"v_mfma_f32_16x16x32_bf16 v[92:95], a[252:255], v[212:215], v[92:95]\n"
"v_mfma_f32_16x16x32_bf16 a[168:171], a[184:187], v[208:211], a[168:171]\n"
"s_waitcnt lgkmcnt(0)\n"
"v_mfma_f32_16x16x32_bf16 a[168:171], a[184:187], v[212:215], a[168:171]\n"
"s_waitcnt vmcnt(13)\n"
"s_barrier\n"
"v_mfma_f32_16x16x32_bf16 v[192:195], a[96:99], a[64:67], v[232:235]\n"
"v_exp_f32_e32 v204, v204\n"
"v_mfma_f32_16x16x32_bf16 v[192:195], a[100:103], a[68:71], v[192:195]\n"
"v_exp_f32_e32 v205, v205\n"
"v_mfma_f32_16x16x32_bf16 v[192:195], a[104:107], a[72:75], v[192:195]\n"
"v_cvt_pk_bf16_f32 v254, v204, v205\n"
"s_add_u32 m0, s78, 20480\n"
"v_mfma_f32_16x16x32_bf16 v[192:195], a[108:111], a[76:79], v[192:195]\n"
"v_exp_f32_e32 v206, v206\n"
"v_mfma_f32_16x16x32_bf16 v[196:199], a[112:115], a[64:67], v[232:235]\n"
"v_exp_f32_e32 v207, v207\n"
"v_mfma_f32_16x16x32_bf16 v[196:199], a[116:119], a[68:71], v[196:199]\n"
"v_cvt_pk_bf16_f32 v255, v206, v207\n"
"v_mfma_f32_16x16x32_bf16 v[196:199], a[120:123], a[72:75], v[196:199]\n"
"buffer_load_dwordx4 v244, s[40:43], s73 offen lds\n"
"v_mfma_f32_16x16x32_bf16 v[196:199], a[124:127], a[76:79], v[196:199]\n"
"s_add_u32 m0, s78, 21376\n"
"v_mfma_f32_16x16x32_bf16 v[200:203], a[192:195], a[64:67], v[232:235]\n"
"buffer_load_dwordx4 v244, s[40:43], s73 offen offset:128 lds\n"
"v_mfma_f32_16x16x32_bf16 v[200:203], a[196:199], a[68:71], v[200:203]\n"
"v_exp_f32_e32 v192, v192\n"
"v_mfma_f32_16x16x32_bf16 v[200:203], a[200:203], a[72:75], v[200:203]\n"
"v_exp_f32_e32 v193, v193\n"
"v_mfma_f32_16x16x32_bf16 v[200:203], a[204:207], a[76:79], v[200:203]\n"
"v_cvt_pk_bf16_f32 v208, v192, v193\n"
"s_add_u32 m0, s78, 22272\n"
"v_mfma_f32_16x16x32_bf16 v[204:207], a[208:211], a[64:67], v[232:235]\n"
"v_exp_f32_e32 v194, v194\n"
"v_mfma_f32_16x16x32_bf16 v[204:207], a[212:215], a[68:71], v[204:207]\n"
"v_exp_f32_e32 v195, v195\n"
"v_mfma_f32_16x16x32_bf16 v[204:207], a[216:219], a[72:75], v[204:207]\n"
"v_cvt_pk_bf16_f32 v209, v194, v195\n"
"v_mfma_f32_16x16x32_bf16 v[204:207], a[220:223], a[76:79], v[204:207]\n"
"buffer_load_dwordx4 v245, s[40:43], s73 offen offset:256 lds\n"
"v_mfma_f32_16x16x32_bf16 v[96:99], a[128:131], v[248:251], v[96:99]\n"
"v_exp_f32_e32 v196, v196\n"
"v_mfma_f32_16x16x32_bf16 v[96:99], a[224:227], v[252:255], v[96:99]\n"
"v_exp_f32_e32 v197, v197\n"
"v_mfma_f32_16x16x32_bf16 v[100:103], a[132:135], v[248:251], v[100:103]\n"
"v_cvt_pk_bf16_f32 v210, v196, v197\n"
"s_add_u32 m0, s78, 23488\n"
"s_add_u32 s64, s73, 10240\n"
"v_mfma_f32_16x16x32_bf16 v[100:103], a[228:231], v[252:255], v[100:103]\n"
"v_exp_f32_e32 v198, v198\n"
"v_mfma_f32_16x16x32_bf16 v[104:107], a[136:139], v[248:251], v[104:107]\n"
"v_exp_f32_e32 v199, v199\n"
"v_mfma_f32_16x16x32_bf16 v[104:107], a[232:235], v[252:255], v[104:107]\n"
"v_cvt_pk_bf16_f32 v211, v198, v199\n"
"v_mfma_f32_16x16x32_bf16 v[108:111], a[140:143], v[248:251], v[108:111]\n"
"buffer_load_dwordx4 v244, s[40:43], s64 offen offset:64 lds\n"
"v_mfma_f32_16x16x32_bf16 v[108:111], a[236:239], v[252:255], v[108:111]\n"
"s_add_u32 m0, s78, 24384\n"
"v_mfma_f32_16x16x32_bf16 v[112:115], a[144:147], v[248:251], v[112:115]\n"
"buffer_load_dwordx4 v244, s[40:43], s64 offen offset:192 lds\n"
"v_mfma_f32_16x16x32_bf16 v[112:115], a[240:243], v[252:255], v[112:115]\n"
"v_exp_f32_e32 v200, v200\n"
"v_mfma_f32_16x16x32_bf16 v[116:119], a[148:151], v[248:251], v[116:119]\n"
"v_exp_f32_e32 v201, v201\n"
"v_mfma_f32_16x16x32_bf16 v[116:119], a[244:247], v[252:255], v[116:119]\n"
"v_cvt_pk_bf16_f32 v212, v200, v201\n"
"s_add_u32 s73, s73, 81920\n"
"v_mfma_f32_16x16x32_bf16 v[120:123], a[152:155], v[248:251], v[120:123]\n"
"v_exp_f32_e32 v202, v202\n"
"v_mfma_f32_16x16x32_bf16 v[120:123], a[248:251], v[252:255], v[120:123]\n"
"v_exp_f32_e32 v203, v203\n"
"v_mfma_f32_16x16x32_bf16 v[124:127], a[156:159], v[248:251], v[124:127]\n"
"v_cvt_pk_bf16_f32 v213, v202, v203\n"
"v_mfma_f32_16x16x32_bf16 v[124:127], a[252:255], v[252:255], v[124:127]\n"
"s_cmp_eq_u32 s73, 1310720\n"
"s_cselect_b32 s73, 0, s73\n"
"v_mfma_f32_16x16x32_bf16 a[172:175], a[184:187], v[248:251], a[172:175]\n"
"s_add_u32 m0, s78, 40960\n"
"v_mfma_f32_16x16x32_bf16 a[172:175], a[184:187], v[252:255], a[172:175]\n"
"buffer_load_dwordx4 v244, s[40:43], s73 offen lds\n"
"s_add_u32 m0, s78, 41856\n"
"v_mfma_f32_16x16x32_bf16 v[192:195], a[96:99], a[80:83], v[236:239]\n"
"v_exp_f32_e32 v204, v204\n"
"buffer_load_dwordx4 v244, s[40:43], s73 offen offset:128 lds\n"
"v_mfma_f32_16x16x32_bf16 v[192:195], a[100:103], a[84:87], v[192:195]\n"
"v_exp_f32_e32 v205, v205\n"
"s_add_u32 m0, s78, 42752\n"
"v_mfma_f32_16x16x32_bf16 v[192:195], a[104:107], a[88:91], v[192:195]\n"
"v_cvt_pk_bf16_f32 v214, v204, v205\n"
"ds_read_b128 a[96:99], v240 offset:0\n"
"buffer_load_dwordx4 v245, s[40:43], s73 offen offset:256 lds\n"
"v_mfma_f32_16x16x32_bf16 v[192:195], a[108:111], a[92:95], v[192:195]\n"
"v_exp_f32_e32 v206, v206\n"
"s_add_u32 m0, s78, 43968\n"
"s_add_u32 s64, s73, 10240\n"
"v_mfma_f32_16x16x32_bf16 v[196:199], a[112:115], a[80:83], v[236:239]\n"
"v_exp_f32_e32 v207, v207\n"
"buffer_load_dwordx4 v244, s[40:43], s64 offen offset:64 lds\n"
"v_mfma_f32_16x16x32_bf16 v[196:199], a[116:119], a[84:87], v[196:199]\n"
"v_cvt_pk_bf16_f32 v215, v206, v207\n"
"ds_read_b128 a[100:103], v241 offset:0\n"
"v_mfma_f32_16x16x32_bf16 v[196:199], a[120:123], a[88:91], v[196:199]\n"
"ds_read_b128 a[104:107], v240 offset:1024\n"
"ds_read_b128 a[108:111], v241 offset:1024\n"
"v_mfma_f32_16x16x32_bf16 v[196:199], a[124:127], a[92:95], v[196:199]\n"
"ds_read_b128 a[112:115], v240 offset:4096\n"
"ds_read_b128 a[116:119], v241 offset:4096\n"
"v_mfma_f32_16x16x32_bf16 v[200:203], a[192:195], a[80:83], v[236:239]\n"
"ds_read_b128 a[120:123], v240 offset:5120\n"
"s_add_u32 m0, s78, 44864\n"
"s_nop 0\n"
"buffer_load_dwordx4 v244, s[40:43], s64 offen offset:192 lds\n"
"v_mfma_f32_16x16x32_bf16 v[200:203], a[196:199], a[84:87], v[200:203]\n"
"v_exp_f32_e32 v192, v192\n"
"s_add_u32 s73, s73, 81920\n"
"v_mfma_f32_16x16x32_bf16 v[200:203], a[200:203], a[88:91], v[200:203]\n"
"v_exp_f32_e32 v193, v193\n"
"s_cmp_eq_u32 s73, 1310720\n"
"s_cselect_b32 s73, 0, s73\n"
"v_mfma_f32_16x16x32_bf16 v[200:203], a[204:207], a[92:95], v[200:203]\n"
"v_cvt_pk_bf16_f32 v248, v192, v193\n"
"ds_read_b128 a[124:127], v241 offset:5120\n"
"s_add_u32 m0, s78, 61440\n"
"v_mfma_f32_16x16x32_bf16 v[204:207], a[208:211], a[80:83], v[236:239]\n"
"v_exp_f32_e32 v194, v194\n"
"buffer_load_dwordx4 v244, s[40:43], s73 offen lds\n"
"v_mfma_f32_16x16x32_bf16 v[204:207], a[212:215], a[84:87], v[204:207]\n"
"v_exp_f32_e32 v195, v195\n"
"s_add_u32 m0, s78, 62336\n"
"v_mfma_f32_16x16x32_bf16 v[204:207], a[216:219], a[88:91], v[204:207]\n"
"v_cvt_pk_bf16_f32 v249, v194, v195\n"
"s_cmp_eq_u32 s69, 131072\n"
"s_cselect_b32 s68, -131072, s81\n"
"v_mfma_f32_16x16x32_bf16 v[204:207], a[220:223], a[92:95], v[204:207]\n"
"s_add_u32 s69, s69, s68\n"
"v_add_u32_e32 v240, s68, v240\n"
"v_add_u32_e32 v241, s68, v241\n"
"v_mfma_f32_16x16x32_bf16 v[128:131], a[128:131], v[208:211], v[128:131]\n"
"v_exp_f32_e32 v196, v196\n"
"buffer_load_dwordx4 v244, s[40:43], s73 offen offset:128 lds\n"
"v_mfma_f32_16x16x32_bf16 v[128:131], a[224:227], v[212:215], v[128:131]\n"
"v_exp_f32_e32 v197, v197\n"
"s_add_u32 m0, s78, 63232\n"
"v_mfma_f32_16x16x32_bf16 v[132:135], a[132:135], v[208:211], v[132:135]\n"
"v_cvt_pk_bf16_f32 v250, v196, v197\n"
"ds_read_b128 a[192:195], v240 offset:0\n"
"buffer_load_dwordx4 v245, s[40:43], s73 offen offset:256 lds\n"
"v_mfma_f32_16x16x32_bf16 v[132:135], a[228:231], v[212:215], v[132:135]\n"
"v_exp_f32_e32 v198, v198\n"
"s_add_u32 m0, s78, 64448\n"
"s_add_u32 s64, s73, 10240\n"
"v_mfma_f32_16x16x32_bf16 v[136:139], a[136:139], v[208:211], v[136:139]\n"
"v_exp_f32_e32 v199, v199\n"
"buffer_load_dwordx4 v244, s[40:43], s64 offen offset:64 lds\n"
"v_mfma_f32_16x16x32_bf16 v[136:139], a[232:235], v[212:215], v[136:139]\n"
"v_cvt_pk_bf16_f32 v251, v198, v199\n"
"ds_read_b128 a[196:199], v241 offset:0\n"
"v_mfma_f32_16x16x32_bf16 v[140:143], a[140:143], v[208:211], v[140:143]\n"
"ds_read_b128 a[200:203], v240 offset:1024\n"
"ds_read_b128 a[204:207], v241 offset:1024\n"
"s_add_u32 m0, s78, 65344\n"
"v_mfma_f32_16x16x32_bf16 v[140:143], a[236:239], v[212:215], v[140:143]\n"
"ds_read_b128 a[208:211], v240 offset:4096\n"
"ds_read_b128 a[212:215], v241 offset:4096\n"
"buffer_load_dwordx4 v244, s[40:43], s64 offen offset:192 lds\n"
"v_mfma_f32_16x16x32_bf16 v[144:147], a[144:147], v[208:211], v[144:147]\n"
"ds_read_b128 a[216:219], v240 offset:5120\n"
"ds_read_b128 a[220:223], v241 offset:5120\n"
"v_mfma_f32_16x16x32_bf16 v[144:147], a[240:243], v[212:215], v[144:147]\n"
"v_exp_f32_e32 v200, v200\n"
"v_mfma_f32_16x16x32_bf16 v[148:151], a[148:151], v[208:211], v[148:151]\n"
"v_exp_f32_e32 v201, v201\n"
"v_mfma_f32_16x16x32_bf16 v[148:151], a[244:247], v[212:215], v[148:151]\n"
"v_cvt_pk_bf16_f32 v252, v200, v201\n"
"s_add_u32 s73, s73, 81920\n"
"s_cmp_eq_u32 s73, 1310720\n"
"s_cselect_b32 s73, 0, s73\n"
"v_mfma_f32_16x16x32_bf16 v[152:155], a[152:155], v[208:211], v[152:155]\n"
"v_exp_f32_e32 v202, v202\n"
"v_mfma_f32_16x16x32_bf16 v[152:155], a[248:251], v[212:215], v[152:155]\n"
"v_exp_f32_e32 v203, v203\n"
"v_mfma_f32_16x16x32_bf16 v[156:159], a[156:159], v[208:211], v[156:159]\n"
"v_cvt_pk_bf16_f32 v253, v202, v203\n"
"s_memrealtime s[74:75]\n"
"v_mfma_f32_16x16x32_bf16 v[156:159], a[252:255], v[212:215], v[156:159]\n"
"s_cmp_eq_u32 s69, 131072\n"
"s_cselect_b32 s68, -131072, s81\n"
"v_mfma_f32_16x16x32_bf16 a[176:179], a[184:187], v[208:211], a[176:179]\n"
"s_add_u32 s69, s69, s68\n"
"v_mfma_f32_16x16x32_bf16 a[176:179], a[184:187], v[212:215], a[176:179]\n"
"v_add_u32_e32 v240, s68, v240\n"
"v_add_u32_e32 v241, s68, v241\n"
"s_waitcnt vmcnt(20) lgkmcnt(0)\n"
"s_barrier\n"
"v_mfma_f32_16x16x32_bf16 v[192:195], a[96:99], a[0:3], v[216:219]\n"
"v_exp_f32_e32 v204, v204\n"
"v_mfma_f32_16x16x32_bf16 v[192:195], a[100:103], a[4:7], v[192:195]\n"
"v_exp_f32_e32 v205, v205\n"
"v_mfma_f32_16x16x32_bf16 v[192:195], a[104:107], a[8:11], v[192:195]\n"
"v_cvt_pk_bf16_f32 v254, v204, v205\n"
"v_mfma_f32_16x16x32_bf16 v[192:195], a[108:111], a[12:15], v[192:195]\n"
"v_exp_f32_e32 v206, v206\n"
"v_mfma_f32_16x16x32_bf16 v[196:199], a[112:115], a[0:3], v[216:219]\n"
"v_exp_f32_e32 v207, v207\n"
"v_mfma_f32_16x16x32_bf16 v[196:199], a[116:119], a[4:7], v[196:199]\n"
"v_cvt_pk_bf16_f32 v255, v206, v207\n"
"v_mfma_f32_16x16x32_bf16 v[196:199], a[120:123], a[8:11], v[196:199]\n"
"s_cmp_eq_u32 s95, 131072\n"
"s_cselect_b32 s68, -131072, s81\n"
"v_mfma_f32_16x16x32_bf16 v[196:199], a[124:127], a[12:15], v[196:199]\n"
"s_add_u32 s95, s95, s68\n"
"v_mfma_f32_16x16x32_bf16 v[200:203], a[192:195], a[0:3], v[216:219]\n"
"v_mfma_f32_16x16x32_bf16 v[200:203], a[196:199], a[4:7], v[200:203]\n"
"v_exp_f32_e32 v192, v192\n"
"v_mfma_f32_16x16x32_bf16 v[200:203], a[200:203], a[8:11], v[200:203]\n"
"v_exp_f32_e32 v193, v193\n"
"v_mfma_f32_16x16x32_bf16 v[200:203], a[204:207], a[12:15], v[200:203]\n"
"v_cvt_pk_bf16_f32 v208, v192, v193\n"
"v_mfma_f32_16x16x32_bf16 v[204:207], a[208:211], a[0:3], v[216:219]\n"
"v_exp_f32_e32 v194, v194\n"
"v_mfma_f32_16x16x32_bf16 v[204:207], a[212:215], a[4:7], v[204:207]\n"
"v_exp_f32_e32 v195, v195\n"
"v_mfma_f32_16x16x32_bf16 v[204:207], a[216:219], a[8:11], v[204:207]\n"
"v_cvt_pk_bf16_f32 v209, v194, v195\n"
"v_mfma_f32_16x16x32_bf16 v[204:207], a[220:223], a[12:15], v[204:207]\n"
"v_mfma_f32_16x16x32_bf16 v[160:163], a[128:131], v[248:251], v[160:163]\n"
"v_exp_f32_e32 v196, v196\n"
"v_mfma_f32_16x16x32_bf16 v[160:163], a[224:227], v[252:255], v[160:163]\n"
"v_exp_f32_e32 v197, v197\n"
"ds_read_b64_tr_b16 a[128:129], v242 offset:8192\n"
"v_mfma_f32_16x16x32_bf16 v[164:167], a[132:135], v[248:251], v[164:167]\n"
"v_cvt_pk_bf16_f32 v210, v196, v197\n"
"ds_read_b64_tr_b16 a[130:131], v242 offset:12288\n"
"v_mfma_f32_16x16x32_bf16 v[164:167], a[228:231], v[252:255], v[164:167]\n"
"v_exp_f32_e32 v198, v198\n"
"ds_read_b64_tr_b16 a[132:133], v243 offset:8192\n"
"v_mfma_f32_16x16x32_bf16 v[168:171], a[136:139], v[248:251], v[168:171]\n"
"v_exp_f32_e32 v199, v199\n"
"ds_read_b64_tr_b16 a[134:135], v243 offset:12288\n"
"v_mfma_f32_16x16x32_bf16 v[168:171], a[232:235], v[252:255], v[168:171]\n"
"v_cvt_pk_bf16_f32 v211, v198, v199\n"
"ds_read_b64_tr_b16 a[136:137], v242 offset:8704\n"
"v_mfma_f32_16x16x32_bf16 v[172:175], a[140:143], v[248:251], v[172:175]\n"
"ds_read_b64_tr_b16 a[138:139], v242 offset:12800\n"
"v_mfma_f32_16x16x32_bf16 v[172:175], a[236:239], v[252:255], v[172:175]\n"
"ds_read_b64_tr_b16 a[140:141], v243 offset:8704\n"
"v_mfma_f32_16x16x32_bf16 v[176:179], a[144:147], v[248:251], v[176:179]\n"
"ds_read_b64_tr_b16 a[142:143], v243 offset:12800\n"
"v_mfma_f32_16x16x32_bf16 v[176:179], a[240:243], v[252:255], v[176:179]\n"
"v_exp_f32_e32 v200, v200\n"
"ds_read_b64_tr_b16 a[144:145], v242 offset:9216\n"
"v_mfma_f32_16x16x32_bf16 v[180:183], a[148:151], v[248:251], v[180:183]\n"
"v_exp_f32_e32 v201, v201\n"
"ds_read_b64_tr_b16 a[146:147], v242 offset:13312\n"
"v_mfma_f32_16x16x32_bf16 v[180:183], a[244:247], v[252:255], v[180:183]\n"
"v_cvt_pk_bf16_f32 v212, v200, v201\n"
"ds_read_b64_tr_b16 a[148:149], v243 offset:9216\n"
"v_mfma_f32_16x16x32_bf16 v[184:187], a[152:155], v[248:251], v[184:187]\n"
"v_exp_f32_e32 v202, v202\n"
"ds_read_b64_tr_b16 a[150:151], v243 offset:13312\n"
"v_mfma_f32_16x16x32_bf16 v[184:187], a[248:251], v[252:255], v[184:187]\n"
"v_exp_f32_e32 v203, v203\n"
"ds_read_b64_tr_b16 a[152:153], v242 offset:9728\n"
"v_mfma_f32_16x16x32_bf16 v[188:191], a[156:159], v[248:251], v[188:191]\n"
"v_cvt_pk_bf16_f32 v213, v202, v203\n"
"ds_read_b64_tr_b16 a[154:155], v242 offset:13824\n"
"v_mfma_f32_16x16x32_bf16 v[188:191], a[252:255], v[252:255], v[188:191]\n"
"ds_read_b64_tr_b16 a[156:157], v243 offset:9728\n"
"v_mfma_f32_16x16x32_bf16 a[180:183], a[184:187], v[248:251], a[180:183]\n"
"ds_read_b64_tr_b16 a[158:159], v243 offset:13824\n"
"v_mfma_f32_16x16x32_bf16 a[180:183], a[184:187], v[252:255], a[180:183]\n"
"v_add_u32_e32 v242, s68, v242\n"
"v_add_u32_e32 v243, s68, v243\n"
"v_mfma_f32_16x16x32_bf16 v[192:195], a[96:99], a[16:19], v[220:223]\n"
"v_exp_f32_e32 v204, v204\n"
"ds_read_b64_tr_b16 a[224:225], v242 offset:8192\n"
"v_mfma_f32_16x16x32_bf16 v[192:195], a[100:103], a[20:23], v[192:195]\n"
"v_exp_f32_e32 v205, v205\n"
"ds_read_b64_tr_b16 a[226:227], v242 offset:12288\n"
"v_mfma_f32_16x16x32_bf16 v[192:195], a[104:107], a[24:27], v[192:195]\n"
"v_cvt_pk_bf16_f32 v214, v204, v205\n"
"ds_read_b64_tr_b16 a[228:229], v243 offset:8192\n"
"v_mfma_f32_16x16x32_bf16 v[192:195], a[108:111], a[28:31], v[192:195]\n"
"v_exp_f32_e32 v206, v206\n"
"ds_read_b64_tr_b16 a[230:231], v243 offset:12288\n"
"v_mfma_f32_16x16x32_bf16 v[196:199], a[112:115], a[16:19], v[220:223]\n"
"v_exp_f32_e32 v207, v207\n"
"ds_read_b64_tr_b16 a[232:233], v242 offset:8704\n"
"v_mfma_f32_16x16x32_bf16 v[196:199], a[116:119], a[20:23], v[196:199]\n"
"v_cvt_pk_bf16_f32 v215, v206, v207\n"
"ds_read_b64_tr_b16 a[234:235], v242 offset:12800\n"
"v_mfma_f32_16x16x32_bf16 v[196:199], a[120:123], a[24:27], v[196:199]\n"
"ds_read_b64_tr_b16 a[236:237], v243 offset:8704\n"
"v_mfma_f32_16x16x32_bf16 v[196:199], a[124:127], a[28:31], v[196:199]\n"
"ds_read_b64_tr_b16 a[238:239], v243 offset:12800\n"
"v_mfma_f32_16x16x32_bf16 v[200:203], a[192:195], a[16:19], v[220:223]\n"
"ds_read_b64_tr_b16 a[240:241], v242 offset:9216\n"
"v_mfma_f32_16x16x32_bf16 v[200:203], a[196:199], a[20:23], v[200:203]\n"
"v_exp_f32_e32 v192, v192\n"
"ds_read_b64_tr_b16 a[242:243], v242 offset:13312\n"
"v_mfma_f32_16x16x32_bf16 v[200:203], a[200:203], a[24:27], v[200:203]\n"
"v_exp_f32_e32 v193, v193\n"
"ds_read_b64_tr_b16 a[244:245], v243 offset:9216\n"
"v_mfma_f32_16x16x32_bf16 v[200:203], a[204:207], a[28:31], v[200:203]\n"
"v_cvt_pk_bf16_f32 v248, v192, v193\n"
"ds_read_b64_tr_b16 a[246:247], v243 offset:13312\n"
"v_mfma_f32_16x16x32_bf16 v[204:207], a[208:211], a[16:19], v[220:223]\n"
"v_exp_f32_e32 v194, v194\n"
"ds_read_b64_tr_b16 a[248:249], v242 offset:9728\n"
"v_mfma_f32_16x16x32_bf16 v[204:207], a[212:215], a[20:23], v[204:207]\n"
"v_exp_f32_e32 v195, v195\n"
"ds_read_b64_tr_b16 a[250:251], v242 offset:13824\n"
"v_mfma_f32_16x16x32_bf16 v[204:207], a[216:219], a[24:27], v[204:207]\n"
"v_cvt_pk_bf16_f32 v249, v194, v195\n"
"ds_read_b64_tr_b16 a[252:253], v243 offset:9728\n"
"v_mfma_f32_16x16x32_bf16 v[204:207], a[220:223], a[28:31], v[204:207]\n"
"ds_read_b64_tr_b16 a[254:255], v243 offset:13824\n"
"s_waitcnt lgkmcnt(8)\n"
"v_mfma_f32_16x16x32_bf16 v[0:3], a[128:131], v[208:211], v[0:3]\n"
"v_exp_f32_e32 v196, v196\n"
"v_mfma_f32_16x16x32_bf16 v[0:3], a[224:227], v[212:215], v[0:3]\n"
"v_exp_f32_e32 v197, v197\n"
"v_mfma_f32_16x16x32_bf16 v[4:7], a[132:135], v[208:211], v[4:7]\n"
"v_cvt_pk_bf16_f32 v250, v196, v197\n"
"v_mfma_f32_16x16x32_bf16 v[4:7], a[228:231], v[212:215], v[4:7]\n"
"v_exp_f32_e32 v198, v198\n"
"v_mfma_f32_16x16x32_bf16 v[8:11], a[136:139], v[208:211], v[8:11]\n"
"v_exp_f32_e32 v199, v199\n"
"v_mfma_f32_16x16x32_bf16 v[8:11], a[232:235], v[212:215], v[8:11]\n"
"v_cvt_pk_bf16_f32 v251, v198, v199\n"
"v_mfma_f32_16x16x32_bf16 v[12:15], a[140:143], v[208:211], v[12:15]\n"
"s_cmp_eq_u32 s95, 131072\n"
"s_cselect_b32 s68, -131072, s81\n"
"v_mfma_f32_16x16x32_bf16 v[12:15], a[236:239], v[212:215], v[12:15]\n"
"s_waitcnt lgkmcnt(0)\n"
"s_add_u32 s95, s95, s68\n"
"v_mfma_f32_16x16x32_bf16 v[16:19], a[144:147], v[208:211], v[16:19]\n"
"v_add_u32_e32 v242, s68, v242\n"
"v_add_u32_e32 v243, s68, v243\n"
"s_waitcnt lgkmcnt(0)\n"
"s_barrier\n"
"s_xor_b32 s70, s60, 2\n"
"s_mul_i32 s70, s70, 12288\n"
"s_add_u32 s70, s70, 114688\n"
"v_add_u32_e32 v240, s70, v246\n"
"v_mfma_f32_16x16x32_bf16 v[16:19], a[240:243], v[212:215], v[16:19]\n"
"v_exp_f32_e32 v200, v200\n"
"v_mfma_f32_16x16x32_bf16 v[20:23], a[148:151], v[208:211], v[20:23]\n"
"v_exp_f32_e32 v201, v201\n"
"v_mfma_f32_16x16x32_bf16 v[20:23], a[244:247], v[212:215], v[20:23]\n"
"v_cvt_pk_bf16_f32 v252, v200, v201\n"
"s_add_u32 m0, s78, 81920\n"
"v_mfma_f32_16x16x32_bf16 v[24:27], a[152:155], v[208:211], v[24:27]\n"
"v_exp_f32_e32 v202, v202\n"
"v_mfma_f32_16x16x32_bf16 v[24:27], a[248:251], v[212:215], v[24:27]\n"
"v_exp_f32_e32 v203, v203\n"
"v_mfma_f32_16x16x32_bf16 v[28:31], a[156:159], v[208:211], v[28:31]\n"
"v_cvt_pk_bf16_f32 v253, v202, v203\n"
"v_mfma_f32_16x16x32_bf16 v[28:31], a[252:255], v[212:215], v[28:31]\n"
"buffer_load_dwordx4 v244, s[40:43], s73 offen lds\n"
"v_mfma_f32_16x16x32_bf16 a[160:163], a[184:187], v[208:211], a[160:163]\n"
"s_add_u32 m0, s78, 82816\n"
"v_mfma_f32_16x16x32_bf16 a[160:163], a[184:187], v[212:215], a[160:163]\n"
"buffer_load_dwordx4 v244, s[40:43], s73 offen offset:128 lds\n"
"s_add_u32 m0, s78, 83712\n"
"v_mfma_f32_16x16x32_bf16 v[192:195], a[96:99], a[32:35], v[224:227]\n"
"v_exp_f32_e32 v204, v204\n"
"buffer_load_dwordx4 v245, s[40:43], s73 offen offset:256 lds\n"
"v_mfma_f32_16x16x32_bf16 v[192:195], a[100:103], a[36:39], v[192:195]\n"
"v_exp_f32_e32 v205, v205\n"
"s_add_u32 m0, s78, 84928\n"
"s_add_u32 s64, s73, 10240\n"
"buffer_load_dwordx4 v244, s[40:43], s64 offen offset:64 lds\n"
"s_add_u32 m0, s78, 85824\n"
"s_nop 0\n"
"buffer_load_dwordx4 v244, s[40:43], s64 offen offset:192 lds\n"
"s_add_u32 s73, s73, 81920\n"
"s_cmp_eq_u32 s73, 1310720\n"
"s_cselect_b32 s73, 0, s73\n"
"v_mfma_f32_16x16x32_bf16 v[192:195], a[104:107], a[40:43], v[192:195]\n"
"v_cvt_pk_bf16_f32 v254, v204, v205\n"
"v_mfma_f32_16x16x32_bf16 v[192:195], a[108:111], a[44:47], v[192:195]\n"
"v_exp_f32_e32 v206, v206\n"
"v_mfma_f32_16x16x32_bf16 v[196:199], a[112:115], a[32:35], v[224:227]\n"
"v_exp_f32_e32 v207, v207\n"
"v_mfma_f32_16x16x32_bf16 v[196:199], a[116:119], a[36:39], v[196:199]\n"
"v_cvt_pk_bf16_f32 v255, v206, v207\n"
"v_accvgpr_read_b32 v218, a160\n"
"v_mfma_f32_16x16x32_bf16 v[196:199], a[120:123], a[40:43], v[196:199]\n"
"v_rcp_f32_e32 v216, v218\n"
"v_mfma_f32_16x16x32_bf16 v[196:199], a[124:127], a[44:47], v[196:199]\n"
"v_mov_b32_e32 v219, v218\n"
"v_pk_mul_f32 v[0:1], v[0:1], v[216:217] op_sel_hi:[1,0]\n"
"v_mfma_f32_16x16x32_bf16 v[200:203], a[192:195], a[32:35], v[224:227]\n"
"v_pk_mul_f32 v[2:3], v[2:3], v[216:217] op_sel_hi:[1,0]\n"
"v_cvt_pk_bf16_f32 v0, v0, v1\n"
"v_mfma_f32_16x16x32_bf16 v[200:203], a[196:199], a[36:39], v[200:203]\n"
"v_exp_f32_e32 v192, v192\n"
"v_mfma_f32_16x16x32_bf16 v[200:203], a[200:203], a[40:43], v[200:203]\n"
"v_exp_f32_e32 v193, v193\n"
"v_mfma_f32_16x16x32_bf16 v[200:203], a[204:207], a[44:47], v[200:203]\n"
"v_cvt_pk_bf16_f32 v208, v192, v193\n"
"v_cvt_pk_bf16_f32 v1, v2, v3\n"
"v_mfma_f32_16x16x32_bf16 v[204:207], a[208:211], a[32:35], v[224:227]\n"
"v_exp_f32_e32 v194, v194\n"
"v_mfma_f32_16x16x32_bf16 v[204:207], a[212:215], a[36:39], v[204:207]\n"
"v_exp_f32_e32 v195, v195\n"
"v_mfma_f32_16x16x32_bf16 v[204:207], a[216:219], a[40:43], v[204:207]\n"
"v_cvt_pk_bf16_f32 v209, v194, v195\n"
"v_pk_mul_f32 v[4:5], v[4:5], v[216:217] op_sel_hi:[1,0]\n"
"v_mfma_f32_16x16x32_bf16 v[204:207], a[220:223], a[44:47], v[204:207]\n"
"v_pk_mul_f32 v[6:7], v[6:7], v[216:217] op_sel_hi:[1,0]\n"
"v_cvt_pk_bf16_f32 v2, v4, v5\n"
"v_mfma_f32_16x16x32_bf16 v[32:35], a[128:131], v[248:251], v[32:35]\n"
"v_exp_f32_e32 v196, v196\n"
"v_mfma_f32_16x16x32_bf16 v[32:35], a[224:227], v[252:255], v[32:35]\n"
"v_exp_f32_e32 v197, v197\n"
"v_mfma_f32_16x16x32_bf16 v[36:39], a[132:135], v[248:251], v[36:39]\n"
"v_cvt_pk_bf16_f32 v210, v196, v197\n"
"v_cvt_pk_bf16_f32 v3, v6, v7\n"
"v_mfma_f32_16x16x32_bf16 v[36:39], a[228:231], v[252:255], v[36:39]\n"
"v_exp_f32_e32 v198, v198\n"
"v_mfma_f32_16x16x32_bf16 v[40:43], a[136:139], v[248:251], v[40:43]\n"
"v_exp_f32_e32 v199, v199\n"
"v_mfma_f32_16x16x32_bf16 v[40:43], a[232:235], v[252:255], v[40:43]\n"
"v_cvt_pk_bf16_f32 v211, v198, v199\n"
"v_mfma_f32_16x16x32_bf16 v[44:47], a[140:143], v[248:251], v[44:47]\n"
"s_add_u32 s81, s80, 0\n"
"buffer_store_dwordx4 v[0:3], v246, s[56:59], s81 offen sc1\n"
"v_mfma_f32_16x16x32_bf16 v[44:47], a[236:239], v[252:255], v[44:47]\n"
"v_pk_mul_f32 v[8:9], v[8:9], v[216:217] op_sel_hi:[1,0]\n"
"v_pk_mul_f32 v[10:11], v[10:11], v[216:217] op_sel_hi:[1,0]\n"
"v_mfma_f32_16x16x32_bf16 v[48:51], a[144:147], v[248:251], v[48:51]\n"
"v_cvt_pk_bf16_f32 v4, v8, v9\n"
"v_cvt_pk_bf16_f32 v5, v10, v11\n"
"v_mfma_f32_16x16x32_bf16 v[48:51], a[240:243], v[252:255], v[48:51]\n"
"v_exp_f32_e32 v200, v200\n"
"v_mfma_f32_16x16x32_bf16 v[52:55], a[148:151], v[248:251], v[52:55]\n"
"v_exp_f32_e32 v201, v201\n"
"v_mfma_f32_16x16x32_bf16 v[52:55], a[244:247], v[252:255], v[52:55]\n"
"v_cvt_pk_bf16_f32 v212, v200, v201\n"
"v_pk_mul_f32 v[12:13], v[12:13], v[216:217] op_sel_hi:[1,0]\n"
"v_mfma_f32_16x16x32_bf16 v[56:59], a[152:155], v[248:251], v[56:59]\n"
"v_exp_f32_e32 v202, v202\n"
"v_mfma_f32_16x16x32_bf16 v[56:59], a[248:251], v[252:255], v[56:59]\n"
"v_exp_f32_e32 v203, v203\n"
"v_mfma_f32_16x16x32_bf16 v[60:63], a[156:159], v[248:251], v[60:63]\n"
"v_cvt_pk_bf16_f32 v213, v202, v203\n"
"v_pk_mul_f32 v[14:15], v[14:15], v[216:217] op_sel_hi:[1,0]\n"
"v_mfma_f32_16x16x32_bf16 v[60:63], a[252:255], v[252:255], v[60:63]\n"
"v_cvt_pk_bf16_f32 v6, v12, v13\n"
"v_cvt_pk_bf16_f32 v7, v14, v15\n"
"v_mfma_f32_16x16x32_bf16 a[164:167], a[184:187], v[248:251], a[164:167]\n"
"s_add_u32 s81, s80, 12288\n"
"buffer_store_dwordx4 v[4:7], v246, s[56:59], s81 offen sc1\n"
"v_mfma_f32_16x16x32_bf16 a[164:167], a[184:187], v[252:255], a[164:167]\n"
"v_pk_mul_f32 v[16:17], v[16:17], v[216:217] op_sel_hi:[1,0]\n"
"v_pk_mul_f32 v[18:19], v[18:19], v[216:217] op_sel_hi:[1,0]\n"
"v_mfma_f32_16x16x32_bf16 v[192:195], a[96:99], a[48:51], v[228:231]\n"
"v_exp_f32_e32 v204, v204\n"
"v_mfma_f32_16x16x32_bf16 v[192:195], a[100:103], a[52:55], v[192:195]\n"
"v_exp_f32_e32 v205, v205\n"
"v_mfma_f32_16x16x32_bf16 v[192:195], a[104:107], a[56:59], v[192:195]\n"
"v_cvt_pk_bf16_f32 v214, v204, v205\n"
"v_cvt_pk_bf16_f32 v8, v16, v17\n"
"v_cvt_pk_bf16_f32 v9, v18, v19\n"
"v_pk_mul_f32 v[20:21], v[20:21], v[216:217] op_sel_hi:[1,0]\n"
"v_pk_mul_f32 v[22:23], v[22:23], v[216:217] op_sel_hi:[1,0]\n"
"v_cvt_pk_bf16_f32 v10, v20, v21\n"
"v_cvt_pk_bf16_f32 v11, v22, v23\n"
"s_add_u32 s81, s80, 24576\n"
"buffer_store_dwordx4 v[8:11], v246, s[56:59], s81 offen sc1\n"
"v_pk_mul_f32 v[24:25], v[24:25], v[216:217] op_sel_hi:[1,0]\n"
"v_pk_mul_f32 v[26:27], v[26:27], v[216:217] op_sel_hi:[1,0]\n"
"v_cvt_pk_bf16_f32 v12, v24, v25\n"
"v_cvt_pk_bf16_f32 v13, v26, v27\n"
"v_pk_mul_f32 v[28:29], v[28:29], v[216:217] op_sel_hi:[1,0]\n"
"v_pk_mul_f32 v[30:31], v[30:31], v[216:217] op_sel_hi:[1,0]\n"
"v_cvt_pk_bf16_f32 v14, v28, v29\n"
"v_cvt_pk_bf16_f32 v15, v30, v31\n"
"s_add_u32 s81, s80, 36864\n"
"buffer_store_dwordx4 v[12:15], v246, s[56:59], s81 offen sc1\n"
"v_mfma_f32_16x16x32_bf16 v[192:195], a[108:111], a[60:63], v[192:195]\n"
"v_exp_f32_e32 v206, v206\n"
"v_mfma_f32_16x16x32_bf16 v[196:199], a[112:115], a[48:51], v[228:231]\n"
"v_exp_f32_e32 v207, v207\n"
"v_mfma_f32_16x16x32_bf16 v[196:199], a[116:119], a[52:55], v[196:199]\n"
"v_cvt_pk_bf16_f32 v215, v206, v207\n"
"v_accvgpr_read_b32 v222, a164\n"
"v_mfma_f32_16x16x32_bf16 v[196:199], a[120:123], a[56:59], v[196:199]\n"
"v_rcp_f32_e32 v220, v222\n"
"v_mfma_f32_16x16x32_bf16 v[196:199], a[124:127], a[60:63], v[196:199]\n"
"v_add_f32_e32 v219, v219, v222\n"
"v_pk_mul_f32 v[32:33], v[32:33], v[220:221] op_sel_hi:[1,0]\n"
"v_mfma_f32_16x16x32_bf16 v[200:203], a[192:195], a[48:51], v[228:231]\n"
"v_pk_mul_f32 v[34:35], v[34:35], v[220:221] op_sel_hi:[1,0]\n"
"v_cvt_pk_bf16_f32 v32, v32, v33\n"
"v_mfma_f32_16x16x32_bf16 v[200:203], a[196:199], a[52:55], v[200:203]\n"
"v_exp_f32_e32 v192, v192\n"
"v_mfma_f32_16x16x32_bf16 v[200:203], a[200:203], a[56:59], v[200:203]\n"
"v_exp_f32_e32 v193, v193\n"
"v_mfma_f32_16x16x32_bf16 v[200:203], a[204:207], a[60:63], v[200:203]\n"
"v_cvt_pk_bf16_f32 v248, v192, v193\n"
"v_cvt_pk_bf16_f32 v33, v34, v35\n"
"v_mfma_f32_16x16x32_bf16 v[204:207], a[208:211], a[48:51], v[228:231]\n"
"v_exp_f32_e32 v194, v194\n"
"v_mfma_f32_16x16x32_bf16 v[204:207], a[212:215], a[52:55], v[204:207]\n"
"v_exp_f32_e32 v195, v195\n"
"v_mfma_f32_16x16x32_bf16 v[204:207], a[216:219], a[56:59], v[204:207]\n"
"v_cvt_pk_bf16_f32 v249, v194, v195\n"
"v_pk_mul_f32 v[36:37], v[36:37], v[220:221] op_sel_hi:[1,0]\n"
"v_mfma_f32_16x16x32_bf16 v[204:207], a[220:223], a[60:63], v[204:207]\n"
"v_pk_mul_f32 v[38:39], v[38:39], v[220:221] op_sel_hi:[1,0]\n"
"v_cvt_pk_bf16_f32 v34, v36, v37\n"
"v_mfma_f32_16x16x32_bf16 v[64:67], a[128:131], v[208:211], v[64:67]\n"
"v_exp_f32_e32 v196, v196\n"
"v_mfma_f32_16x16x32_bf16 v[64:67], a[224:227], v[212:215], v[64:67]\n"
"v_exp_f32_e32 v197, v197\n"
"v_mfma_f32_16x16x32_bf16 v[68:71], a[132:135], v[208:211], v[68:71]\n"
"v_cvt_pk_bf16_f32 v250, v196, v197\n"
"v_cvt_pk_bf16_f32 v35, v38, v39\n"
"v_mfma_f32_16x16x32_bf16 v[68:71], a[228:231], v[212:215], v[68:71]\n"
"v_exp_f32_e32 v198, v198\n"
"v_mfma_f32_16x16x32_bf16 v[72:75], a[136:139], v[208:211], v[72:75]\n"
"v_exp_f32_e32 v199, v199\n"
"v_mfma_f32_16x16x32_bf16 v[72:75], a[232:235], v[212:215], v[72:75]\n"
"v_cvt_pk_bf16_f32 v251, v198, v199\n"
"v_mfma_f32_16x16x32_bf16 v[76:79], a[140:143], v[208:211], v[76:79]\n"
"s_add_u32 s81, s80, 1024\n"
"buffer_store_dwordx4 v[32:35], v246, s[56:59], s81 offen sc1\n"
"v_mfma_f32_16x16x32_bf16 v[76:79], a[236:239], v[212:215], v[76:79]\n"
"v_pk_mul_f32 v[40:41], v[40:41], v[220:221] op_sel_hi:[1,0]\n"
"v_pk_mul_f32 v[42:43], v[42:43], v[220:221] op_sel_hi:[1,0]\n"
"v_mfma_f32_16x16x32_bf16 v[80:83], a[144:147], v[208:211], v[80:83]\n"
"v_cvt_pk_bf16_f32 v36, v40, v41\n"
"v_cvt_pk_bf16_f32 v37, v42, v43\n"
"v_mfma_f32_16x16x32_bf16 v[80:83], a[240:243], v[212:215], v[80:83]\n"
"v_exp_f32_e32 v200, v200\n"
"v_mfma_f32_16x16x32_bf16 v[84:87], a[148:151], v[208:211], v[84:87]\n"
"v_exp_f32_e32 v201, v201\n"
"v_mfma_f32_16x16x32_bf16 v[84:87], a[244:247], v[212:215], v[84:87]\n"
"v_cvt_pk_bf16_f32 v252, v200, v201\n"
"v_pk_mul_f32 v[44:45], v[44:45], v[220:221] op_sel_hi:[1,0]\n"
"v_mfma_f32_16x16x32_bf16 v[88:91], a[152:155], v[208:211], v[88:91]\n"
"v_exp_f32_e32 v202, v202\n"
"v_mfma_f32_16x16x32_bf16 v[88:91], a[248:251], v[212:215], v[88:91]\n"
"v_exp_f32_e32 v203, v203\n"
"v_mfma_f32_16x16x32_bf16 v[92:95], a[156:159], v[208:211], v[92:95]\n"
"v_cvt_pk_bf16_f32 v253, v202, v203\n"
"v_pk_mul_f32 v[46:47], v[46:47], v[220:221] op_sel_hi:[1,0]\n"
"v_mfma_f32_16x16x32_bf16 v[92:95], a[252:255], v[212:215], v[92:95]\n"
"v_cvt_pk_bf16_f32 v38, v44, v45\n"
"v_cvt_pk_bf16_f32 v39, v46, v47\n"
"v_mfma_f32_16x16x32_bf16 a[168:171], a[184:187], v[208:211], a[168:171]\n"
"s_add_u32 s81, s80, 13312\n"
"buffer_store_dwordx4 v[36:39], v246, s[56:59], s81 offen sc1\n"
"v_mfma_f32_16x16x32_bf16 a[168:171], a[184:187], v[212:215], a[168:171]\n"
"v_pk_mul_f32 v[48:49], v[48:49], v[220:221] op_sel_hi:[1,0]\n"
"v_pk_mul_f32 v[50:51], v[50:51], v[220:221] op_sel_hi:[1,0]\n"
"v_mfma_f32_16x16x32_bf16 v[192:195], a[96:99], a[64:67], v[232:235]\n"
"v_exp_f32_e32 v204, v204\n"
"v_mfma_f32_16x16x32_bf16 v[192:195], a[100:103], a[68:71], v[192:195]\n"
"v_exp_f32_e32 v205, v205\n"
"v_mfma_f32_16x16x32_bf16 v[192:195], a[104:107], a[72:75], v[192:195]\n"
"v_cvt_pk_bf16_f32 v254, v204, v205\n"
"v_cvt_pk_bf16_f32 v40, v48, v49\n"
"v_cvt_pk_bf16_f32 v41, v50, v51\n"
"v_pk_mul_f32 v[52:53], v[52:53], v[220:221] op_sel_hi:[1,0]\n"
"v_pk_mul_f32 v[54:55], v[54:55], v[220:221] op_sel_hi:[1,0]\n"
"v_cvt_pk_bf16_f32 v42, v52, v53\n"
"v_cvt_pk_bf16_f32 v43, v54, v55\n"
"s_add_u32 s81, s80, 25600\n"
"buffer_store_dwordx4 v[40:43], v246, s[56:59], s81 offen sc1\n"
"v_pk_mul_f32 v[56:57], v[56:57], v[220:221] op_sel_hi:[1,0]\n"
"v_pk_mul_f32 v[58:59], v[58:59], v[220:221] op_sel_hi:[1,0]\n"
"v_cvt_pk_bf16_f32 v44, v56, v57\n"
"v_cvt_pk_bf16_f32 v45, v58, v59\n"
"v_pk_mul_f32 v[60:61], v[60:61], v[220:221] op_sel_hi:[1,0]\n"
"v_pk_mul_f32 v[62:63], v[62:63], v[220:221] op_sel_hi:[1,0]\n"
"v_cvt_pk_bf16_f32 v46, v60, v61\n"
"v_cvt_pk_bf16_f32 v47, v62, v63\n"
"s_add_u32 s81, s80, 37888\n"
"buffer_store_dwordx4 v[44:47], v246, s[56:59], s81 offen sc1\n"
"v_mfma_f32_16x16x32_bf16 v[192:195], a[108:111], a[76:79], v[192:195]\n"
"v_exp_f32_e32 v206, v206\n"
"v_mfma_f32_16x16x32_bf16 v[196:199], a[112:115], a[64:67], v[232:235]\n"
"v_exp_f32_e32 v207, v207\n"
"v_mfma_f32_16x16x32_bf16 v[196:199], a[116:119], a[68:71], v[196:199]\n"
"v_cvt_pk_bf16_f32 v255, v206, v207\n"
"v_accvgpr_read_b32 v226, a168\n"
"v_mfma_f32_16x16x32_bf16 v[196:199], a[120:123], a[72:75], v[196:199]\n"
"v_rcp_f32_e32 v224, v226\n"
"v_mfma_f32_16x16x32_bf16 v[196:199], a[124:127], a[76:79], v[196:199]\n"
"v_add_f32_e32 v219, v219, v226\n"
"v_pk_mul_f32 v[64:65], v[64:65], v[224:225] op_sel_hi:[1,0]\n"
"v_mfma_f32_16x16x32_bf16 v[200:203], a[192:195], a[64:67], v[232:235]\n"
"v_pk_mul_f32 v[66:67], v[66:67], v[224:225] op_sel_hi:[1,0]\n"
"v_cvt_pk_bf16_f32 v64, v64, v65\n"
"v_mfma_f32_16x16x32_bf16 v[200:203], a[196:199], a[68:71], v[200:203]\n"
"v_exp_f32_e32 v192, v192\n"
"v_mfma_f32_16x16x32_bf16 v[200:203], a[200:203], a[72:75], v[200:203]\n"
"v_exp_f32_e32 v193, v193\n"
"v_mfma_f32_16x16x32_bf16 v[200:203], a[204:207], a[76:79], v[200:203]\n"
"v_cvt_pk_bf16_f32 v208, v192, v193\n"
"v_cvt_pk_bf16_f32 v65, v66, v67\n"
"v_mfma_f32_16x16x32_bf16 v[204:207], a[208:211], a[64:67], v[232:235]\n"
"v_exp_f32_e32 v194, v194\n"
"v_mfma_f32_16x16x32_bf16 v[204:207], a[212:215], a[68:71], v[204:207]\n"
"v_exp_f32_e32 v195, v195\n"
"v_mfma_f32_16x16x32_bf16 v[204:207], a[216:219], a[72:75], v[204:207]\n"
"v_cvt_pk_bf16_f32 v209, v194, v195\n"
"v_pk_mul_f32 v[68:69], v[68:69], v[224:225] op_sel_hi:[1,0]\n"
"v_mfma_f32_16x16x32_bf16 v[204:207], a[220:223], a[76:79], v[204:207]\n"
"v_pk_mul_f32 v[70:71], v[70:71], v[224:225] op_sel_hi:[1,0]\n"
"v_cvt_pk_bf16_f32 v66, v68, v69\n"
"v_mfma_f32_16x16x32_bf16 v[96:99], a[128:131], v[248:251], v[96:99]\n"
"v_exp_f32_e32 v196, v196\n"
"v_mfma_f32_16x16x32_bf16 v[96:99], a[224:227], v[252:255], v[96:99]\n"
"v_exp_f32_e32 v197, v197\n"
"v_mfma_f32_16x16x32_bf16 v[100:103], a[132:135], v[248:251], v[100:103]\n"
"v_cvt_pk_bf16_f32 v210, v196, v197\n"
"v_cvt_pk_bf16_f32 v67, v70, v71\n"
"v_mfma_f32_16x16x32_bf16 v[100:103], a[228:231], v[252:255], v[100:103]\n"
"v_exp_f32_e32 v198, v198\n"
"v_mfma_f32_16x16x32_bf16 v[104:107], a[136:139], v[248:251], v[104:107]\n"
"v_exp_f32_e32 v199, v199\n"
"v_mfma_f32_16x16x32_bf16 v[104:107], a[232:235], v[252:255], v[104:107]\n"
"v_cvt_pk_bf16_f32 v211, v198, v199\n"
"v_mfma_f32_16x16x32_bf16 v[108:111], a[140:143], v[248:251], v[108:111]\n"
"s_add_u32 s81, s80, 2048\n"
"buffer_store_dwordx4 v[64:67], v246, s[56:59], s81 offen sc1\n"
"v_mfma_f32_16x16x32_bf16 v[108:111], a[236:239], v[252:255], v[108:111]\n"
"v_pk_mul_f32 v[72:73], v[72:73], v[224:225] op_sel_hi:[1,0]\n"
"v_pk_mul_f32 v[74:75], v[74:75], v[224:225] op_sel_hi:[1,0]\n"
"v_mfma_f32_16x16x32_bf16 v[112:115], a[144:147], v[248:251], v[112:115]\n"
"v_cvt_pk_bf16_f32 v68, v72, v73\n"
"v_cvt_pk_bf16_f32 v69, v74, v75\n"
"v_mfma_f32_16x16x32_bf16 v[112:115], a[240:243], v[252:255], v[112:115]\n"
"v_exp_f32_e32 v200, v200\n"
"v_mfma_f32_16x16x32_bf16 v[116:119], a[148:151], v[248:251], v[116:119]\n"
"v_exp_f32_e32 v201, v201\n"
"v_mfma_f32_16x16x32_bf16 v[116:119], a[244:247], v[252:255], v[116:119]\n"
"v_cvt_pk_bf16_f32 v212, v200, v201\n"
"v_pk_mul_f32 v[76:77], v[76:77], v[224:225] op_sel_hi:[1,0]\n"
"v_mfma_f32_16x16x32_bf16 v[120:123], a[152:155], v[248:251], v[120:123]\n"
"v_exp_f32_e32 v202, v202\n"
"v_mfma_f32_16x16x32_bf16 v[120:123], a[248:251], v[252:255], v[120:123]\n"
"v_exp_f32_e32 v203, v203\n"
"v_mfma_f32_16x16x32_bf16 v[124:127], a[156:159], v[248:251], v[124:127]\n"
"v_cvt_pk_bf16_f32 v213, v202, v203\n"
"v_pk_mul_f32 v[78:79], v[78:79], v[224:225] op_sel_hi:[1,0]\n"
"v_mfma_f32_16x16x32_bf16 v[124:127], a[252:255], v[252:255], v[124:127]\n"
"v_cvt_pk_bf16_f32 v70, v76, v77\n"
"v_cvt_pk_bf16_f32 v71, v78, v79\n"
"v_mfma_f32_16x16x32_bf16 a[172:175], a[184:187], v[248:251], a[172:175]\n"
"s_add_u32 s81, s80, 14336\n"
"buffer_store_dwordx4 v[68:71], v246, s[56:59], s81 offen sc1\n"
"v_mfma_f32_16x16x32_bf16 a[172:175], a[184:187], v[252:255], a[172:175]\n"
"v_pk_mul_f32 v[80:81], v[80:81], v[224:225] op_sel_hi:[1,0]\n"
"v_pk_mul_f32 v[82:83], v[82:83], v[224:225] op_sel_hi:[1,0]\n"
"v_mfma_f32_16x16x32_bf16 v[192:195], a[96:99], a[80:83], v[236:239]\n"
"v_exp_f32_e32 v204, v204\n"
"v_mfma_f32_16x16x32_bf16 v[192:195], a[100:103], a[84:87], v[192:195]\n"
"v_exp_f32_e32 v205, v205\n"
"v_mfma_f32_16x16x32_bf16 v[192:195], a[104:107], a[88:91], v[192:195]\n"
"v_cvt_pk_bf16_f32 v214, v204, v205\n"
"v_cvt_pk_bf16_f32 v72, v80, v81\n"
"v_cvt_pk_bf16_f32 v73, v82, v83\n"
"v_pk_mul_f32 v[84:85], v[84:85], v[224:225] op_sel_hi:[1,0]\n"
"v_pk_mul_f32 v[86:87], v[86:87], v[224:225] op_sel_hi:[1,0]\n"
"v_cvt_pk_bf16_f32 v74, v84, v85\n"
"v_cvt_pk_bf16_f32 v75, v86, v87\n"
"s_add_u32 s81, s80, 26624\n"
"buffer_store_dwordx4 v[72:75], v246, s[56:59], s81 offen sc1\n"
"v_pk_mul_f32 v[88:89], v[88:89], v[224:225] op_sel_hi:[1,0]\n"
"v_pk_mul_f32 v[90:91], v[90:91], v[224:225] op_sel_hi:[1,0]\n"
"v_cvt_pk_bf16_f32 v76, v88, v89\n"
"v_cvt_pk_bf16_f32 v77, v90, v91\n"
"v_pk_mul_f32 v[92:93], v[92:93], v[224:225] op_sel_hi:[1,0]\n"
"v_pk_mul_f32 v[94:95], v[94:95], v[224:225] op_sel_hi:[1,0]\n"
"v_cvt_pk_bf16_f32 v78, v92, v93\n"
"v_cvt_pk_bf16_f32 v79, v94, v95\n"
"s_add_u32 s81, s80, 38912\n"
"buffer_store_dwordx4 v[76:79], v246, s[56:59], s81 offen sc1\n"
"v_mfma_f32_16x16x32_bf16 v[192:195], a[108:111], a[92:95], v[192:195]\n"
"v_exp_f32_e32 v206, v206\n"
"v_mfma_f32_16x16x32_bf16 v[196:199], a[112:115], a[80:83], v[236:239]\n"
"v_exp_f32_e32 v207, v207\n"
"v_mfma_f32_16x16x32_bf16 v[196:199], a[116:119], a[84:87], v[196:199]\n"
"v_cvt_pk_bf16_f32 v215, v206, v207\n"
"v_accvgpr_read_b32 v230, a172\n"
"v_mfma_f32_16x16x32_bf16 v[196:199], a[120:123], a[88:91], v[196:199]\n"
"v_rcp_f32_e32 v228, v230\n"
"v_mfma_f32_16x16x32_bf16 v[196:199], a[124:127], a[92:95], v[196:199]\n"
"v_add_f32_e32 v219, v219, v230\n"
"v_pk_mul_f32 v[96:97], v[96:97], v[228:229] op_sel_hi:[1,0]\n"
"v_mfma_f32_16x16x32_bf16 v[200:203], a[192:195], a[80:83], v[236:239]\n"
"v_pk_mul_f32 v[98:99], v[98:99], v[228:229] op_sel_hi:[1,0]\n"
"v_cvt_pk_bf16_f32 v96, v96, v97\n"
"v_mfma_f32_16x16x32_bf16 v[200:203], a[196:199], a[84:87], v[200:203]\n"
"v_exp_f32_e32 v192, v192\n"
"v_mfma_f32_16x16x32_bf16 v[200:203], a[200:203], a[88:91], v[200:203]\n"
"v_exp_f32_e32 v193, v193\n"
"v_mfma_f32_16x16x32_bf16 v[200:203], a[204:207], a[92:95], v[200:203]\n"
"v_cvt_pk_bf16_f32 v248, v192, v193\n"
"v_cvt_pk_bf16_f32 v97, v98, v99\n"
"v_mfma_f32_16x16x32_bf16 v[204:207], a[208:211], a[80:83], v[236:239]\n"
"v_exp_f32_e32 v194, v194\n"
"v_mfma_f32_16x16x32_bf16 v[204:207], a[212:215], a[84:87], v[204:207]\n"
"v_exp_f32_e32 v195, v195\n"
"v_mfma_f32_16x16x32_bf16 v[204:207], a[216:219], a[88:91], v[204:207]\n"
"v_cvt_pk_bf16_f32 v249, v194, v195\n"
"v_pk_mul_f32 v[100:101], v[100:101], v[228:229] op_sel_hi:[1,0]\n"
"v_mfma_f32_16x16x32_bf16 v[204:207], a[220:223], a[92:95], v[204:207]\n"
"v_pk_mul_f32 v[102:103], v[102:103], v[228:229] op_sel_hi:[1,0]\n"
"v_cvt_pk_bf16_f32 v98, v100, v101\n"
"v_mfma_f32_16x16x32_bf16 v[128:131], a[128:131], v[208:211], v[128:131]\n"
"v_exp_f32_e32 v196, v196\n"
"v_mfma_f32_16x16x32_bf16 v[128:131], a[224:227], v[212:215], v[128:131]\n"
"v_exp_f32_e32 v197, v197\n"
"v_mfma_f32_16x16x32_bf16 v[132:135], a[132:135], v[208:211], v[132:135]\n"
"v_cvt_pk_bf16_f32 v250, v196, v197\n"
"v_cvt_pk_bf16_f32 v99, v102, v103\n"
"v_mfma_f32_16x16x32_bf16 v[132:135], a[228:231], v[212:215], v[132:135]\n"
"v_exp_f32_e32 v198, v198\n"
"v_mfma_f32_16x16x32_bf16 v[136:139], a[136:139], v[208:211], v[136:139]\n"
"v_exp_f32_e32 v199, v199\n"
"v_mfma_f32_16x16x32_bf16 v[136:139], a[232:235], v[212:215], v[136:139]\n"
"v_cvt_pk_bf16_f32 v251, v198, v199\n"
"v_mfma_f32_16x16x32_bf16 v[140:143], a[140:143], v[208:211], v[140:143]\n"
"s_add_u32 s81, s67, 0\n"
"buffer_store_dwordx4 v[96:99], v246, s[56:59], s81 offen sc1\n"
"v_mfma_f32_16x16x32_bf16 v[140:143], a[236:239], v[212:215], v[140:143]\n"
"v_pk_mul_f32 v[104:105], v[104:105], v[228:229] op_sel_hi:[1,0]\n"
"v_pk_mul_f32 v[106:107], v[106:107], v[228:229] op_sel_hi:[1,0]\n"
"v_mfma_f32_16x16x32_bf16 v[144:147], a[144:147], v[208:211], v[144:147]\n"
"v_cvt_pk_bf16_f32 v100, v104, v105\n"
"v_cvt_pk_bf16_f32 v101, v106, v107\n"
"v_mfma_f32_16x16x32_bf16 v[144:147], a[240:243], v[212:215], v[144:147]\n"
"v_exp_f32_e32 v200, v200\n"
"v_mfma_f32_16x16x32_bf16 v[148:151], a[148:151], v[208:211], v[148:151]\n"
"v_exp_f32_e32 v201, v201\n"
"v_mfma_f32_16x16x32_bf16 v[148:151], a[244:247], v[212:215], v[148:151]\n"
"v_cvt_pk_bf16_f32 v252, v200, v201\n"
"v_pk_mul_f32 v[108:109], v[108:109], v[228:229] op_sel_hi:[1,0]\n"
"v_mfma_f32_16x16x32_bf16 v[152:155], a[152:155], v[208:211], v[152:155]\n"
"v_exp_f32_e32 v202, v202\n"
"v_mfma_f32_16x16x32_bf16 v[152:155], a[248:251], v[212:215], v[152:155]\n"
"v_exp_f32_e32 v203, v203\n"
"v_mfma_f32_16x16x32_bf16 v[156:159], a[156:159], v[208:211], v[156:159]\n"
"v_cvt_pk_bf16_f32 v253, v202, v203\n"
"v_pk_mul_f32 v[110:111], v[110:111], v[228:229] op_sel_hi:[1,0]\n"
"v_mfma_f32_16x16x32_bf16 v[156:159], a[252:255], v[212:215], v[156:159]\n"
"v_cvt_pk_bf16_f32 v102, v108, v109\n"
"v_cvt_pk_bf16_f32 v103, v110, v111\n"
"v_mfma_f32_16x16x32_bf16 a[176:179], a[184:187], v[208:211], a[176:179]\n"
"s_add_u32 s81, s67, 12288\n"
"buffer_store_dwordx4 v[100:103], v246, s[56:59], s81 offen sc1\n"
"v_mfma_f32_16x16x32_bf16 a[176:179], a[184:187], v[212:215], a[176:179]\n"
"v_exp_f32_e32 v204, v204\n"
"v_exp_f32_e32 v205, v205\n"
"s_nop 0\n"
"v_cvt_pk_bf16_f32 v254, v204, v205\n"
"v_exp_f32_e32 v206, v206\n"
"v_exp_f32_e32 v207, v207\n"
"s_nop 0\n"
"v_cvt_pk_bf16_f32 v255, v206, v207\n"
"v_mfma_f32_16x16x32_bf16 v[160:163], a[128:131], v[248:251], v[160:163]\n"
"v_pk_mul_f32 v[112:113], v[112:113], v[228:229] op_sel_hi:[1,0]\n"
"v_pk_mul_f32 v[114:115], v[114:115], v[228:229] op_sel_hi:[1,0]\n"
"v_mfma_f32_16x16x32_bf16 v[160:163], a[224:227], v[252:255], v[160:163]\n"
"v_cvt_pk_bf16_f32 v104, v112, v113\n"
"v_cvt_pk_bf16_f32 v105, v114, v115\n"
"v_mfma_f32_16x16x32_bf16 v[164:167], a[132:135], v[248:251], v[164:167]\n"
"v_pk_mul_f32 v[116:117], v[116:117], v[228:229] op_sel_hi:[1,0]\n"
"v_pk_mul_f32 v[118:119], v[118:119], v[228:229] op_sel_hi:[1,0]\n"
"v_cvt_pk_bf16_f32 v106, v116, v117\n"
"v_cvt_pk_bf16_f32 v107, v118, v119\n"
"s_add_u32 s81, s67, 24576\n"
"buffer_store_dwordx4 v[104:107], v246, s[56:59], s81 offen sc1\n"
"v_pk_mul_f32 v[120:121], v[120:121], v[228:229] op_sel_hi:[1,0]\n"
"v_pk_mul_f32 v[122:123], v[122:123], v[228:229] op_sel_hi:[1,0]\n"
"v_cvt_pk_bf16_f32 v108, v120, v121\n"
"v_cvt_pk_bf16_f32 v109, v122, v123\n"
"v_pk_mul_f32 v[124:125], v[124:125], v[228:229] op_sel_hi:[1,0]\n"
"v_pk_mul_f32 v[126:127], v[126:127], v[228:229] op_sel_hi:[1,0]\n"
"v_cvt_pk_bf16_f32 v110, v124, v125\n"
"v_cvt_pk_bf16_f32 v111, v126, v127\n"
"s_add_u32 s81, s67, 36864\n"
"buffer_store_dwordx4 v[108:111], v246, s[56:59], s81 offen sc1\n"
"ds_write_b128 v240, v[96:99] offset:0\n"
"ds_write_b128 v240, v[100:103] offset:1024\n"
"ds_write_b128 v240, v[104:107] offset:2048\n"
"ds_write_b128 v240, v[108:111] offset:3072\n"
"v_mfma_f32_16x16x32_bf16 v[164:167], a[228:231], v[252:255], v[164:167]\n"
"v_accvgpr_read_b32 v234, a176\n"
"v_mfma_f32_16x16x32_bf16 v[168:171], a[136:139], v[248:251], v[168:171]\n"
"v_rcp_f32_e32 v232, v234\n"
"v_mfma_f32_16x16x32_bf16 v[168:171], a[232:235], v[252:255], v[168:171]\n"
"v_add_f32_e32 v219, v219, v234\n"
"v_pk_mul_f32 v[128:129], v[128:129], v[232:233] op_sel_hi:[1,0]\n"
"v_mfma_f32_16x16x32_bf16 v[172:175], a[140:143], v[248:251], v[172:175]\n"
"v_pk_mul_f32 v[130:131], v[130:131], v[232:233] op_sel_hi:[1,0]\n"
"v_cvt_pk_bf16_f32 v128, v128, v129\n"
"v_mfma_f32_16x16x32_bf16 v[172:175], a[236:239], v[252:255], v[172:175]\n"
"v_cvt_pk_bf16_f32 v129, v130, v131\n"
"v_pk_mul_f32 v[132:133], v[132:133], v[232:233] op_sel_hi:[1,0]\n"
"v_mfma_f32_16x16x32_bf16 v[176:179], a[144:147], v[248:251], v[176:179]\n"
"v_pk_mul_f32 v[134:135], v[134:135], v[232:233] op_sel_hi:[1,0]\n"
"v_cvt_pk_bf16_f32 v130, v132, v133\n"
"v_mfma_f32_16x16x32_bf16 v[176:179], a[240:243], v[252:255], v[176:179]\n"
"v_cvt_pk_bf16_f32 v131, v134, v135\n"
"v_mfma_f32_16x16x32_bf16 v[180:183], a[148:151], v[248:251], v[180:183]\n"
"s_add_u32 s81, s67, 1024\n"
"buffer_store_dwordx4 v[128:131], v246, s[56:59], s81 offen sc1\n"
"v_mfma_f32_16x16x32_bf16 v[180:183], a[244:247], v[252:255], v[180:183]\n"
"v_pk_mul_f32 v[136:137], v[136:137], v[232:233] op_sel_hi:[1,0]\n"
"v_pk_mul_f32 v[138:139], v[138:139], v[232:233] op_sel_hi:[1,0]\n"
"v_mfma_f32_16x16x32_bf16 v[184:187], a[152:155], v[248:251], v[184:187]\n"
"v_cvt_pk_bf16_f32 v132, v136, v137\n"
"v_cvt_pk_bf16_f32 v133, v138, v139\n"
"v_mfma_f32_16x16x32_bf16 v[184:187], a[248:251], v[252:255], v[184:187]\n"
"v_pk_mul_f32 v[140:141], v[140:141], v[232:233] op_sel_hi:[1,0]\n"
"v_pk_mul_f32 v[142:143], v[142:143], v[232:233] op_sel_hi:[1,0]\n"
"v_mfma_f32_16x16x32_bf16 v[188:191], a[156:159], v[248:251], v[188:191]\n"
"v_cvt_pk_bf16_f32 v134, v140, v141\n"
"v_cvt_pk_bf16_f32 v135, v142, v143\n"
"v_mfma_f32_16x16x32_bf16 v[188:191], a[252:255], v[252:255], v[188:191]\n"
"s_add_u32 s81, s67, 13312\n"
"buffer_store_dwordx4 v[132:135], v246, s[56:59], s81 offen sc1\n"
"v_mfma_f32_16x16x32_bf16 a[180:183], a[184:187], v[248:251], a[180:183]\n"
"v_pk_mul_f32 v[144:145], v[144:145], v[232:233] op_sel_hi:[1,0]\n"
"v_pk_mul_f32 v[146:147], v[146:147], v[232:233] op_sel_hi:[1,0]\n"
"v_mfma_f32_16x16x32_bf16 a[180:183], a[184:187], v[252:255], a[180:183]\n"
"v_cvt_pk_bf16_f32 v136, v144, v145\n"
"v_cvt_pk_bf16_f32 v137, v146, v147\n"
"v_pk_mul_f32 v[148:149], v[148:149], v[232:233] op_sel_hi:[1,0]\n"
"v_pk_mul_f32 v[150:151], v[150:151], v[232:233] op_sel_hi:[1,0]\n"
"v_cvt_pk_bf16_f32 v138, v148, v149\n"
"v_cvt_pk_bf16_f32 v139, v150, v151\n"
"s_add_u32 s81, s67, 25600\n"
"buffer_store_dwordx4 v[136:139], v246, s[56:59], s81 offen sc1\n"
"v_pk_mul_f32 v[152:153], v[152:153], v[232:233] op_sel_hi:[1,0]\n"
"v_pk_mul_f32 v[154:155], v[154:155], v[232:233] op_sel_hi:[1,0]\n"
"v_cvt_pk_bf16_f32 v140, v152, v153\n"
"v_cvt_pk_bf16_f32 v141, v154, v155\n"
"v_pk_mul_f32 v[156:157], v[156:157], v[232:233] op_sel_hi:[1,0]\n"
"v_pk_mul_f32 v[158:159], v[158:159], v[232:233] op_sel_hi:[1,0]\n"
"v_cvt_pk_bf16_f32 v142, v156, v157\n"
"v_cvt_pk_bf16_f32 v143, v158, v159\n"
"s_add_u32 s81, s67, 37888\n"
"buffer_store_dwordx4 v[140:143], v246, s[56:59], s81 offen sc1\n"
"ds_write_b128 v240, v[128:131] offset:4096\n"
"ds_write_b128 v240, v[132:135] offset:5120\n"
"ds_write_b128 v240, v[136:139] offset:6144\n"
"ds_write_b128 v240, v[140:143] offset:7168\n"
"v_accvgpr_read_b32 v238, a180\n"
"v_rcp_f32_e32 v236, v238\n"
"v_add_f32_e32 v219, v219, v238\n"
"v_pk_mul_f32 v[160:161], v[160:161], v[236:237] op_sel_hi:[1,0]\n"
"v_pk_mul_f32 v[162:163], v[162:163], v[236:237] op_sel_hi:[1,0]\n"
"v_cvt_pk_bf16_f32 v160, v160, v161\n"
"v_cvt_pk_bf16_f32 v161, v162, v163\n"
"v_pk_mul_f32 v[164:165], v[164:165], v[236:237] op_sel_hi:[1,0]\n"
"v_pk_mul_f32 v[166:167], v[166:167], v[236:237] op_sel_hi:[1,0]\n"
"v_cvt_pk_bf16_f32 v162, v164, v165\n"
"v_cvt_pk_bf16_f32 v163, v166, v167\n"
"s_add_u32 s81, s67, 2048\n"
"buffer_store_dwordx4 v[160:163], v246, s[56:59], s81 offen sc1\n"
"v_pk_mul_f32 v[168:169], v[168:169], v[236:237] op_sel_hi:[1,0]\n"
"v_pk_mul_f32 v[170:171], v[170:171], v[236:237] op_sel_hi:[1,0]\n"
"v_cvt_pk_bf16_f32 v164, v168, v169\n"
"v_cvt_pk_bf16_f32 v165, v170, v171\n"
"v_pk_mul_f32 v[172:173], v[172:173], v[236:237] op_sel_hi:[1,0]\n"
"v_pk_mul_f32 v[174:175], v[174:175], v[236:237] op_sel_hi:[1,0]\n"
"v_cvt_pk_bf16_f32 v166, v172, v173\n"
"v_cvt_pk_bf16_f32 v167, v174, v175\n"
"s_add_u32 s81, s67, 14336\n"
"buffer_store_dwordx4 v[164:167], v246, s[56:59], s81 offen sc1\n"
"v_pk_mul_f32 v[176:177], v[176:177], v[236:237] op_sel_hi:[1,0]\n"
"v_pk_mul_f32 v[178:179], v[178:179], v[236:237] op_sel_hi:[1,0]\n"
"v_cvt_pk_bf16_f32 v168, v176, v177\n"
"v_cvt_pk_bf16_f32 v169, v178, v179\n"
"v_pk_mul_f32 v[180:181], v[180:181], v[236:237] op_sel_hi:[1,0]\n"
"v_pk_mul_f32 v[182:183], v[182:183], v[236:237] op_sel_hi:[1,0]\n"
"v_cvt_pk_bf16_f32 v170, v180, v181\n"
"v_cvt_pk_bf16_f32 v171, v182, v183\n"
"s_add_u32 s81, s67, 26624\n"
"buffer_store_dwordx4 v[168:171], v246, s[56:59], s81 offen sc1\n"
"v_pk_mul_f32 v[184:185], v[184:185], v[236:237] op_sel_hi:[1,0]\n"
"v_pk_mul_f32 v[186:187], v[186:187], v[236:237] op_sel_hi:[1,0]\n"
"v_cvt_pk_bf16_f32 v172, v184, v185\n"
"v_cvt_pk_bf16_f32 v173, v186, v187\n"
"v_pk_mul_f32 v[188:189], v[188:189], v[236:237] op_sel_hi:[1,0]\n"
"v_pk_mul_f32 v[190:191], v[190:191], v[236:237] op_sel_hi:[1,0]\n"
"v_cvt_pk_bf16_f32 v174, v188, v189\n"
"v_cvt_pk_bf16_f32 v175, v190, v191\n"
"s_add_u32 s81, s67, 38912\n"
"buffer_store_dwordx4 v[172:175], v246, s[56:59], s81 offen sc1\n"
"ds_write_b128 v240, v[160:163] offset:8192\n"
"ds_write_b128 v240, v[164:167] offset:9216\n"
"ds_write_b128 v240, v[168:171] offset:10240\n"
"ds_write_b128 v240, v[172:175] offset:11264\n"
"s_mov_b32 s70, 0x5f800000\n"
"v_cmp_nge_f32_e32 vcc, s70, v219\n"
"s_nop 0\n"
"s_cmp_lg_u64 vcc, 0\n"
"s_cselect_b32 s80, 1, 0\n"
"v_mov_b32_e32 v250, s80\n"
"s_lshl_b32 s81, s60, 2\n"
"s_add_u32 s81, s81, 106496\n"
"v_mov_b32_e32 v251, s81\n"
"ds_write_b32 v251, v250\n"
"s_mul_i32 s70, s60, 12288\n"
"s_add_u32 s70, s70, 114688\n"
"v_add_u32_e32 v237, s70, v246\n"
"L_hhd%=:\n"
"v_mbcnt_lo_u32_b32 v226, -1, 0\n"
"v_mbcnt_hi_u32_b32 v226, -1, v226\n"
"v_lshrrev_b32_e32 v227, 5, v226\n"
"v_mul_u32_u24_e32 v216, 0xa00, v227\n"
"v_bfe_u32 v227, v226, 4, 1\n"
"v_mul_u32_u24_e32 v227, 0x120, v227\n"
"v_add_u32_e32 v216, v216, v227\n"
"v_bfe_u32 v227, v226, 2, 2\n"
"v_lshl_add_u32 v216, v227, 6, v216\n"
"v_and_b32_e32 v227, 3, v226\n"
"v_lshl_add_u32 v216, v227, 3, v216\n"
"v_xor_b32_e32 v217, 32, v216\n"
"s_lshr_b32 s70, s60, 1\n"
"s_mul_i32 s70, s70, 40960\n"
"s_sub_u32 s71, 40960, s70\n"
"v_add_u32_e32 v222, s70, v216\n"
"v_add_u32_e32 v224, s71, v216\n"
"v_add_u32_e32 v218, 61440, v216\n"
"v_add_u32_e32 v220, 122880, v216\n"
"v_add_u32_e32 v223, s70, v217\n"
"v_add_u32_e32 v225, s71, v217\n"
"v_add_u32_e32 v219, 61440, v217\n"
"v_add_u32_e32 v221, 122880, v217\n"
"v_mov_b32_e32 v238, 106496\n"
"s_waitcnt vmcnt(24) lgkmcnt(0)\n"
"s_barrier\n"
"ds_read_b128 v[232:235], v238\n"
"ds_read_b64_tr_b16 a[96:97], v222 offset:0\n"
"ds_read_b64_tr_b16 a[98:99], v222 offset:5120\n"
"ds_read_b64_tr_b16 a[136:137], v222 offset:10240\n"
"ds_read_b64_tr_b16 a[138:139], v222 offset:15360\n"
"ds_read_b64_tr_b16 a[100:101], v223 offset:0\n"
"ds_read_b64_tr_b16 a[102:103], v223 offset:5120\n"
"ds_read_b64_tr_b16 a[140:141], v223 offset:10240\n"
"ds_read_b64_tr_b16 a[142:143], v223 offset:15360\n"
"ds_read_b64_tr_b16 a[104:105], v222 offset:512\n"
"ds_read_b64_tr_b16 a[106:107], v222 offset:5632\n"
"ds_read_b64_tr_b16 a[144:145], v222 offset:10752\n"
"ds_read_b64_tr_b16 a[146:147], v222 offset:15872\n"
"ds_read_b64_tr_b16 a[108:109], v223 offset:512\n"
"ds_read_b64_tr_b16 a[110:111], v223 offset:5632\n"
"ds_read_b64_tr_b16 a[148:149], v223 offset:10752\n"
"ds_read_b64_tr_b16 a[150:151], v223 offset:15872\n"
"ds_read_b64_tr_b16 a[112:113], v222 offset:1024\n"
"ds_read_b64_tr_b16 a[114:115], v222 offset:6144\n"
"ds_read_b64_tr_b16 a[152:153], v222 offset:11264\n"
"ds_read_b64_tr_b16 a[154:155], v222 offset:16384\n"
"ds_read_b64_tr_b16 a[116:117], v223 offset:1024\n"
"ds_read_b64_tr_b16 a[118:119], v223 offset:6144\n"
"ds_read_b64_tr_b16 a[156:157], v223 offset:11264\n"
"ds_read_b64_tr_b16 a[158:159], v223 offset:16384\n"
"ds_read_b64_tr_b16 a[120:121], v222 offset:1536\n"
"ds_read_b64_tr_b16 a[122:123], v222 offset:6656\n"
"ds_read_b64_tr_b16 a[160:161], v222 offset:11776\n"
"ds_read_b64_tr_b16 a[162:163], v222 offset:16896\n"
"ds_read_b64_tr_b16 a[124:125], v223 offset:1536\n"
"ds_read_b64_tr_b16 a[126:127], v223 offset:6656\n"
"ds_read_b64_tr_b16 a[164:165], v223 offset:11776\n"
"ds_read_b64_tr_b16 a[166:167], v223 offset:16896\n"
"ds_read_b64_tr_b16 a[128:129], v222 offset:2048\n"
"ds_read_b64_tr_b16 a[130:131], v222 offset:7168\n"
"ds_read_b64_tr_b16 a[168:169], v222 offset:12288\n"
"ds_read_b64_tr_b16 a[170:171], v222 offset:17408\n"
"ds_read_b64_tr_b16 a[132:133], v223 offset:2048\n"
"ds_read_b64_tr_b16 a[134:135], v223 offset:7168\n"
"ds_read_b64_tr_b16 a[172:173], v223 offset:12288\n"
"ds_read_b64_tr_b16 a[174:175], v223 offset:17408\n"
"s_waitcnt lgkmcnt(15)\n"
"v_or3_b32 v232, v232, v233, v234\n"
"v_or_b32_e32 v232, v232, v235\n"
"s_nop 0\n"
"v_readfirstlane_b32 s80, v232\n"
"s_nop 0\n"
"s_andn2_b32 s80, s80, s76\n"
"s_cbranch_scc1 L_slow%=\n"
"s_waitcnt vmcnt(24) lgkmcnt(15)\n"
"v_mfma_f32_16x16x32_bf16 v[96:99], a[96:99], v[0:3], 0\n"
"s_lshr_b32 s70, %16, 1\n"
"ds_read_b128 a[0:3], v237 offset:0\n"
"v_mfma_f32_16x16x32_bf16 v[96:99], a[136:139], v[4:7], v[96:99]\n"
"s_lshl_b32 s70, s70, 7\n"
"ds_read_b128 a[16:19], v237 offset:4096\n"
"v_mfma_f32_16x16x32_bf16 v[100:103], a[96:99], v[32:35], 0\n"
"s_add_u32 s70, s70, 32768\n"
"v_mfma_f32_16x16x32_bf16 v[100:103], a[136:139], v[36:39], v[100:103]\n"
"v_mbcnt_lo_u32_b32 v226, -1, 0\n"
"v_mbcnt_hi_u32_b32 v226, -1, v226\n"
"v_mfma_f32_16x16x32_bf16 v[104:107], a[96:99], v[64:67], 0\n"
"ds_read_b128 a[32:35], v237 offset:8192\n"
"v_or_b32_e32 v226, s60, v226\n"
"v_mfma_f32_16x16x32_bf16 v[104:107], a[136:139], v[68:71], v[104:107]\n"
"ds_read_b64_tr_b16 a[96:97], v222 offset:20480\n"
"ds_read_b128 a[4:7], v237 offset:1024\n"
"v_mfma_f32_16x16x32_bf16 v[108:111], a[100:103], v[0:3], 0\n"
"ds_read_b64_tr_b16 a[98:99], v222 offset:25600\n"
"v_mfma_f32_16x16x32_bf16 v[108:111], a[140:143], v[4:7], v[108:111]\n"
"ds_read_b64_tr_b16 a[136:137], v222 offset:30720\n"
"ds_read_b128 a[20:23], v237 offset:5120\n"
"v_mfma_f32_16x16x32_bf16 v[112:115], a[100:103], v[32:35], 0\n"
"ds_read_b64_tr_b16 a[138:139], v222 offset:35840\n"
"ds_read_b128 a[36:39], v237 offset:9216\n"
"v_mfma_f32_16x16x32_bf16 v[112:115], a[140:143], v[36:39], v[112:115]\n"
"v_cmp_eq_u32_e32 vcc, 0, v226\n"
"v_bfrev_b32_e32 v226, 1\n"
"v_mfma_f32_16x16x32_bf16 v[116:119], a[100:103], v[64:67], 0\n"
"v_mov_b32_e32 v227, s70\n"
"v_cndmask_b32_e32 v226, v226, v227, vcc\n"
"v_mfma_f32_16x16x32_bf16 v[116:119], a[140:143], v[68:71], v[116:119]\n"
"ds_read_b64_tr_b16 a[100:101], v223 offset:20480\n"
"v_mov_b32_e32 v228, s62\n"
"v_mfma_f32_16x16x32_bf16 v[120:123], a[104:107], v[0:3], 0\n"
"ds_read_b64_tr_b16 a[102:103], v223 offset:25600\n"
"v_mov_b32_e32 v229, s65\n"
"v_mfma_f32_16x16x32_bf16 v[120:123], a[144:147], v[4:7], v[120:123]\n"
"ds_read_b64_tr_b16 a[140:141], v223 offset:30720\n"
"ds_read_b128 a[8:11], v237 offset:2048\n"
"v_mfma_f32_16x16x32_bf16 v[124:127], a[104:107], v[32:35], 0\n"
"ds_read_b64_tr_b16 a[142:143], v223 offset:35840\n"
"v_mov_b32_e32 v238, 0\n"
"v_mfma_f32_16x16x32_bf16 v[124:127], a[144:147], v[36:39], v[124:127]\n"
"ds_read_b128 a[24:27], v237 offset:6144\n"
"s_bfe_u32 s71, %15, 0x20003\n"
"v_mfma_f32_16x16x32_bf16 v[128:131], a[104:107], v[64:67], 0\n"
"s_add_u32 s92, s71, 1\n"
"ds_read_b128 a[40:43], v237 offset:10240\n"
"v_mfma_f32_16x16x32_bf16 v[128:131], a[144:147], v[68:71], v[128:131]\n"
"ds_read_b64_tr_b16 a[104:105], v222 offset:20992\n"
"s_and_b32 s92, s92, 3\n"
"v_mfma_f32_16x16x32_bf16 v[132:135], a[108:111], v[0:3], 0\n"
"ds_read_b64_tr_b16 a[106:107], v222 offset:26112\n"
"ds_read_b128 a[12:15], v237 offset:3072\n"
"v_mfma_f32_16x16x32_bf16 v[132:135], a[148:151], v[4:7], v[132:135]\n"
"ds_read_b64_tr_b16 a[144:145], v222 offset:31232\n"
"s_lshr_b32 s70, %16, 3\n"
"v_mfma_f32_16x16x32_bf16 v[136:139], a[108:111], v[32:35], 0\n"
"ds_read_b64_tr_b16 a[146:147], v222 offset:36352\n"
"ds_read_b128 a[28:31], v237 offset:7168\n"
"ds_read_b128 a[44:47], v237 offset:11264\n"
"v_mfma_f32_16x16x32_bf16 v[136:139], a[148:151], v[36:39], v[136:139]\n"
"s_lshl_b32 s70, s70, 2\n"
"s_add_u32 s92, s92, s70\n"
"v_mfma_f32_16x16x32_bf16 v[140:143], a[108:111], v[64:67], 0\n"
"s_lshl_b32 s92, s92, 7\n"
"s_add_u32 s92, s92, 32768\n"
"v_mfma_f32_16x16x32_bf16 v[140:143], a[148:151], v[68:71], v[140:143]\n"
"ds_read_b64_tr_b16 a[108:109], v223 offset:20992\n"
"s_add_u32 s93, s71, 2\n"
"v_mfma_f32_16x16x32_bf16 v[144:147], a[112:115], v[0:3], 0\n"
"ds_read_b64_tr_b16 a[110:111], v223 offset:26112\n"
"s_and_b32 s93, s93, 3\n"
"v_mfma_f32_16x16x32_bf16 v[144:147], a[152:155], v[4:7], v[144:147]\n"
"ds_read_b64_tr_b16 a[148:149], v223 offset:31232\n"
"s_lshr_b32 s70, %16, 3\n"
"v_mfma_f32_16x16x32_bf16 v[148:151], a[112:115], v[32:35], 0\n"
"ds_read_b64_tr_b16 a[150:151], v223 offset:36352\n"
"s_lshl_b32 s70, s70, 2\n"
"v_mfma_f32_16x16x32_bf16 v[148:151], a[152:155], v[36:39], v[148:151]\n"
"s_add_u32 s93, s93, s70\n"
"v_mfma_f32_16x16x32_bf16 v[152:155], a[112:115], v[64:67], 0\n"
"s_lshl_b32 s93, s93, 7\n"
"v_mfma_f32_16x16x32_bf16 v[152:155], a[152:155], v[68:71], v[152:155]\n"
"ds_read_b64_tr_b16 a[112:113], v222 offset:21504\n"
"v_mfma_f32_16x16x32_bf16 v[156:159], a[116:119], v[0:3], 0\n"
"ds_read_b64_tr_b16 a[114:115], v222 offset:26624\n"
"s_add_u32 s93, s93, 32768\n"
"v_mfma_f32_16x16x32_bf16 v[156:159], a[156:159], v[4:7], v[156:159]\n"
"ds_read_b64_tr_b16 a[152:153], v222 offset:31744\n"
"s_add_u32 s94, s71, 3\n"
"v_mfma_f32_16x16x32_bf16 v[160:163], a[116:119], v[32:35], 0\n"
"ds_read_b64_tr_b16 a[154:155], v222 offset:36864\n"
"s_and_b32 s94, s94, 3\n"
"v_mfma_f32_16x16x32_bf16 v[160:163], a[156:159], v[36:39], v[160:163]\n"
"s_lshr_b32 s70, %16, 3\n"
"v_mfma_f32_16x16x32_bf16 v[164:167], a[116:119], v[64:67], 0\n"
"s_lshl_b32 s70, s70, 2\n"
"v_mfma_f32_16x16x32_bf16 v[164:167], a[156:159], v[68:71], v[164:167]\n"
"s_waitcnt lgkmcnt(15)\n"
"ds_read_b64_tr_b16 a[116:117], v223 offset:21504\n"
"v_mfma_f32_16x16x32_bf16 v[168:171], a[120:123], v[0:3], 0\n"
"ds_read_b64_tr_b16 a[118:119], v223 offset:26624\n"
"s_add_u32 s94, s94, s70\n"
"v_mfma_f32_16x16x32_bf16 v[168:171], a[160:163], v[4:7], v[168:171]\n"
"ds_read_b64_tr_b16 a[156:157], v223 offset:31744\n"
"s_lshl_b32 s94, s94, 7\n"
"v_mfma_f32_16x16x32_bf16 v[172:175], a[120:123], v[32:35], 0\n"
"ds_read_b64_tr_b16 a[158:159], v223 offset:36864\n"
"s_add_u32 s94, s94, 32768\n"
"v_mfma_f32_16x16x32_bf16 v[172:175], a[160:163], v[36:39], v[172:175]\n"
"s_lshr_b32 s70, %16, 3\n"
"v_mfma_f32_16x16x32_bf16 v[176:179], a[120:123], v[64:67], 0\n"
"s_mul_i32 s70, s70, 393216\n"
"v_mfma_f32_16x16x32_bf16 v[176:179], a[160:163], v[68:71], v[176:179]\n"
"ds_read_b64_tr_b16 a[120:121], v222 offset:22016\n"
"v_mfma_f32_16x16x32_bf16 v[180:183], a[124:127], v[0:3], 0\n"
"ds_read_b64_tr_b16 a[122:123], v222 offset:27136\n"
"s_and_b32 s71, s60, 1\n"
"v_mfma_f32_16x16x32_bf16 v[180:183], a[164:167], v[4:7], v[180:183]\n"
"ds_read_b64_tr_b16 a[160:161], v222 offset:32256\n"
"s_mul_i32 s71, s71, 6144\n"
"v_mfma_f32_16x16x32_bf16 v[184:187], a[124:127], v[32:35], 0\n"
"ds_read_b64_tr_b16 a[162:163], v222 offset:37376\n"
"s_add_u32 s70, s70, s71\n"
"v_mfma_f32_16x16x32_bf16 v[184:187], a[164:167], v[36:39], v[184:187]\n"
"s_lshr_b32 s71, s60, 1\n"
"v_mfma_f32_16x16x32_bf16 v[188:191], a[124:127], v[64:67], 0\n"
"s_mul_i32 s71, s71, 3072\n"
"v_mfma_f32_16x16x32_bf16 v[188:191], a[164:167], v[68:71], v[188:191]\n"
"ds_read_b64_tr_b16 a[124:125], v223 offset:22016\n"
"v_mfma_f32_16x16x32_bf16 v[192:195], a[128:131], v[0:3], 0\n"
"ds_read_b64_tr_b16 a[126:127], v223 offset:27136\n"
"s_add_u32 s70, s70, s71\n"
"v_mfma_f32_16x16x32_bf16 v[192:195], a[168:171], v[4:7], v[192:195]\n"
"ds_read_b64_tr_b16 a[164:165], v223 offset:32256\n"
"v_mfma_f32_16x16x32_bf16 v[196:199], a[128:131], v[32:35], 0\n"
"ds_read_b64_tr_b16 a[166:167], v223 offset:37376\n"
"v_mfma_f32_16x16x32_bf16 v[196:199], a[168:171], v[36:39], v[196:199]\n"
"s_add_u32 s44, %8, s70\n"
"s_addc_u32 s45, %9, 0\n"
"v_mfma_f32_16x16x32_bf16 v[200:203], a[128:131], v[64:67], 0\n"
"s_and_b32 s45, s45, 0xffff\n"
"s_mov_b32 s46, 384000\n"
"v_mfma_f32_16x16x32_bf16 v[200:203], a[168:171], v[68:71], v[200:203]\n"
"ds_read_b64_tr_b16 a[128:129], v222 offset:22528\n"
"s_mov_b32 s47, 0x20000\n"
"v_mfma_f32_16x16x32_bf16 v[204:207], a[132:135], v[0:3], 0\n"
"ds_read_b64_tr_b16 a[130:131], v222 offset:27648\n"
"s_bfe_u32 s71, %15, 0x20003\n"
"v_mfma_f32_16x16x32_bf16 v[204:207], a[172:175], v[4:7], v[204:207]\n"
"ds_read_b64_tr_b16 a[168:169], v222 offset:32768\n"
"s_add_u32 s71, s71, 1\n"
"v_mfma_f32_16x16x32_bf16 v[208:211], a[132:135], v[32:35], 0\n"
"ds_read_b64_tr_b16 a[170:171], v222 offset:37888\n"
"s_and_b32 s71, s71, 3\n"
"v_mfma_f32_16x16x32_bf16 v[208:211], a[172:175], v[36:39], v[208:211]\n"
"s_mul_i32 s72, s71, 98304\n"
"v_mfma_f32_16x16x32_bf16 v[212:215], a[132:135], v[64:67], 0\n"
"v_mfma_f32_16x16x32_bf16 v[212:215], a[172:175], v[68:71], v[212:215]\n"
"ds_read_b64_tr_b16 a[132:133], v223 offset:22528\n"
"ds_read_b64_tr_b16 a[134:135], v223 offset:27648\n"
"ds_read_b64_tr_b16 a[172:173], v223 offset:32768\n"
"ds_read_b64_tr_b16 a[174:175], v223 offset:37888\n"
"s_waitcnt vmcnt(0) lgkmcnt(15)\n"
"s_barrier\n"
"buffer_store_dwordx2 v[228:229], v226, s[36:39], 0 offen sc1\n"
"v_mfma_f32_16x16x32_bf16 v[96:99], a[96:99], v[8:11], v[96:99]\n"
"s_add_u32 m0, s78, 102400\n"
"v_mfma_f32_16x16x32_bf16 v[96:99], a[136:139], v[12:15], v[96:99]\n"
"v_mfma_f32_16x16x32_bf16 v[100:103], a[96:99], v[40:43], v[100:103]\n"
"buffer_load_dwordx4 v244, s[40:43], s73 offen lds\n"
"v_mfma_f32_16x16x32_bf16 v[100:103], a[136:139], v[44:47], v[100:103]\n"
"v_mfma_f32_16x16x32_bf16 v[104:107], a[96:99], v[72:75], v[104:107]\n"
"s_add_u32 m0, s78, 103296\n"
"v_mfma_f32_16x16x32_bf16 v[104:107], a[136:139], v[76:79], v[104:107]\n"
"ds_read_b64_tr_b16 a[96:97], v224 offset:0\n"
"v_mfma_f32_16x16x32_bf16 v[108:111], a[100:103], v[8:11], v[108:111]\n"
"ds_read_b64_tr_b16 a[98:99], v224 offset:5120\n"
"v_mfma_f32_16x16x32_bf16 v[108:111], a[140:143], v[12:15], v[108:111]\n"
"ds_read_b64_tr_b16 a[136:137], v224 offset:10240\n"
"v_mfma_f32_16x16x32_bf16 v[112:115], a[100:103], v[40:43], v[112:115]\n"
"ds_read_b64_tr_b16 a[138:139], v224 offset:15360\n"
"v_mfma_f32_16x16x32_bf16 v[112:115], a[140:143], v[44:47], v[112:115]\n"
"buffer_load_dwordx4 v244, s[40:43], s73 offen offset:128 lds\n"
"v_mfma_f32_16x16x32_bf16 v[116:119], a[100:103], v[72:75], v[116:119]\n"
"s_add_u32 m0, s78, 104192\n"
"v_mfma_f32_16x16x32_bf16 v[116:119], a[140:143], v[76:79], v[116:119]\n"
"ds_read_b64_tr_b16 a[100:101], v225 offset:0\n"
"v_mfma_f32_16x16x32_bf16 v[120:123], a[104:107], v[8:11], v[120:123]\n"
"ds_read_b64_tr_b16 a[102:103], v225 offset:5120\n"
"v_mfma_f32_16x16x32_bf16 v[120:123], a[144:147], v[12:15], v[120:123]\n"
"ds_read_b64_tr_b16 a[140:141], v225 offset:10240\n"
"v_mfma_f32_16x16x32_bf16 v[124:127], a[104:107], v[40:43], v[124:127]\n"
"ds_read_b64_tr_b16 a[142:143], v225 offset:15360\n"
"v_mfma_f32_16x16x32_bf16 v[124:127], a[144:147], v[44:47], v[124:127]\n"
"buffer_load_dwordx4 v245, s[40:43], s73 offen offset:256 lds\n"
"v_mfma_f32_16x16x32_bf16 v[128:131], a[104:107], v[72:75], v[128:131]\n"
"s_add_u32 m0, s78, 105408\n"
"s_add_u32 s64, s73, 10240\n"
"v_mfma_f32_16x16x32_bf16 v[128:131], a[144:147], v[76:79], v[128:131]\n"
"ds_read_b64_tr_b16 a[104:105], v224 offset:512\n"
"buffer_load_dwordx4 v244, s[40:43], s64 offen offset:64 lds\n"
"v_mfma_f32_16x16x32_bf16 v[132:135], a[108:111], v[8:11], v[132:135]\n"
"ds_read_b64_tr_b16 a[106:107], v224 offset:5632\n"
"s_add_u32 m0, s78, 106304\n"
"v_mfma_f32_16x16x32_bf16 v[132:135], a[148:151], v[12:15], v[132:135]\n"
"ds_read_b64_tr_b16 a[144:145], v224 offset:10752\n"
"v_mfma_f32_16x16x32_bf16 v[136:139], a[108:111], v[40:43], v[136:139]\n"
"ds_read_b64_tr_b16 a[146:147], v224 offset:15872\n"
"v_mfma_f32_16x16x32_bf16 v[136:139], a[148:151], v[44:47], v[136:139]\n"
"buffer_load_dwordx4 v244, s[40:43], s64 offen offset:192 lds\n"
"v_mfma_f32_16x16x32_bf16 v[140:143], a[108:111], v[72:75], v[140:143]\n"
"s_add_u32 s73, s73, 81920\n"
"v_mfma_f32_16x16x32_bf16 v[140:143], a[148:151], v[76:79], v[140:143]\n"
"ds_read_b64_tr_b16 a[108:109], v225 offset:512\n"
"v_mfma_f32_16x16x32_bf16 v[144:147], a[112:115], v[8:11], v[144:147]\n"
"ds_read_b64_tr_b16 a[110:111], v225 offset:5632\n"
"v_mfma_f32_16x16x32_bf16 v[144:147], a[152:155], v[12:15], v[144:147]\n"
"ds_read_b64_tr_b16 a[148:149], v225 offset:10752\n"
"v_mfma_f32_16x16x32_bf16 v[148:151], a[112:115], v[40:43], v[148:151]\n"
"ds_read_b64_tr_b16 a[150:151], v225 offset:15872\n"
"v_mfma_f32_16x16x32_bf16 v[148:151], a[152:155], v[44:47], v[148:151]\n"
"s_cmp_eq_u32 s73, 1310720\n"
"s_cselect_b32 s73, 0, s73\n"
"v_mfma_f32_16x16x32_bf16 v[152:155], a[112:115], v[72:75], v[152:155]\n"
"buffer_load_dwordx2 v[230:231], v238, s[36:39], s92 offen sc1\n"
"s_add_u32 m0, s78, 122880\n"
"v_mfma_f32_16x16x32_bf16 v[152:155], a[152:155], v[76:79], v[152:155]\n"
"ds_read_b64_tr_b16 a[112:113], v224 offset:1024\n"
"v_mfma_f32_16x16x32_bf16 v[156:159], a[116:119], v[8:11], v[156:159]\n"
"ds_read_b64_tr_b16 a[114:115], v224 offset:6144\n"
"v_mfma_f32_16x16x32_bf16 v[156:159], a[156:159], v[12:15], v[156:159]\n"
"ds_read_b64_tr_b16 a[152:153], v224 offset:11264\n"
"v_mfma_f32_16x16x32_bf16 v[160:163], a[116:119], v[40:43], v[160:163]\n"
"ds_read_b64_tr_b16 a[154:155], v224 offset:16384\n"
"v_mfma_f32_16x16x32_bf16 v[160:163], a[156:159], v[44:47], v[160:163]\n"
"buffer_load_dwordx4 v244, s[40:43], s73 offen lds\n"
"v_mfma_f32_16x16x32_bf16 v[164:167], a[116:119], v[72:75], v[164:167]\n"
"s_add_u32 m0, s78, 123776\n"
"v_mfma_f32_16x16x32_bf16 v[164:167], a[156:159], v[76:79], v[164:167]\n"
"s_waitcnt lgkmcnt(15)\n"
"ds_read_b64_tr_b16 a[116:117], v225 offset:1024\n"
"v_mfma_f32_16x16x32_bf16 v[168:171], a[120:123], v[8:11], v[168:171]\n"
"ds_read_b64_tr_b16 a[118:119], v225 offset:6144\n"
"v_mfma_f32_16x16x32_bf16 v[168:171], a[160:163], v[12:15], v[168:171]\n"
"ds_read_b64_tr_b16 a[156:157], v225 offset:11264\n"
"v_mfma_f32_16x16x32_bf16 v[172:175], a[120:123], v[40:43], v[172:175]\n"
"ds_read_b64_tr_b16 a[158:159], v225 offset:16384\n"
"v_mfma_f32_16x16x32_bf16 v[172:175], a[160:163], v[44:47], v[172:175]\n"
"buffer_load_dwordx4 v244, s[40:43], s73 offen offset:128 lds\n"
"v_mfma_f32_16x16x32_bf16 v[176:179], a[120:123], v[72:75], v[176:179]\n"
"s_add_u32 m0, s78, 124672\n"
"s_nop 0\n"
"buffer_load_dwordx4 v245, s[40:43], s73 offen offset:256 lds\n"
"v_mfma_f32_16x16x32_bf16 v[176:179], a[160:163], v[76:79], v[176:179]\n"
"ds_read_b64_tr_b16 a[120:121], v224 offset:1536\n"
"v_mfma_f32_16x16x32_bf16 v[180:183], a[124:127], v[8:11], v[180:183]\n"
"ds_read_b64_tr_b16 a[122:123], v224 offset:6656\n"
"v_mfma_f32_16x16x32_bf16 v[180:183], a[164:167], v[12:15], v[180:183]\n"
"ds_read_b64_tr_b16 a[160:161], v224 offset:11776\n"
"v_mfma_f32_16x16x32_bf16 v[184:187], a[124:127], v[40:43], v[184:187]\n"
"ds_read_b64_tr_b16 a[162:163], v224 offset:16896\n"
"v_mfma_f32_16x16x32_bf16 v[184:187], a[164:167], v[44:47], v[184:187]\n"
"s_add_u32 m0, s78, 125888\n"
"s_add_u32 s64, s73, 10240\n"
"v_mfma_f32_16x16x32_bf16 v[188:191], a[124:127], v[72:75], v[188:191]\n"
"buffer_load_dwordx4 v244, s[40:43], s64 offen offset:64 lds\n"
"v_mfma_f32_16x16x32_bf16 v[188:191], a[164:167], v[76:79], v[188:191]\n"
"ds_read_b64_tr_b16 a[124:125], v225 offset:1536\n"
"s_add_u32 m0, s78, 126784\n"
"v_mfma_f32_16x16x32_bf16 v[192:195], a[128:131], v[8:11], v[192:195]\n"
"ds_read_b64_tr_b16 a[126:127], v225 offset:6656\n"
"v_mfma_f32_16x16x32_bf16 v[192:195], a[168:171], v[12:15], v[192:195]\n"
"ds_read_b64_tr_b16 a[164:165], v225 offset:11776\n"
"v_mfma_f32_16x16x32_bf16 v[196:199], a[128:131], v[40:43], v[196:199]\n"
"ds_read_b64_tr_b16 a[166:167], v225 offset:16896\n"
"v_mfma_f32_16x16x32_bf16 v[196:199], a[168:171], v[44:47], v[196:199]\n"
"buffer_load_dwordx4 v244, s[40:43], s64 offen offset:192 lds\n"
"v_mfma_f32_16x16x32_bf16 v[200:203], a[128:131], v[72:75], v[200:203]\n"
"s_add_u32 s73, s73, 81920\n"
"s_cmp_eq_u32 s73, 1310720\n"
"s_cselect_b32 s73, 0, s73\n"
"v_mfma_f32_16x16x32_bf16 v[200:203], a[168:171], v[76:79], v[200:203]\n"
"ds_read_b64_tr_b16 a[128:129], v224 offset:2048\n"
"v_mfma_f32_16x16x32_bf16 v[204:207], a[132:135], v[8:11], v[204:207]\n"
"ds_read_b64_tr_b16 a[130:131], v224 offset:7168\n"
"v_mfma_f32_16x16x32_bf16 v[204:207], a[172:175], v[12:15], v[204:207]\n"
"ds_read_b64_tr_b16 a[168:169], v224 offset:12288\n"
"v_mfma_f32_16x16x32_bf16 v[208:211], a[132:135], v[40:43], v[208:211]\n"
"ds_read_b64_tr_b16 a[170:171], v224 offset:17408\n"
"v_mfma_f32_16x16x32_bf16 v[208:211], a[172:175], v[44:47], v[208:211]\n"
"v_mfma_f32_16x16x32_bf16 v[212:215], a[132:135], v[72:75], v[212:215]\n"
"v_mfma_f32_16x16x32_bf16 v[212:215], a[172:175], v[76:79], v[212:215]\n"
"ds_read_b64_tr_b16 a[132:133], v225 offset:2048\n"
"ds_read_b64_tr_b16 a[134:135], v225 offset:7168\n"
"ds_read_b64_tr_b16 a[172:173], v225 offset:12288\n"
"ds_read_b64_tr_b16 a[174:175], v225 offset:17408\n"
"s_waitcnt vmcnt(5)\n"
"v_cmp_eq_u32_e64 s[98:99], s62, v230\n"
"v_cmp_eq_u32_e32 vcc, s65, v231\n"
"s_nop 0\n"
"s_and_b64 s[98:99], s[98:99], vcc\n"
"s_cmp_eq_u64 s[98:99], -1\n"
"s_cbranch_scc1 L_pf0d%=\n"
"s_mov_b32 s63, 0x40000\n"
"L_pf0%=:\n"
"buffer_load_dwordx2 v[230:231], v238, s[36:39], s92 offen sc1\n"
"s_waitcnt vmcnt(0)\n"
"v_cmp_eq_u32_e64 s[98:99], s62, v230\n"
"v_cmp_eq_u32_e32 vcc, s65, v231\n"
"s_nop 0\n"
"s_and_b64 s[98:99], s[98:99], vcc\n"
"s_cmp_eq_u64 s[98:99], -1\n"
"s_cbranch_scc1 L_pf0d%=\n"
"s_sleep 1\n"
"s_sub_u32 s63, s63, 1\n"
"s_cmp_lg_u32 s63, 0\n"
"s_cbranch_scc1 L_pf0%=\n"
"L_pf0d%=:\n"
"s_waitcnt vmcnt(12) lgkmcnt(15)\n"
"s_barrier\n"
"v_mfma_f32_16x16x32_bf16 v[96:99], a[96:99], a[0:3], v[96:99]\n"
"buffer_load_dwordx4 a[176:179], v246, s[44:47], s72 offen sc1\n"
"s_lshr_b32 s80, %16, 5\n"
"v_mfma_f32_16x16x32_bf16 v[96:99], a[136:139], a[4:7], v[96:99]\n"
"v_mfma_f32_16x16x32_bf16 v[100:103], a[96:99], a[16:19], v[100:103]\n"
"buffer_load_dwordx4 a[180:183], v246, s[44:47], s72 offen offset:1024 sc1\n"
"v_mfma_f32_16x16x32_bf16 v[100:103], a[136:139], a[20:23], v[100:103]\n"
"v_mfma_f32_16x16x32_bf16 v[104:107], a[96:99], a[32:35], v[104:107]\n"
"s_bfe_u32 s71, %15, 0x20003\n"
"v_mfma_f32_16x16x32_bf16 v[104:107], a[136:139], a[36:39], v[104:107]\n"
"ds_read_b64_tr_b16 a[96:97], v224 offset:20480\n"
"buffer_load_dwordx4 a[184:187], v246, s[44:47], s72 offen offset:2048 sc1\n"
"v_mfma_f32_16x16x32_bf16 v[108:111], a[100:103], a[0:3], v[108:111]\n"
"ds_read_b64_tr_b16 a[98:99], v224 offset:25600\n"
"v_mfma_f32_16x16x32_bf16 v[108:111], a[140:143], a[4:7], v[108:111]\n"
"ds_read_b64_tr_b16 a[136:137], v224 offset:30720\n"
"s_add_u32 s72, s72, 12288\n"
"v_mfma_f32_16x16x32_bf16 v[112:115], a[100:103], a[16:19], v[112:115]\n"
"ds_read_b64_tr_b16 a[138:139], v224 offset:35840\n"
"v_mfma_f32_16x16x32_bf16 v[112:115], a[140:143], a[20:23], v[112:115]\n"
"s_lshl_b32 s70, s80, 2\n"
"v_mfma_f32_16x16x32_bf16 v[116:119], a[100:103], a[32:35], v[116:119]\n"
"s_cmp_eq_u32 s72, 393216\n"
"s_cselect_b32 s72, 0, s72\n"
"v_mfma_f32_16x16x32_bf16 v[116:119], a[140:143], a[36:39], v[116:119]\n"
"ds_read_b64_tr_b16 a[100:101], v225 offset:20480\n"
"v_mfma_f32_16x16x32_bf16 v[120:123], a[104:107], a[0:3], v[120:123]\n"
"ds_read_b64_tr_b16 a[102:103], v225 offset:25600\n"
"buffer_load_dwordx4 a[188:191], v246, s[44:47], s72 offen sc1\n"
"v_mfma_f32_16x16x32_bf16 v[120:123], a[144:147], a[4:7], v[120:123]\n"
"ds_read_b64_tr_b16 a[140:141], v225 offset:30720\n"
"v_mfma_f32_16x16x32_bf16 v[124:127], a[104:107], a[16:19], v[124:127]\n"
"ds_read_b64_tr_b16 a[142:143], v225 offset:35840\n"
"s_add_u32 s70, s70, s71\n"
"v_mfma_f32_16x16x32_bf16 v[124:127], a[144:147], a[20:23], v[124:127]\n"
"buffer_load_dwordx4 a[192:195], v246, s[44:47], s72 offen offset:1024 sc1\n"
"v_mfma_f32_16x16x32_bf16 v[128:131], a[104:107], a[32:35], v[128:131]\n"
"v_mfma_f32_16x16x32_bf16 v[128:131], a[144:147], a[36:39], v[128:131]\n"
"ds_read_b64_tr_b16 a[104:105], v224 offset:20992\n"
"buffer_load_dwordx4 a[196:199], v246, s[44:47], s72 offen offset:2048 sc1\n"
"v_mfma_f32_16x16x32_bf16 v[132:135], a[108:111], a[0:3], v[132:135]\n"
"ds_read_b64_tr_b16 a[106:107], v224 offset:26112\n"
"v_mfma_f32_16x16x32_bf16 v[132:135], a[148:151], a[4:7], v[132:135]\n"
"ds_read_b64_tr_b16 a[144:145], v224 offset:31232\n"
"s_lshl_b32 s95, s70, 9\n"
"v_mfma_f32_16x16x32_bf16 v[136:139], a[108:111], a[16:19], v[136:139]\n"
"ds_read_b64_tr_b16 a[146:147], v224 offset:36352\n"
"s_add_u32 s72, s72, 12288\n"
"v_mfma_f32_16x16x32_bf16 v[136:139], a[148:151], a[20:23], v[136:139]\n"
"v_mfma_f32_16x16x32_bf16 v[140:143], a[108:111], a[32:35], v[140:143]\n"
"s_cmp_eq_u32 s72, 393216\n"
"s_cselect_b32 s72, 0, s72\n"
"v_mfma_f32_16x16x32_bf16 v[140:143], a[148:151], a[36:39], v[140:143]\n"
"ds_read_b64_tr_b16 a[108:109], v225 offset:20992\n"
"v_mfma_f32_16x16x32_bf16 v[144:147], a[112:115], a[0:3], v[144:147]\n"
"ds_read_b64_tr_b16 a[110:111], v225 offset:26112\n"
"s_add_u32 s95, s95, 65536\n"
"v_mfma_f32_16x16x32_bf16 v[144:147], a[152:155], a[4:7], v[144:147]\n"
"ds_read_b64_tr_b16 a[148:149], v225 offset:31232\n"
"s_add_u32 m0, s78, 143360\n"
"v_mfma_f32_16x16x32_bf16 v[148:151], a[112:115], a[16:19], v[148:151]\n"
"ds_read_b64_tr_b16 a[150:151], v225 offset:36352\n"
"v_mfma_f32_16x16x32_bf16 v[148:151], a[152:155], a[20:23], v[148:151]\n"
"buffer_load_dwordx4 v244, s[40:43], s73 offen lds\n"
"v_mfma_f32_16x16x32_bf16 v[152:155], a[112:115], a[32:35], v[152:155]\n"
"v_mfma_f32_16x16x32_bf16 v[152:155], a[152:155], a[36:39], v[152:155]\n"
"ds_read_b64_tr_b16 a[112:113], v224 offset:21504\n"
"s_mul_i32 s70, s70, 245760\n"
"v_mfma_f32_16x16x32_bf16 v[156:159], a[116:119], a[0:3], v[156:159]\n"
"ds_read_b64_tr_b16 a[114:115], v224 offset:26624\n"
"s_add_u32 m0, s78, 144256\n"
"v_mfma_f32_16x16x32_bf16 v[156:159], a[156:159], a[4:7], v[156:159]\n"
"ds_read_b64_tr_b16 a[152:153], v224 offset:31744\n"
"v_mfma_f32_16x16x32_bf16 v[160:163], a[116:119], a[16:19], v[160:163]\n"
"ds_read_b64_tr_b16 a[154:155], v224 offset:36864\n"
"v_mfma_f32_16x16x32_bf16 v[160:163], a[156:159], a[20:23], v[160:163]\n"
"buffer_load_dwordx4 v244, s[40:43], s73 offen offset:128 lds\n"
"v_mfma_f32_16x16x32_bf16 v[164:167], a[116:119], a[32:35], v[164:167]\n"
"s_add_u32 s70, s70, 41943040\n"
"v_mfma_f32_16x16x32_bf16 v[164:167], a[156:159], a[36:39], v[164:167]\n"
"s_waitcnt lgkmcnt(15)\n"
"ds_read_b64_tr_b16 a[116:117], v225 offset:21504\n"
"v_mfma_f32_16x16x32_bf16 v[168:171], a[120:123], a[0:3], v[168:171]\n"
"ds_read_b64_tr_b16 a[118:119], v225 offset:26624\n"
"s_add_u32 m0, s78, 145152\n"
"v_mfma_f32_16x16x32_bf16 v[168:171], a[160:163], a[4:7], v[168:171]\n"
"ds_read_b64_tr_b16 a[156:157], v225 offset:31744\n"
"v_mfma_f32_16x16x32_bf16 v[172:175], a[120:123], a[16:19], v[172:175]\n"
"ds_read_b64_tr_b16 a[158:159], v225 offset:36864\n"
"v_mfma_f32_16x16x32_bf16 v[172:175], a[160:163], a[20:23], v[172:175]\n"
"buffer_load_dwordx4 v245, s[40:43], s73 offen offset:256 lds\n"
"v_mfma_f32_16x16x32_bf16 v[176:179], a[120:123], a[32:35], v[176:179]\n"
"s_add_u32 s48, %8, s70\n"
"s_addc_u32 s49, %9, 0\n"
"v_mfma_f32_16x16x32_bf16 v[176:179], a[160:163], a[36:39], v[176:179]\n"
"ds_read_b64_tr_b16 a[120:121], v224 offset:22016\n"
"s_add_u32 m0, s78, 146368\n"
"s_add_u32 s64, s73, 10240\n"
"v_mfma_f32_16x16x32_bf16 v[180:183], a[124:127], a[0:3], v[180:183]\n"
"ds_read_b64_tr_b16 a[122:123], v224 offset:27136\n"
"v_mfma_f32_16x16x32_bf16 v[180:183], a[164:167], a[4:7], v[180:183]\n"
"ds_read_b64_tr_b16 a[160:161], v224 offset:32256\n"
"v_mfma_f32_16x16x32_bf16 v[184:187], a[124:127], a[16:19], v[184:187]\n"
"ds_read_b64_tr_b16 a[162:163], v224 offset:37376\n"
"s_and_b32 s49, s49, 0xffff\n"
"v_mfma_f32_16x16x32_bf16 v[184:187], a[164:167], a[20:23], v[184:187]\n"
"buffer_load_dwordx4 v244, s[40:43], s64 offen offset:64 lds\n"
"v_mfma_f32_16x16x32_bf16 v[188:191], a[124:127], a[32:35], v[188:191]\n"
"s_add_u32 m0, s78, 147264\n"
"v_mfma_f32_16x16x32_bf16 v[188:191], a[164:167], a[36:39], v[188:191]\n"
"ds_read_b64_tr_b16 a[124:125], v225 offset:22016\n"
"v_mfma_f32_16x16x32_bf16 v[192:195], a[128:131], a[0:3], v[192:195]\n"
"ds_read_b64_tr_b16 a[126:127], v225 offset:27136\n"
"v_mfma_f32_16x16x32_bf16 v[192:195], a[168:171], a[4:7], v[192:195]\n"
"ds_read_b64_tr_b16 a[164:165], v225 offset:32256\n"
"s_mov_b32 s50, 245760\n"
"v_mfma_f32_16x16x32_bf16 v[196:199], a[128:131], a[16:19], v[196:199]\n"
"ds_read_b64_tr_b16 a[166:167], v225 offset:37376\n"
"v_mfma_f32_16x16x32_bf16 v[196:199], a[168:171], a[20:23], v[196:199]\n"
"buffer_load_dwordx4 v244, s[40:43], s64 offen offset:192 lds\n"
"v_mfma_f32_16x16x32_bf16 v[200:203], a[128:131], a[32:35], v[200:203]\n"
"s_add_u32 s73, s73, 81920\n"
"s_cmp_eq_u32 s73, 1310720\n"
"s_cselect_b32 s73, 0, s73\n"
"v_mfma_f32_16x16x32_bf16 v[200:203], a[168:171], a[36:39], v[200:203]\n"
"ds_read_b64_tr_b16 a[128:129], v224 offset:22528\n"
"v_mfma_f32_16x16x32_bf16 v[204:207], a[132:135], a[0:3], v[204:207]\n"
"ds_read_b64_tr_b16 a[130:131], v224 offset:27648\n"
"s_mov_b32 s51, 0x20000\n"
"v_mfma_f32_16x16x32_bf16 v[204:207], a[172:175], a[4:7], v[204:207]\n"
"ds_read_b64_tr_b16 a[168:169], v224 offset:32768\n"
"v_mfma_f32_16x16x32_bf16 v[208:211], a[132:135], a[16:19], v[208:211]\n"
"ds_read_b64_tr_b16 a[170:171], v224 offset:37888\n"
"v_mfma_f32_16x16x32_bf16 v[208:211], a[172:175], a[20:23], v[208:211]\n"
"v_mfma_f32_16x16x32_bf16 v[212:215], a[132:135], a[32:35], v[212:215]\n"
"v_mfma_f32_16x16x32_bf16 v[212:215], a[172:175], a[36:39], v[212:215]\n"
"ds_read_b64_tr_b16 a[132:133], v225 offset:22528\n"
"ds_read_b64_tr_b16 a[134:135], v225 offset:27648\n"
"ds_read_b64_tr_b16 a[172:173], v225 offset:32768\n"
"ds_read_b64_tr_b16 a[174:175], v225 offset:37888\n"
"s_waitcnt vmcnt(23) lgkmcnt(15)\n"
"s_barrier\n"
"v_mfma_f32_16x16x32_bf16 v[96:99], a[96:99], a[8:11], v[96:99]\n"
"buffer_load_dwordx4 a[200:203], v246, s[44:47], s72 offen sc1\n"
"s_and_b32 s70, s80, 7\n"
"v_mfma_f32_16x16x32_bf16 v[96:99], a[136:139], a[12:15], v[96:99]\n"
"v_mfma_f32_16x16x32_bf16 v[100:103], a[96:99], a[24:27], v[100:103]\n"
"buffer_load_dwordx4 a[204:207], v246, s[44:47], s72 offen offset:1024 sc1\n"
"v_mfma_f32_16x16x32_bf16 v[100:103], a[136:139], a[28:31], v[100:103]\n"
"s_mul_i32 s70, s70, 192\n"
"v_mfma_f32_16x16x32_bf16 v[104:107], a[96:99], a[40:43], v[104:107]\n"
"v_mfma_f32_16x16x32_bf16 v[104:107], a[136:139], a[44:47], v[104:107]\n"
"ds_read_b64_tr_b16 a[96:97], v218 offset:20480\n"
"buffer_load_dwordx4 a[208:211], v246, s[44:47], s72 offen offset:2048 sc1\n"
"v_mfma_f32_16x16x32_bf16 v[108:111], a[100:103], a[8:11], v[108:111]\n"
"ds_read_b64_tr_b16 a[98:99], v218 offset:25600\n"
"s_and_b32 s71, s60, 1\n"
"v_mfma_f32_16x16x32_bf16 v[108:111], a[140:143], a[12:15], v[108:111]\n"
"ds_read_b64_tr_b16 a[136:137], v218 offset:30720\n"
"s_add_u32 s72, s72, 12288\n"
"v_mfma_f32_16x16x32_bf16 v[112:115], a[100:103], a[24:27], v[112:115]\n"
"ds_read_b64_tr_b16 a[138:139], v218 offset:35840\n"
"v_mfma_f32_16x16x32_bf16 v[112:115], a[140:143], a[28:31], v[112:115]\n"
"s_mul_i32 s71, s71, 96\n"
"v_mfma_f32_16x16x32_bf16 v[116:119], a[100:103], a[40:43], v[116:119]\n"
"s_cmp_eq_u32 s72, 393216\n"
"s_cselect_b32 s72, 0, s72\n"
"v_mfma_f32_16x16x32_bf16 v[116:119], a[140:143], a[44:47], v[116:119]\n"
"ds_read_b64_tr_b16 a[100:101], v219 offset:20480\n"
"v_mfma_f32_16x16x32_bf16 v[120:123], a[104:107], a[8:11], v[120:123]\n"
"ds_read_b64_tr_b16 a[102:103], v219 offset:25600\n"
"buffer_load_dwordx4 a[212:215], v246, s[44:47], s72 offen sc1\n"
"v_mfma_f32_16x16x32_bf16 v[120:123], a[144:147], a[12:15], v[120:123]\n"
"ds_read_b64_tr_b16 a[140:141], v219 offset:30720\n"
"s_add_u32 s70, s70, s71\n"
"v_mfma_f32_16x16x32_bf16 v[124:127], a[104:107], a[24:27], v[124:127]\n"
"ds_read_b64_tr_b16 a[142:143], v219 offset:35840\n"
"v_mfma_f32_16x16x32_bf16 v[124:127], a[144:147], a[28:31], v[124:127]\n"
"buffer_load_dwordx4 a[216:219], v246, s[44:47], s72 offen offset:1024 sc1\n"
"s_lshr_b32 s71, s60, 1\n"
"v_mfma_f32_16x16x32_bf16 v[128:131], a[104:107], a[40:43], v[128:131]\n"
"v_mfma_f32_16x16x32_bf16 v[128:131], a[144:147], a[44:47], v[128:131]\n"
"ds_read_b64_tr_b16 a[104:105], v218 offset:20992\n"
"buffer_load_dwordx4 a[220:223], v246, s[44:47], s72 offen offset:2048 sc1\n"
"v_mfma_f32_16x16x32_bf16 v[132:135], a[108:111], a[8:11], v[132:135]\n"
"ds_read_b64_tr_b16 a[106:107], v218 offset:26112\n"
"s_mul_i32 s71, s71, 48\n"
"v_mfma_f32_16x16x32_bf16 v[132:135], a[148:151], a[12:15], v[132:135]\n"
"ds_read_b64_tr_b16 a[144:145], v218 offset:31232\n"
"v_mfma_f32_16x16x32_bf16 v[136:139], a[108:111], a[24:27], v[136:139]\n"
"ds_read_b64_tr_b16 a[146:147], v218 offset:36352\n"
"s_add_u32 s72, s72, 12288\n"
"v_mfma_f32_16x16x32_bf16 v[136:139], a[148:151], a[28:31], v[136:139]\n"
"s_add_u32 s70, s70, s71\n"
"v_mfma_f32_16x16x32_bf16 v[140:143], a[108:111], a[40:43], v[140:143]\n"
"s_cmp_eq_u32 s72, 393216\n"
"s_cselect_b32 s72, 0, s72\n"
"v_mfma_f32_16x16x32_bf16 v[140:143], a[148:151], a[44:47], v[140:143]\n"
"ds_read_b64_tr_b16 a[108:109], v219 offset:20992\n"
"v_mfma_f32_16x16x32_bf16 v[144:147], a[112:115], a[8:11], v[144:147]\n"
"ds_read_b64_tr_b16 a[110:111], v219 offset:26112\n"
"s_mul_i32 s70, s70, 2560\n"
"v_mfma_f32_16x16x32_bf16 v[144:147], a[152:155], a[12:15], v[144:147]\n"
"ds_read_b64_tr_b16 a[148:149], v219 offset:31232\n"
"s_add_u32 m0, s78, 0\n"
"v_mfma_f32_16x16x32_bf16 v[148:151], a[112:115], a[24:27], v[148:151]\n"
"ds_read_b64_tr_b16 a[150:151], v219 offset:36352\n"
"v_mfma_f32_16x16x32_bf16 v[148:151], a[152:155], a[28:31], v[148:151]\n"
"buffer_load_dwordx4 v244, s[40:43], s73 offen lds\n"
"v_mfma_f32_16x16x32_bf16 v[152:155], a[112:115], a[40:43], v[152:155]\n"
"s_lshr_b32 s71, s80, 3\n"
"v_mfma_f32_16x16x32_bf16 v[152:155], a[152:155], a[44:47], v[152:155]\n"
"ds_read_b64_tr_b16 a[112:113], v218 offset:21504\n"
"v_mfma_f32_16x16x32_bf16 v[156:159], a[116:119], a[8:11], v[156:159]\n"
"ds_read_b64_tr_b16 a[114:115], v218 offset:26624\n"
"s_add_u32 m0, s78, 896\n"
"v_mfma_f32_16x16x32_bf16 v[156:159], a[156:159], a[12:15], v[156:159]\n"
"ds_read_b64_tr_b16 a[152:153], v218 offset:31744\n"
"s_mul_i32 s71, s71, 1280\n"
"v_mfma_f32_16x16x32_bf16 v[160:163], a[116:119], a[24:27], v[160:163]\n"
"ds_read_b64_tr_b16 a[154:155], v218 offset:36864\n"
"v_mfma_f32_16x16x32_bf16 v[160:163], a[156:159], a[28:31], v[160:163]\n"
"buffer_load_dwordx4 v244, s[40:43], s73 offen offset:128 lds\n"
"v_mfma_f32_16x16x32_bf16 v[164:167], a[116:119], a[40:43], v[164:167]\n"
"s_add_u32 s70, s70, s71\n"
"v_mfma_f32_16x16x32_bf16 v[164:167], a[156:159], a[44:47], v[164:167]\n"
"s_waitcnt lgkmcnt(15)\n"
"ds_read_b64_tr_b16 a[116:117], v219 offset:21504\n"
"v_mfma_f32_16x16x32_bf16 v[168:171], a[120:123], a[8:11], v[168:171]\n"
"ds_read_b64_tr_b16 a[118:119], v219 offset:26624\n"
"s_add_u32 m0, s78, 1792\n"
"v_mfma_f32_16x16x32_bf16 v[168:171], a[160:163], a[12:15], v[168:171]\n"
"ds_read_b64_tr_b16 a[156:157], v219 offset:31744\n"
"s_bfe_u32 s71, %15, 0x20003\n"
"v_mfma_f32_16x16x32_bf16 v[172:175], a[120:123], a[24:27], v[172:175]\n"
"ds_read_b64_tr_b16 a[158:159], v219 offset:36864\n"
"v_mfma_f32_16x16x32_bf16 v[172:175], a[160:163], a[28:31], v[172:175]\n"
"buffer_load_dwordx4 v245, s[40:43], s73 offen offset:256 lds\n"
"v_mfma_f32_16x16x32_bf16 v[176:179], a[120:123], a[40:43], v[176:179]\n"
"s_add_u32 m0, s78, 3008\n"
"s_add_u32 s64, s73, 10240\n"
"v_mfma_f32_16x16x32_bf16 v[176:179], a[160:163], a[44:47], v[176:179]\n"
"ds_read_b64_tr_b16 a[120:121], v218 offset:22016\n"
"s_mul_i32 s71, s71, 320\n"
"v_mfma_f32_16x16x32_bf16 v[180:183], a[124:127], a[8:11], v[180:183]\n"
"ds_read_b64_tr_b16 a[122:123], v218 offset:27136\n"
"v_mfma_f32_16x16x32_bf16 v[180:183], a[164:167], a[12:15], v[180:183]\n"
"ds_read_b64_tr_b16 a[160:161], v218 offset:32256\n"
"s_add_u32 s70, s70, s71\n"
"v_mfma_f32_16x16x32_bf16 v[184:187], a[124:127], a[24:27], v[184:187]\n"
"ds_read_b64_tr_b16 a[162:163], v218 offset:37376\n"
"v_mfma_f32_16x16x32_bf16 v[184:187], a[164:167], a[28:31], v[184:187]\n"
"buffer_load_dwordx4 v244, s[40:43], s64 offen offset:64 lds\n"
"v_mfma_f32_16x16x32_bf16 v[188:191], a[124:127], a[40:43], v[188:191]\n"
"s_add_u32 m0, s78, 3904\n"
"s_add_u32 s52, %12, s70\n"
"s_addc_u32 s53, %13, 0\n"
"v_mfma_f32_16x16x32_bf16 v[188:191], a[164:167], a[44:47], v[188:191]\n"
"ds_read_b64_tr_b16 a[124:125], v219 offset:22016\n"
"v_mfma_f32_16x16x32_bf16 v[192:195], a[128:131], a[8:11], v[192:195]\n"
"ds_read_b64_tr_b16 a[126:127], v219 offset:27136\n"
"v_mfma_f32_16x16x32_bf16 v[192:195], a[168:171], a[12:15], v[192:195]\n"
"ds_read_b64_tr_b16 a[164:165], v219 offset:32256\n"
"s_and_b32 s53, s53, 0xffff\n"
"v_mfma_f32_16x16x32_bf16 v[196:199], a[128:131], a[24:27], v[196:199]\n"
"ds_read_b64_tr_b16 a[166:167], v219 offset:37376\n"
"v_mfma_f32_16x16x32_bf16 v[196:199], a[168:171], a[28:31], v[196:199]\n"
"buffer_load_dwordx4 v244, s[40:43], s64 offen offset:192 lds\n"
"v_mfma_f32_16x16x32_bf16 v[200:203], a[128:131], a[40:43], v[200:203]\n"
"s_add_u32 s73, s73, 81920\n"
"s_cmp_eq_u32 s73, 1310720\n"
"s_cselect_b32 s73, 0, s73\n"
"v_mfma_f32_16x16x32_bf16 v[200:203], a[168:171], a[44:47], v[200:203]\n"
"ds_read_b64_tr_b16 a[128:129], v218 offset:22528\n"
"s_mov_b32 s54, 120640\n"
"v_mfma_f32_16x16x32_bf16 v[204:207], a[132:135], a[8:11], v[204:207]\n"
"ds_read_b64_tr_b16 a[130:131], v218 offset:27648\n"
"v_mfma_f32_16x16x32_bf16 v[204:207], a[172:175], a[12:15], v[204:207]\n"
"ds_read_b64_tr_b16 a[168:169], v218 offset:32768\n"
"s_mov_b32 s55, 0x20000\n"
"v_mfma_f32_16x16x32_bf16 v[208:211], a[132:135], a[24:27], v[208:211]\n"
"ds_read_b64_tr_b16 a[170:171], v218 offset:37888\n"
"v_mfma_f32_16x16x32_bf16 v[208:211], a[172:175], a[28:31], v[208:211]\n"
"v_mfma_f32_16x16x32_bf16 v[212:215], a[132:135], a[40:43], v[212:215]\n"
"v_mfma_f32_16x16x32_bf16 v[212:215], a[172:175], a[44:47], v[212:215]\n"
"ds_read_b64_tr_b16 a[132:133], v219 offset:22528\n"
"ds_read_b64_tr_b16 a[134:135], v219 offset:27648\n"
"ds_read_b64_tr_b16 a[172:173], v219 offset:32768\n"
"ds_read_b64_tr_b16 a[174:175], v219 offset:37888\n"
"s_waitcnt vmcnt(16) lgkmcnt(15)\n"
"s_barrier\n"
"v_mfma_f32_16x16x32_bf16 v[96:99], a[96:99], a[176:179], v[96:99]\n"
"buffer_load_dwordx2 v[232:233], v238, s[36:39], s93 offen sc1\n"
"v_mfma_f32_16x16x32_bf16 v[96:99], a[136:139], a[188:191], v[96:99]\n"
"buffer_load_dwordx4 a[224:227], v246, s[44:47], s72 offen sc1\n"
"v_mfma_f32_16x16x32_bf16 v[100:103], a[96:99], a[180:183], v[100:103]\n"
"v_mfma_f32_16x16x32_bf16 v[100:103], a[136:139], a[192:195], v[100:103]\n"
"buffer_load_dwordx4 a[228:231], v246, s[44:47], s72 offen offset:1024 sc1\n"
"v_mfma_f32_16x16x32_bf16 v[104:107], a[96:99], a[184:187], v[104:107]\n"
"buffer_load_dwordx4 a[232:235], v246, s[44:47], s72 offen offset:2048 sc1\n"
"v_mfma_f32_16x16x32_bf16 v[104:107], a[136:139], a[196:199], v[104:107]\n"
"ds_read_b64_tr_b16 a[96:97], v218 offset:40960\n"
"v_mfma_f32_16x16x32_bf16 v[108:111], a[100:103], a[176:179], v[108:111]\n"
"ds_read_b64_tr_b16 a[98:99], v218 offset:46080\n"
"s_add_u32 s72, s72, 12288\n"
"v_mfma_f32_16x16x32_bf16 v[108:111], a[140:143], a[188:191], v[108:111]\n"
"ds_read_b64_tr_b16 a[136:137], v218 offset:51200\n"
"v_mfma_f32_16x16x32_bf16 v[112:115], a[100:103], a[180:183], v[112:115]\n"
"ds_read_b64_tr_b16 a[138:139], v218 offset:56320\n"
"v_mfma_f32_16x16x32_bf16 v[112:115], a[140:143], a[192:195], v[112:115]\n"
"s_cmp_eq_u32 s72, 393216\n"
"s_cselect_b32 s72, 0, s72\n"
"v_mfma_f32_16x16x32_bf16 v[116:119], a[100:103], a[184:187], v[116:119]\n"
"buffer_load_dwordx4 a[236:239], v246, s[44:47], s72 offen sc1\n"
"v_mfma_f32_16x16x32_bf16 v[116:119], a[140:143], a[196:199], v[116:119]\n"
"ds_read_b64_tr_b16 a[100:101], v219 offset:40960\n"
"buffer_load_dwordx4 a[240:243], v246, s[44:47], s72 offen offset:1024 sc1\n"
"v_mfma_f32_16x16x32_bf16 v[120:123], a[104:107], a[176:179], v[120:123]\n"
"ds_read_b64_tr_b16 a[102:103], v219 offset:46080\n"
"buffer_load_dwordx4 a[244:247], v246, s[44:47], s72 offen offset:2048 sc1\n"
"v_mfma_f32_16x16x32_bf16 v[120:123], a[144:147], a[188:191], v[120:123]\n"
"ds_read_b64_tr_b16 a[140:141], v219 offset:51200\n"
"v_mfma_f32_16x16x32_bf16 v[124:127], a[104:107], a[180:183], v[124:127]\n"
"ds_read_b64_tr_b16 a[142:143], v219 offset:56320\n"
"s_add_u32 s72, s72, 12288\n"
"v_mfma_f32_16x16x32_bf16 v[124:127], a[144:147], a[192:195], v[124:127]\n"
"s_cmp_eq_u32 s72, 393216\n"
"s_cselect_b32 s72, 0, s72\n"
"v_mfma_f32_16x16x32_bf16 v[128:131], a[104:107], a[184:187], v[128:131]\n"
"v_mfma_f32_16x16x32_bf16 v[128:131], a[144:147], a[196:199], v[128:131]\n"
"ds_read_b64_tr_b16 a[104:105], v218 offset:41472\n"
"s_add_u32 m0, s78, 20480\n"
"v_mfma_f32_16x16x32_bf16 v[132:135], a[108:111], a[176:179], v[132:135]\n"
"ds_read_b64_tr_b16 a[106:107], v218 offset:46592\n"
"v_mfma_f32_16x16x32_bf16 v[132:135], a[148:151], a[188:191], v[132:135]\n"
"ds_read_b64_tr_b16 a[144:145], v218 offset:51712\n"
"v_mfma_f32_16x16x32_bf16 v[136:139], a[108:111], a[180:183], v[136:139]\n"
"ds_read_b64_tr_b16 a[146:147], v218 offset:56832\n"
"v_mfma_f32_16x16x32_bf16 v[136:139], a[148:151], a[192:195], v[136:139]\n"
"buffer_load_dwordx4 v244, s[40:43], s73 offen lds\n"
"v_mfma_f32_16x16x32_bf16 v[140:143], a[108:111], a[184:187], v[140:143]\n"
"s_add_u32 m0, s78, 21376\n"
"s_nop 0\n"
"buffer_load_dwordx4 v244, s[40:43], s73 offen offset:128 lds\n"
"v_mfma_f32_16x16x32_bf16 v[140:143], a[148:151], a[196:199], v[140:143]\n"
"ds_read_b64_tr_b16 a[108:109], v219 offset:41472\n"
"s_add_u32 m0, s78, 22272\n"
"v_mfma_f32_16x16x32_bf16 v[144:147], a[112:115], a[176:179], v[144:147]\n"
"ds_read_b64_tr_b16 a[110:111], v219 offset:46592\n"
"v_mfma_f32_16x16x32_bf16 v[144:147], a[152:155], a[188:191], v[144:147]\n"
"ds_read_b64_tr_b16 a[148:149], v219 offset:51712\n"
"v_mfma_f32_16x16x32_bf16 v[148:151], a[112:115], a[180:183], v[148:151]\n"
"ds_read_b64_tr_b16 a[150:151], v219 offset:56832\n"
"v_mfma_f32_16x16x32_bf16 v[148:151], a[152:155], a[192:195], v[148:151]\n"
"buffer_load_dwordx4 v245, s[40:43], s73 offen offset:256 lds\n"
"v_mfma_f32_16x16x32_bf16 v[152:155], a[112:115], a[184:187], v[152:155]\n"
"s_add_u32 m0, s78, 23488\n"
"s_add_u32 s64, s73, 10240\n"
"v_mfma_f32_16x16x32_bf16 v[152:155], a[152:155], a[196:199], v[152:155]\n"
"ds_read_b64_tr_b16 a[112:113], v218 offset:41984\n"
"buffer_load_dwordx4 v244, s[40:43], s64 offen offset:64 lds\n"
"v_mfma_f32_16x16x32_bf16 v[156:159], a[116:119], a[176:179], v[156:159]\n"
"ds_read_b64_tr_b16 a[114:115], v218 offset:47104\n"
"s_add_u32 m0, s78, 24384\n"
"v_mfma_f32_16x16x32_bf16 v[156:159], a[156:159], a[188:191], v[156:159]\n"
"ds_read_b64_tr_b16 a[152:153], v218 offset:52224\n"
"v_mfma_f32_16x16x32_bf16 v[160:163], a[116:119], a[180:183], v[160:163]\n"
"ds_read_b64_tr_b16 a[154:155], v218 offset:57344\n"
"v_mfma_f32_16x16x32_bf16 v[160:163], a[156:159], a[192:195], v[160:163]\n"
"buffer_load_dwordx4 v244, s[40:43], s64 offen offset:192 lds\n"
"v_mfma_f32_16x16x32_bf16 v[164:167], a[116:119], a[184:187], v[164:167]\n"
"s_add_u32 s73, s73, 81920\n"
"s_cmp_eq_u32 s73, 1310720\n"
"s_cselect_b32 s73, 0, s73\n"
"v_mfma_f32_16x16x32_bf16 v[164:167], a[156:159], a[196:199], v[164:167]\n"
"s_waitcnt lgkmcnt(15)\n"
"ds_read_b64_tr_b16 a[116:117], v219 offset:41984\n"
"v_mfma_f32_16x16x32_bf16 v[168:171], a[120:123], a[176:179], v[168:171]\n"
"ds_read_b64_tr_b16 a[118:119], v219 offset:47104\n"
"s_add_u32 m0, s78, 40960\n"
"v_mfma_f32_16x16x32_bf16 v[168:171], a[160:163], a[188:191], v[168:171]\n"
"ds_read_b64_tr_b16 a[156:157], v219 offset:52224\n"
"v_mfma_f32_16x16x32_bf16 v[172:175], a[120:123], a[180:183], v[172:175]\n"
"ds_read_b64_tr_b16 a[158:159], v219 offset:57344\n"
"v_mfma_f32_16x16x32_bf16 v[172:175], a[160:163], a[192:195], v[172:175]\n"
"buffer_load_dwordx4 v244, s[40:43], s73 offen lds\n"
"v_mfma_f32_16x16x32_bf16 v[176:179], a[120:123], a[184:187], v[176:179]\n"
"s_add_u32 m0, s78, 41856\n"
"s_nop 0\n"
"buffer_load_dwordx4 v244, s[40:43], s73 offen offset:128 lds\n"
"v_mfma_f32_16x16x32_bf16 v[176:179], a[160:163], a[196:199], v[176:179]\n"
"ds_read_b64_tr_b16 a[120:121], v218 offset:42496\n"
"v_mfma_f32_16x16x32_bf16 v[180:183], a[124:127], a[176:179], v[180:183]\n"
"ds_read_b64_tr_b16 a[122:123], v218 offset:47616\n"
"s_add_u32 m0, s78, 42752\n"
"v_mfma_f32_16x16x32_bf16 v[180:183], a[164:167], a[188:191], v[180:183]\n"
"ds_read_b64_tr_b16 a[160:161], v218 offset:52736\n"
"v_mfma_f32_16x16x32_bf16 v[184:187], a[124:127], a[180:183], v[184:187]\n"
"ds_read_b64_tr_b16 a[162:163], v218 offset:57856\n"
"v_mfma_f32_16x16x32_bf16 v[184:187], a[164:167], a[192:195], v[184:187]\n"
"buffer_load_dwordx4 v245, s[40:43], s73 offen offset:256 lds\n"
"v_mfma_f32_16x16x32_bf16 v[188:191], a[124:127], a[184:187], v[188:191]\n"
"s_add_u32 m0, s78, 43968\n"
"s_add_u32 s64, s73, 10240\n"
"v_mfma_f32_16x16x32_bf16 v[188:191], a[164:167], a[196:199], v[188:191]\n"
"ds_read_b64_tr_b16 a[124:125], v219 offset:42496\n"
"v_mfma_f32_16x16x32_bf16 v[192:195], a[128:131], a[176:179], v[192:195]\n"
"ds_read_b64_tr_b16 a[126:127], v219 offset:47616\n"
"v_mfma_f32_16x16x32_bf16 v[192:195], a[168:171], a[188:191], v[192:195]\n"
"ds_read_b64_tr_b16 a[164:165], v219 offset:52736\n"
"v_mfma_f32_16x16x32_bf16 v[196:199], a[128:131], a[180:183], v[196:199]\n"
"ds_read_b64_tr_b16 a[166:167], v219 offset:57856\n"
"v_mfma_f32_16x16x32_bf16 v[196:199], a[168:171], a[192:195], v[196:199]\n"
"buffer_load_dwordx4 v244, s[40:43], s64 offen offset:64 lds\n"
"v_mfma_f32_16x16x32_bf16 v[200:203], a[128:131], a[184:187], v[200:203]\n"
"s_add_u32 m0, s78, 44864\n"
"s_nop 0\n"
"buffer_load_dwordx4 v244, s[40:43], s64 offen offset:192 lds\n"
"v_mfma_f32_16x16x32_bf16 v[200:203], a[168:171], a[196:199], v[200:203]\n"
"ds_read_b64_tr_b16 a[128:129], v218 offset:43008\n"
"s_add_u32 s73, s73, 81920\n"
"v_mfma_f32_16x16x32_bf16 v[204:207], a[132:135], a[176:179], v[204:207]\n"
"ds_read_b64_tr_b16 a[130:131], v218 offset:48128\n"
"s_cmp_eq_u32 s73, 1310720\n"
"s_cselect_b32 s73, 0, s73\n"
"v_mfma_f32_16x16x32_bf16 v[204:207], a[172:175], a[188:191], v[204:207]\n"
"ds_read_b64_tr_b16 a[168:169], v218 offset:53248\n"
"v_mfma_f32_16x16x32_bf16 v[208:211], a[132:135], a[180:183], v[208:211]\n"
"ds_read_b64_tr_b16 a[170:171], v218 offset:58368\n"
"v_mfma_f32_16x16x32_bf16 v[208:211], a[172:175], a[192:195], v[208:211]\n"
"v_mfma_f32_16x16x32_bf16 v[212:215], a[132:135], a[184:187], v[212:215]\n"
"v_mfma_f32_16x16x32_bf16 v[212:215], a[172:175], a[196:199], v[212:215]\n"
"ds_read_b64_tr_b16 a[132:133], v219 offset:43008\n"
"ds_read_b64_tr_b16 a[134:135], v219 offset:48128\n"
"ds_read_b64_tr_b16 a[172:173], v219 offset:53248\n"
"ds_read_b64_tr_b16 a[174:175], v219 offset:58368\n"
"s_waitcnt vmcnt(16)\n"
"v_cmp_eq_u32_e64 s[98:99], s62, v232\n"
"v_cmp_eq_u32_e32 vcc, s65, v233\n"
"s_nop 0\n"
"s_and_b64 s[98:99], s[98:99], vcc\n"
"s_cmp_eq_u64 s[98:99], -1\n"
"s_cbranch_scc1 L_pf1d%=\n"
"s_mov_b32 s63, 0x40000\n"
"L_pf1%=:\n"
"buffer_load_dwordx2 v[232:233], v238, s[36:39], s93 offen sc1\n"
"s_waitcnt vmcnt(0)\n"
"v_cmp_eq_u32_e64 s[98:99], s62, v232\n"
"v_cmp_eq_u32_e32 vcc, s65, v233\n"
"s_nop 0\n"
"s_and_b64 s[98:99], s[98:99], vcc\n"
"s_cmp_eq_u64 s[98:99], -1\n"
"s_cbranch_scc1 L_pf1d%=\n"
"s_sleep 1\n"
"s_sub_u32 s63, s63, 1\n"
"s_cmp_lg_u32 s63, 0\n"
"s_cbranch_scc1 L_pf1%=\n"
"L_pf1d%=:\n"
"s_waitcnt vmcnt(22) lgkmcnt(15)\n"
"s_barrier\n"
"v_mfma_f32_16x16x32_bf16 v[96:99], a[96:99], a[200:203], v[96:99]\n"
"buffer_load_dwordx4 a[248:251], v246, s[44:47], s72 offen sc1\n"
"v_mfma_f32_16x16x32_bf16 v[96:99], a[136:139], a[212:215], v[96:99]\n"
"buffer_load_dwordx4 a[252:255], v246, s[44:47], s72 offen offset:1024 sc1\n"
"v_mfma_f32_16x16x32_bf16 v[100:103], a[96:99], a[204:207], v[100:103]\n"
"buffer_load_dwordx4 a[48:51], v246, s[44:47], s72 offen offset:2048 sc1\n"
"v_mfma_f32_16x16x32_bf16 v[100:103], a[136:139], a[216:219], v[100:103]\n"
"v_mfma_f32_16x16x32_bf16 v[104:107], a[96:99], a[208:211], v[104:107]\n"
"s_add_u32 s72, s72, 12288\n"
"v_mfma_f32_16x16x32_bf16 v[104:107], a[136:139], a[220:223], v[104:107]\n"
"ds_read_b64_tr_b16 a[96:97], v220 offset:0\n"
"v_mfma_f32_16x16x32_bf16 v[108:111], a[100:103], a[200:203], v[108:111]\n"
"ds_read_b64_tr_b16 a[98:99], v220 offset:5120\n"
"v_mfma_f32_16x16x32_bf16 v[108:111], a[140:143], a[212:215], v[108:111]\n"
"ds_read_b64_tr_b16 a[136:137], v220 offset:10240\n"
"v_mfma_f32_16x16x32_bf16 v[112:115], a[100:103], a[204:207], v[112:115]\n"
"ds_read_b64_tr_b16 a[138:139], v220 offset:15360\n"
"v_mfma_f32_16x16x32_bf16 v[112:115], a[140:143], a[216:219], v[112:115]\n"
"s_cmp_eq_u32 s72, 393216\n"
"s_cselect_b32 s72, 0, s72\n"
"v_mfma_f32_16x16x32_bf16 v[116:119], a[100:103], a[208:211], v[116:119]\n"
"buffer_load_dwordx4 a[52:55], v246, s[44:47], s72 offen sc1\n"
"buffer_load_dwordx4 a[56:59], v246, s[44:47], s72 offen offset:1024 sc1\n"
"v_mfma_f32_16x16x32_bf16 v[116:119], a[140:143], a[220:223], v[116:119]\n"
"ds_read_b64_tr_b16 a[100:101], v221 offset:0\n"
"buffer_load_dwordx4 a[60:63], v246, s[44:47], s72 offen offset:2048 sc1\n"
"v_mfma_f32_16x16x32_bf16 v[120:123], a[104:107], a[200:203], v[120:123]\n"
"ds_read_b64_tr_b16 a[102:103], v221 offset:5120\n"
"s_add_u32 s72, s72, 12288\n"
"v_mfma_f32_16x16x32_bf16 v[120:123], a[144:147], a[212:215], v[120:123]\n"
"ds_read_b64_tr_b16 a[140:141], v221 offset:10240\n"
"v_mfma_f32_16x16x32_bf16 v[124:127], a[104:107], a[204:207], v[124:127]\n"
"ds_read_b64_tr_b16 a[142:143], v221 offset:15360\n"
"v_mfma_f32_16x16x32_bf16 v[124:127], a[144:147], a[216:219], v[124:127]\n"
"s_cmp_eq_u32 s72, 393216\n"
"s_cselect_b32 s72, 0, s72\n"
"v_mfma_f32_16x16x32_bf16 v[128:131], a[104:107], a[208:211], v[128:131]\n"
"buffer_load_dwordx4 a[64:67], v246, s[44:47], s72 offen sc1\n"
"buffer_load_dwordx4 a[68:71], v246, s[44:47], s72 offen offset:1024 sc1\n"
"v_mfma_f32_16x16x32_bf16 v[128:131], a[144:147], a[220:223], v[128:131]\n"
"ds_read_b64_tr_b16 a[104:105], v220 offset:512\n"
"buffer_load_dwordx4 a[72:75], v246, s[44:47], s72 offen offset:2048 sc1\n"
"v_mfma_f32_16x16x32_bf16 v[132:135], a[108:111], a[200:203], v[132:135]\n"
"ds_read_b64_tr_b16 a[106:107], v220 offset:5632\n"
"s_add_u32 s72, s72, 12288\n"
"v_mfma_f32_16x16x32_bf16 v[132:135], a[148:151], a[212:215], v[132:135]\n"
"ds_read_b64_tr_b16 a[144:145], v220 offset:10752\n"
"v_mfma_f32_16x16x32_bf16 v[136:139], a[108:111], a[204:207], v[136:139]\n"
"ds_read_b64_tr_b16 a[146:147], v220 offset:15872\n"
"v_mfma_f32_16x16x32_bf16 v[136:139], a[148:151], a[216:219], v[136:139]\n"
"s_cmp_eq_u32 s72, 393216\n"
"s_cselect_b32 s72, 0, s72\n"
"v_mfma_f32_16x16x32_bf16 v[140:143], a[108:111], a[208:211], v[140:143]\n"
"s_add_u32 m0, s78, 61440\n"
"s_nop 0\n"
"buffer_load_dwordx4 v244, s[40:43], s73 offen lds\n"
"v_mfma_f32_16x16x32_bf16 v[140:143], a[148:151], a[220:223], v[140:143]\n"
"ds_read_b64_tr_b16 a[108:109], v221 offset:512\n"
"s_add_u32 m0, s78, 62336\n"
"v_mfma_f32_16x16x32_bf16 v[144:147], a[112:115], a[200:203], v[144:147]\n"
"ds_read_b64_tr_b16 a[110:111], v221 offset:5632\n"
"v_mfma_f32_16x16x32_bf16 v[144:147], a[152:155], a[212:215], v[144:147]\n"
"ds_read_b64_tr_b16 a[148:149], v221 offset:10752\n"
"v_mfma_f32_16x16x32_bf16 v[148:151], a[112:115], a[204:207], v[148:151]\n"
"ds_read_b64_tr_b16 a[150:151], v221 offset:15872\n"
"v_mfma_f32_16x16x32_bf16 v[148:151], a[152:155], a[216:219], v[148:151]\n"
"buffer_load_dwordx4 v244, s[40:43], s73 offen offset:128 lds\n"
"v_mfma_f32_16x16x32_bf16 v[152:155], a[112:115], a[208:211], v[152:155]\n"
"s_add_u32 m0, s78, 63232\n"
"s_nop 0\n"
"buffer_load_dwordx4 v245, s[40:43], s73 offen offset:256 lds\n"
"v_mfma_f32_16x16x32_bf16 v[152:155], a[152:155], a[220:223], v[152:155]\n"
"ds_read_b64_tr_b16 a[112:113], v220 offset:1024\n"
"v_mfma_f32_16x16x32_bf16 v[156:159], a[116:119], a[200:203], v[156:159]\n"
"ds_read_b64_tr_b16 a[114:115], v220 offset:6144\n"
"v_mfma_f32_16x16x32_bf16 v[156:159], a[156:159], a[212:215], v[156:159]\n"
"ds_read_b64_tr_b16 a[152:153], v220 offset:11264\n"
"v_mfma_f32_16x16x32_bf16 v[160:163], a[116:119], a[204:207], v[160:163]\n"
"ds_read_b64_tr_b16 a[154:155], v220 offset:16384\n"
"v_mfma_f32_16x16x32_bf16 v[160:163], a[156:159], a[216:219], v[160:163]\n"
"s_add_u32 m0, s78, 64448\n"
"s_add_u32 s64, s73, 10240\n"
"v_mfma_f32_16x16x32_bf16 v[164:167], a[116:119], a[208:211], v[164:167]\n"
"buffer_load_dwordx4 v244, s[40:43], s64 offen offset:64 lds\n"
"v_mfma_f32_16x16x32_bf16 v[164:167], a[156:159], a[220:223], v[164:167]\n"
"s_waitcnt lgkmcnt(15)\n"
"ds_read_b64_tr_b16 a[116:117], v221 offset:1024\n"
"v_mfma_f32_16x16x32_bf16 v[168:171], a[120:123], a[200:203], v[168:171]\n"
"ds_read_b64_tr_b16 a[118:119], v221 offset:6144\n"
"s_add_u32 m0, s78, 65344\n"
"v_mfma_f32_16x16x32_bf16 v[168:171], a[160:163], a[212:215], v[168:171]\n"
"ds_read_b64_tr_b16 a[156:157], v221 offset:11264\n"
"v_mfma_f32_16x16x32_bf16 v[172:175], a[120:123], a[204:207], v[172:175]\n"
"ds_read_b64_tr_b16 a[158:159], v221 offset:16384\n"
"v_mfma_f32_16x16x32_bf16 v[172:175], a[160:163], a[216:219], v[172:175]\n"
"buffer_load_dwordx4 v244, s[40:43], s64 offen offset:192 lds\n"
"v_mfma_f32_16x16x32_bf16 v[176:179], a[120:123], a[208:211], v[176:179]\n"
"s_add_u32 s73, s73, 81920\n"
"s_cmp_eq_u32 s73, 1310720\n"
"s_cselect_b32 s73, 0, s73\n"
"v_mfma_f32_16x16x32_bf16 v[176:179], a[160:163], a[220:223], v[176:179]\n"
"ds_read_b64_tr_b16 a[120:121], v220 offset:1536\n"
"s_add_u32 m0, s78, 81920\n"
"v_mfma_f32_16x16x32_bf16 v[180:183], a[124:127], a[200:203], v[180:183]\n"
"ds_read_b64_tr_b16 a[122:123], v220 offset:6656\n"
"v_mfma_f32_16x16x32_bf16 v[180:183], a[164:167], a[212:215], v[180:183]\n"
"ds_read_b64_tr_b16 a[160:161], v220 offset:11776\n"
"v_mfma_f32_16x16x32_bf16 v[184:187], a[124:127], a[204:207], v[184:187]\n"
"ds_read_b64_tr_b16 a[162:163], v220 offset:16896\n"
"v_mfma_f32_16x16x32_bf16 v[184:187], a[164:167], a[216:219], v[184:187]\n"
"buffer_load_dwordx4 v244, s[40:43], s73 offen lds\n"
"v_mfma_f32_16x16x32_bf16 v[188:191], a[124:127], a[208:211], v[188:191]\n"
"s_add_u32 m0, s78, 82816\n"
"s_nop 0\n"
"buffer_load_dwordx4 v244, s[40:43], s73 offen offset:128 lds\n"
"v_mfma_f32_16x16x32_bf16 v[188:191], a[164:167], a[220:223], v[188:191]\n"
"ds_read_b64_tr_b16 a[124:125], v221 offset:1536\n"
"s_add_u32 m0, s78, 83712\n"
"v_mfma_f32_16x16x32_bf16 v[192:195], a[128:131], a[200:203], v[192:195]\n"
"ds_read_b64_tr_b16 a[126:127], v221 offset:6656\n"
"v_mfma_f32_16x16x32_bf16 v[192:195], a[168:171], a[212:215], v[192:195]\n"
"ds_read_b64_tr_b16 a[164:165], v221 offset:11776\n"
"v_mfma_f32_16x16x32_bf16 v[196:199], a[128:131], a[204:207], v[196:199]\n"
"ds_read_b64_tr_b16 a[166:167], v221 offset:16896\n"
"v_mfma_f32_16x16x32_bf16 v[196:199], a[168:171], a[216:219], v[196:199]\n"
"buffer_load_dwordx4 v245, s[40:43], s73 offen offset:256 lds\n"
"v_mfma_f32_16x16x32_bf16 v[200:203], a[128:131], a[208:211], v[200:203]\n"
"s_add_u32 m0, s78, 84928\n"
"s_add_u32 s64, s73, 10240\n"
"v_mfma_f32_16x16x32_bf16 v[200:203], a[168:171], a[220:223], v[200:203]\n"
"ds_read_b64_tr_b16 a[128:129], v220 offset:2048\n"
"buffer_load_dwordx4 v244, s[40:43], s64 offen offset:64 lds\n"
"v_mfma_f32_16x16x32_bf16 v[204:207], a[132:135], a[200:203], v[204:207]\n"
"ds_read_b64_tr_b16 a[130:131], v220 offset:7168\n"
"s_add_u32 m0, s78, 85824\n"
"s_nop 0\n"
"buffer_load_dwordx4 v244, s[40:43], s64 offen offset:192 lds\n"
"s_add_u32 s73, s73, 81920\n"
"s_cmp_eq_u32 s73, 1310720\n"
"s_cselect_b32 s73, 0, s73\n"
"v_mfma_f32_16x16x32_bf16 v[204:207], a[172:175], a[212:215], v[204:207]\n"
"ds_read_b64_tr_b16 a[168:169], v220 offset:12288\n"
"v_mfma_f32_16x16x32_bf16 v[208:211], a[132:135], a[204:207], v[208:211]\n"
"ds_read_b64_tr_b16 a[170:171], v220 offset:17408\n"
"v_mfma_f32_16x16x32_bf16 v[208:211], a[172:175], a[216:219], v[208:211]\n"
"v_mfma_f32_16x16x32_bf16 v[212:215], a[132:135], a[208:211], v[212:215]\n"
"v_mfma_f32_16x16x32_bf16 v[212:215], a[172:175], a[220:223], v[212:215]\n"
"ds_read_b64_tr_b16 a[132:133], v221 offset:2048\n"
"ds_read_b64_tr_b16 a[134:135], v221 offset:7168\n"
"ds_read_b64_tr_b16 a[172:173], v221 offset:12288\n"
"ds_read_b64_tr_b16 a[174:175], v221 offset:17408\n"
"s_waitcnt vmcnt(29) lgkmcnt(15)\n"
"s_barrier\n"
"v_mfma_f32_16x16x32_bf16 v[96:99], a[96:99], a[224:227], v[96:99]\n"
"buffer_load_dwordx2 v[234:235], v238, s[36:39], s94 offen sc1\n"
"v_mfma_f32_16x16x32_bf16 v[96:99], a[136:139], a[236:239], v[96:99]\n"
"buffer_load_dwordx4 a[76:79], v246, s[44:47], s72 offen sc1\n"
"v_mfma_f32_16x16x32_bf16 v[100:103], a[96:99], a[228:231], v[100:103]\n"
"v_mfma_f32_16x16x32_bf16 v[100:103], a[136:139], a[240:243], v[100:103]\n"
"buffer_load_dwordx4 a[80:83], v246, s[44:47], s72 offen offset:1024 sc1\n"
"v_mfma_f32_16x16x32_bf16 v[104:107], a[96:99], a[232:235], v[104:107]\n"
"v_mfma_f32_16x16x32_bf16 v[104:107], a[136:139], a[244:247], v[104:107]\n"
"ds_read_b64_tr_b16 a[96:97], v220 offset:20480\n"
"buffer_load_dwordx4 a[84:87], v246, s[44:47], s72 offen offset:2048 sc1\n"
"v_mfma_f32_16x16x32_bf16 v[108:111], a[100:103], a[224:227], v[108:111]\n"
"ds_read_b64_tr_b16 a[98:99], v220 offset:25600\n"
"v_mfma_f32_16x16x32_bf16 v[108:111], a[140:143], a[236:239], v[108:111]\n"
"ds_read_b64_tr_b16 a[136:137], v220 offset:30720\n"
"s_add_u32 s72, s72, 12288\n"
"v_mfma_f32_16x16x32_bf16 v[112:115], a[100:103], a[228:231], v[112:115]\n"
"ds_read_b64_tr_b16 a[138:139], v220 offset:35840\n"
"v_mfma_f32_16x16x32_bf16 v[112:115], a[140:143], a[240:243], v[112:115]\n"
"s_cmp_eq_u32 s72, 393216\n"
"s_cselect_b32 s72, 0, s72\n"
"v_mfma_f32_16x16x32_bf16 v[116:119], a[100:103], a[232:235], v[116:119]\n"
"v_mfma_f32_16x16x32_bf16 v[116:119], a[140:143], a[244:247], v[116:119]\n"
"ds_read_b64_tr_b16 a[100:101], v221 offset:20480\n"
"buffer_load_dwordx4 a[88:91], v246, s[44:47], s72 offen sc1\n"
"v_mfma_f32_16x16x32_bf16 v[120:123], a[104:107], a[224:227], v[120:123]\n"
"ds_read_b64_tr_b16 a[102:103], v221 offset:25600\n"
"v_mfma_f32_16x16x32_bf16 v[120:123], a[144:147], a[236:239], v[120:123]\n"
"ds_read_b64_tr_b16 a[140:141], v221 offset:30720\n"
"buffer_load_dwordx4 a[92:95], v246, s[44:47], s72 offen offset:1024 sc1\n"
"v_mfma_f32_16x16x32_bf16 v[124:127], a[104:107], a[228:231], v[124:127]\n"
"ds_read_b64_tr_b16 a[142:143], v221 offset:35840\n"
"v_mfma_f32_16x16x32_bf16 v[124:127], a[144:147], a[240:243], v[124:127]\n"
"buffer_load_dwordx4 v[16:19], v246, s[44:47], s72 offen offset:2048 sc1\n"
"v_mfma_f32_16x16x32_bf16 v[128:131], a[104:107], a[232:235], v[128:131]\n"
"v_mfma_f32_16x16x32_bf16 v[128:131], a[144:147], a[244:247], v[128:131]\n"
"ds_read_b64_tr_b16 a[104:105], v220 offset:20992\n"
"s_add_u32 s72, s72, 12288\n"
"v_mfma_f32_16x16x32_bf16 v[132:135], a[108:111], a[224:227], v[132:135]\n"
"ds_read_b64_tr_b16 a[106:107], v220 offset:26112\n"
"v_mfma_f32_16x16x32_bf16 v[132:135], a[148:151], a[236:239], v[132:135]\n"
"ds_read_b64_tr_b16 a[144:145], v220 offset:31232\n"
"v_mfma_f32_16x16x32_bf16 v[136:139], a[108:111], a[228:231], v[136:139]\n"
"ds_read_b64_tr_b16 a[146:147], v220 offset:36352\n"
"v_mfma_f32_16x16x32_bf16 v[136:139], a[148:151], a[240:243], v[136:139]\n"
"s_cmp_eq_u32 s72, 393216\n"
"s_cselect_b32 s72, 0, s72\n"
"v_mfma_f32_16x16x32_bf16 v[140:143], a[108:111], a[232:235], v[140:143]\n"
"buffer_load_dwordx4 v[20:23], v246, s[44:47], s72 offen sc1\n"
"v_mfma_f32_16x16x32_bf16 v[140:143], a[148:151], a[244:247], v[140:143]\n"
"ds_read_b64_tr_b16 a[108:109], v221 offset:20992\n"
"buffer_load_dwordx4 v[24:27], v246, s[44:47], s72 offen offset:1024 sc1\n"
"v_mfma_f32_16x16x32_bf16 v[144:147], a[112:115], a[224:227], v[144:147]\n"
"ds_read_b64_tr_b16 a[110:111], v221 offset:26112\n"
"v_mfma_f32_16x16x32_bf16 v[144:147], a[152:155], a[236:239], v[144:147]\n"
"ds_read_b64_tr_b16 a[148:149], v221 offset:31232\n"
"buffer_load_dwordx4 v[28:31], v246, s[44:47], s72 offen offset:2048 sc1\n"
"v_mfma_f32_16x16x32_bf16 v[148:151], a[112:115], a[228:231], v[148:151]\n"
"ds_read_b64_tr_b16 a[150:151], v221 offset:36352\n"
"v_mfma_f32_16x16x32_bf16 v[148:151], a[152:155], a[240:243], v[148:151]\n"
"s_add_u32 s72, s72, 12288\n"
"v_mfma_f32_16x16x32_bf16 v[152:155], a[112:115], a[232:235], v[152:155]\n"
"v_mfma_f32_16x16x32_bf16 v[152:155], a[152:155], a[244:247], v[152:155]\n"
"ds_read_b64_tr_b16 a[112:113], v220 offset:21504\n"
"v_mfma_f32_16x16x32_bf16 v[156:159], a[116:119], a[224:227], v[156:159]\n"
"ds_read_b64_tr_b16 a[114:115], v220 offset:26624\n"
"v_mfma_f32_16x16x32_bf16 v[156:159], a[156:159], a[236:239], v[156:159]\n"
"ds_read_b64_tr_b16 a[152:153], v220 offset:31744\n"
"v_mfma_f32_16x16x32_bf16 v[160:163], a[116:119], a[228:231], v[160:163]\n"
"ds_read_b64_tr_b16 a[154:155], v220 offset:36864\n"
"v_mfma_f32_16x16x32_bf16 v[160:163], a[156:159], a[240:243], v[160:163]\n"
"s_cmp_eq_u32 s72, 393216\n"
"s_cselect_b32 s72, 0, s72\n"
"v_mfma_f32_16x16x32_bf16 v[164:167], a[116:119], a[232:235], v[164:167]\n"
"s_add_u32 m0, s78, 102400\n"
"s_nop 0\n"
"buffer_load_dwordx4 v244, s[40:43], s73 offen lds\n"
"v_mfma_f32_16x16x32_bf16 v[164:167], a[156:159], a[244:247], v[164:167]\n"
"s_waitcnt lgkmcnt(15)\n"
"ds_read_b64_tr_b16 a[116:117], v221 offset:21504\n"
"v_mfma_f32_16x16x32_bf16 v[168:171], a[120:123], a[224:227], v[168:171]\n"
"ds_read_b64_tr_b16 a[118:119], v221 offset:26624\n"
"s_add_u32 m0, s78, 103296\n"
"v_mfma_f32_16x16x32_bf16 v[168:171], a[160:163], a[236:239], v[168:171]\n"
"ds_read_b64_tr_b16 a[156:157], v221 offset:31744\n"
"v_mfma_f32_16x16x32_bf16 v[172:175], a[120:123], a[228:231], v[172:175]\n"
"ds_read_b64_tr_b16 a[158:159], v221 offset:36864\n"
"v_mfma_f32_16x16x32_bf16 v[172:175], a[160:163], a[240:243], v[172:175]\n"
"buffer_load_dwordx4 v244, s[40:43], s73 offen offset:128 lds\n"
"v_mfma_f32_16x16x32_bf16 v[176:179], a[120:123], a[232:235], v[176:179]\n"
"s_add_u32 m0, s78, 104192\n"
"v_mfma_f32_16x16x32_bf16 v[176:179], a[160:163], a[244:247], v[176:179]\n"
"ds_read_b64_tr_b16 a[120:121], v220 offset:22016\n"
"v_mfma_f32_16x16x32_bf16 v[180:183], a[124:127], a[224:227], v[180:183]\n"
"ds_read_b64_tr_b16 a[122:123], v220 offset:27136\n"
"v_mfma_f32_16x16x32_bf16 v[180:183], a[164:167], a[236:239], v[180:183]\n"
"ds_read_b64_tr_b16 a[160:161], v220 offset:32256\n"
"v_mfma_f32_16x16x32_bf16 v[184:187], a[124:127], a[228:231], v[184:187]\n"
"ds_read_b64_tr_b16 a[162:163], v220 offset:37376\n"
"v_mfma_f32_16x16x32_bf16 v[184:187], a[164:167], a[240:243], v[184:187]\n"
"buffer_load_dwordx4 v245, s[40:43], s73 offen offset:256 lds\n"
"v_mfma_f32_16x16x32_bf16 v[188:191], a[124:127], a[232:235], v[188:191]\n"
"s_add_u32 m0, s78, 105408\n"
"s_add_u32 s64, s73, 10240\n"
"v_mfma_f32_16x16x32_bf16 v[188:191], a[164:167], a[244:247], v[188:191]\n"
"ds_read_b64_tr_b16 a[124:125], v221 offset:22016\n"
"buffer_load_dwordx4 v244, s[40:43], s64 offen offset:64 lds\n"
"v_mfma_f32_16x16x32_bf16 v[192:195], a[128:131], a[224:227], v[192:195]\n"
"ds_read_b64_tr_b16 a[126:127], v221 offset:27136\n"
"s_add_u32 m0, s78, 106304\n"
"v_mfma_f32_16x16x32_bf16 v[192:195], a[168:171], a[236:239], v[192:195]\n"
"ds_read_b64_tr_b16 a[164:165], v221 offset:32256\n"
"v_mfma_f32_16x16x32_bf16 v[196:199], a[128:131], a[228:231], v[196:199]\n"
"ds_read_b64_tr_b16 a[166:167], v221 offset:37376\n"
"v_mfma_f32_16x16x32_bf16 v[196:199], a[168:171], a[240:243], v[196:199]\n"
"buffer_load_dwordx4 v244, s[40:43], s64 offen offset:192 lds\n"
"v_mfma_f32_16x16x32_bf16 v[200:203], a[128:131], a[232:235], v[200:203]\n"
"s_add_u32 s73, s73, 81920\n"
"v_mfma_f32_16x16x32_bf16 v[200:203], a[168:171], a[244:247], v[200:203]\n"
"ds_read_b64_tr_b16 a[128:129], v220 offset:22528\n"
"s_cmp_eq_u32 s73, 1310720\n"
"s_cselect_b32 s73, 0, s73\n"
"v_mfma_f32_16x16x32_bf16 v[204:207], a[132:135], a[224:227], v[204:207]\n"
"ds_read_b64_tr_b16 a[130:131], v220 offset:27648\n"
"v_mfma_f32_16x16x32_bf16 v[204:207], a[172:175], a[236:239], v[204:207]\n"
"ds_read_b64_tr_b16 a[168:169], v220 offset:32768\n"
"v_mfma_f32_16x16x32_bf16 v[208:211], a[132:135], a[228:231], v[208:211]\n"
"ds_read_b64_tr_b16 a[170:171], v220 offset:37888\n"
"v_mfma_f32_16x16x32_bf16 v[208:211], a[172:175], a[240:243], v[208:211]\n"
"v_mfma_f32_16x16x32_bf16 v[212:215], a[132:135], a[232:235], v[212:215]\n"
"v_mfma_f32_16x16x32_bf16 v[212:215], a[172:175], a[244:247], v[212:215]\n"
"ds_read_b64_tr_b16 a[132:133], v221 offset:22528\n"
"ds_read_b64_tr_b16 a[134:135], v221 offset:27648\n"
"ds_read_b64_tr_b16 a[172:173], v221 offset:32768\n"
"ds_read_b64_tr_b16 a[174:175], v221 offset:37888\n"
"s_waitcnt vmcnt(14)\n"
"v_cmp_eq_u32_e64 s[98:99], s62, v234\n"
"v_cmp_eq_u32_e32 vcc, s65, v235\n"
"s_nop 0\n"
"s_and_b64 s[98:99], s[98:99], vcc\n"
"s_cmp_eq_u64 s[98:99], -1\n"
"s_cbranch_scc1 L_pf2d%=\n"
"s_mov_b32 s63, 0x40000\n"
"L_pf2%=:\n"
"buffer_load_dwordx2 v[234:235], v238, s[36:39], s94 offen sc1\n"
"s_waitcnt vmcnt(0)\n"
"v_cmp_eq_u32_e64 s[98:99], s62, v234\n"
"v_cmp_eq_u32_e32 vcc, s65, v235\n"
"s_nop 0\n"
"s_and_b64 s[98:99], s[98:99], vcc\n"
"s_cmp_eq_u64 s[98:99], -1\n"
"s_cbranch_scc1 L_pf2d%=\n"
"s_sleep 1\n"
"s_sub_u32 s63, s63, 1\n"
"s_cmp_lg_u32 s63, 0\n"
"s_cbranch_scc1 L_pf2%=\n"
"L_pf2d%=:\n"
"s_waitcnt vmcnt(28) lgkmcnt(15)\n"
"s_barrier\n"
"v_mfma_f32_16x16x32_bf16 v[96:99], a[96:99], a[248:251], v[96:99]\n"
"buffer_load_dwordx4 v[48:51], v246, s[44:47], s72 offen sc1\n"
"v_mfma_f32_16x16x32_bf16 v[96:99], a[136:139], a[52:55], v[96:99]\n"
"v_mfma_f32_16x16x32_bf16 v[100:103], a[96:99], a[252:255], v[100:103]\n"
"buffer_load_dwordx4 v[52:55], v246, s[44:47], s72 offen offset:1024 sc1\n"
"v_mfma_f32_16x16x32_bf16 v[100:103], a[136:139], a[56:59], v[100:103]\n"
"v_mfma_f32_16x16x32_bf16 v[104:107], a[96:99], a[48:51], v[104:107]\n"
"buffer_load_dwordx4 v[56:59], v246, s[44:47], s72 offen offset:2048 sc1\n"
"v_mfma_f32_16x16x32_bf16 v[104:107], a[136:139], a[60:63], v[104:107]\n"
"ds_read_b64_tr_b16 a[96:97], v216 offset:0\n"
"v_mfma_f32_16x16x32_bf16 v[108:111], a[100:103], a[248:251], v[108:111]\n"
"ds_read_b64_tr_b16 a[98:99], v216 offset:5120\n"
"s_add_u32 s72, s72, 12288\n"
"v_mfma_f32_16x16x32_bf16 v[108:111], a[140:143], a[52:55], v[108:111]\n"
"ds_read_b64_tr_b16 a[136:137], v216 offset:10240\n"
"v_mfma_f32_16x16x32_bf16 v[112:115], a[100:103], a[252:255], v[112:115]\n"
"ds_read_b64_tr_b16 a[138:139], v216 offset:15360\n"
"v_mfma_f32_16x16x32_bf16 v[112:115], a[140:143], a[56:59], v[112:115]\n"
"s_cmp_eq_u32 s72, 393216\n"
"s_cselect_b32 s72, 0, s72\n"
"v_mfma_f32_16x16x32_bf16 v[116:119], a[100:103], a[48:51], v[116:119]\n"
"buffer_load_dwordx4 v[60:63], v246, s[44:47], s72 offen sc1\n"
"v_mfma_f32_16x16x32_bf16 v[116:119], a[140:143], a[60:63], v[116:119]\n"
"ds_read_b64_tr_b16 a[100:101], v217 offset:0\n"
"v_mfma_f32_16x16x32_bf16 v[120:123], a[104:107], a[248:251], v[120:123]\n"
"ds_read_b64_tr_b16 a[102:103], v217 offset:5120\n"
"buffer_load_dwordx4 v[80:83], v246, s[44:47], s72 offen offset:1024 sc1\n"
"v_mfma_f32_16x16x32_bf16 v[120:123], a[144:147], a[52:55], v[120:123]\n"
"ds_read_b64_tr_b16 a[140:141], v217 offset:10240\n"
"v_mfma_f32_16x16x32_bf16 v[124:127], a[104:107], a[252:255], v[124:127]\n"
"ds_read_b64_tr_b16 a[142:143], v217 offset:15360\n"
"buffer_load_dwordx4 v[84:87], v246, s[44:47], s72 offen offset:2048 sc1\n"
"v_mfma_f32_16x16x32_bf16 v[124:127], a[144:147], a[56:59], v[124:127]\n"
"v_mfma_f32_16x16x32_bf16 v[128:131], a[104:107], a[48:51], v[128:131]\n"
"s_add_u32 s72, s72, 12288\n"
"v_mfma_f32_16x16x32_bf16 v[128:131], a[144:147], a[60:63], v[128:131]\n"
"ds_read_b64_tr_b16 a[104:105], v216 offset:512\n"
"v_mfma_f32_16x16x32_bf16 v[132:135], a[108:111], a[248:251], v[132:135]\n"
"ds_read_b64_tr_b16 a[106:107], v216 offset:5632\n"
"v_mfma_f32_16x16x32_bf16 v[132:135], a[148:151], a[52:55], v[132:135]\n"
"ds_read_b64_tr_b16 a[144:145], v216 offset:10752\n"
"v_mfma_f32_16x16x32_bf16 v[136:139], a[108:111], a[252:255], v[136:139]\n"
"ds_read_b64_tr_b16 a[146:147], v216 offset:15872\n"
"v_mfma_f32_16x16x32_bf16 v[136:139], a[148:151], a[56:59], v[136:139]\n"
"s_cmp_eq_u32 s72, 393216\n"
"s_cselect_b32 s72, 0, s72\n"
"v_mfma_f32_16x16x32_bf16 v[140:143], a[108:111], a[48:51], v[140:143]\n"
"buffer_load_dwordx4 v[88:91], v246, s[44:47], s72 offen sc1\n"
"buffer_load_dwordx4 v[92:95], v246, s[44:47], s72 offen offset:1024 sc1\n"
"v_mfma_f32_16x16x32_bf16 v[140:143], a[148:151], a[60:63], v[140:143]\n"
"ds_read_b64_tr_b16 a[108:109], v217 offset:512\n"
"v_mfma_f32_16x16x32_bf16 v[144:147], a[112:115], a[248:251], v[144:147]\n"
"ds_read_b64_tr_b16 a[110:111], v217 offset:5632\n"
"buffer_load_dwordx4 v[0:3], v246, s[44:47], s72 offen offset:2048 sc1\n"
"v_mfma_f32_16x16x32_bf16 v[144:147], a[152:155], a[52:55], v[144:147]\n"
"ds_read_b64_tr_b16 a[148:149], v217 offset:10752\n"
"v_mfma_f32_16x16x32_bf16 v[148:151], a[112:115], a[252:255], v[148:151]\n"
"ds_read_b64_tr_b16 a[150:151], v217 offset:15872\n"
"s_add_u32 s72, s72, 12288\n"
"v_mfma_f32_16x16x32_bf16 v[148:151], a[152:155], a[56:59], v[148:151]\n"
"v_mfma_f32_16x16x32_bf16 v[152:155], a[112:115], a[48:51], v[152:155]\n"
"s_cmp_eq_u32 s72, 393216\n"
"s_cselect_b32 s72, 0, s72\n"
"v_mfma_f32_16x16x32_bf16 v[152:155], a[152:155], a[60:63], v[152:155]\n"
"ds_read_b64_tr_b16 a[112:113], v216 offset:1024\n"
"v_mfma_f32_16x16x32_bf16 v[156:159], a[116:119], a[248:251], v[156:159]\n"
"ds_read_b64_tr_b16 a[114:115], v216 offset:6144\n"
"s_add_u32 m0, s78, 122880\n"
"v_mfma_f32_16x16x32_bf16 v[156:159], a[156:159], a[52:55], v[156:159]\n"
"ds_read_b64_tr_b16 a[152:153], v216 offset:11264\n"
"v_mfma_f32_16x16x32_bf16 v[160:163], a[116:119], a[252:255], v[160:163]\n"
"ds_read_b64_tr_b16 a[154:155], v216 offset:16384\n"
"v_mfma_f32_16x16x32_bf16 v[160:163], a[156:159], a[56:59], v[160:163]\n"
"buffer_load_dwordx4 v244, s[40:43], s73 offen lds\n"
"v_mfma_f32_16x16x32_bf16 v[164:167], a[116:119], a[48:51], v[164:167]\n"
"s_add_u32 m0, s78, 123776\n"
"v_mfma_f32_16x16x32_bf16 v[164:167], a[156:159], a[60:63], v[164:167]\n"
"s_waitcnt lgkmcnt(15)\n"
"ds_read_b64_tr_b16 a[116:117], v217 offset:1024\n"
"v_mfma_f32_16x16x32_bf16 v[168:171], a[120:123], a[248:251], v[168:171]\n"
"ds_read_b64_tr_b16 a[118:119], v217 offset:6144\n"
"v_mfma_f32_16x16x32_bf16 v[168:171], a[160:163], a[52:55], v[168:171]\n"
"ds_read_b64_tr_b16 a[156:157], v217 offset:11264\n"
"v_mfma_f32_16x16x32_bf16 v[172:175], a[120:123], a[252:255], v[172:175]\n"
"ds_read_b64_tr_b16 a[158:159], v217 offset:16384\n"
"v_mfma_f32_16x16x32_bf16 v[172:175], a[160:163], a[56:59], v[172:175]\n"
"buffer_load_dwordx4 v244, s[40:43], s73 offen offset:128 lds\n"
"v_mfma_f32_16x16x32_bf16 v[176:179], a[120:123], a[48:51], v[176:179]\n"
"s_add_u32 m0, s78, 124672\n"
"s_nop 0\n"
"buffer_load_dwordx4 v245, s[40:43], s73 offen offset:256 lds\n"
"v_mfma_f32_16x16x32_bf16 v[176:179], a[160:163], a[60:63], v[176:179]\n"
"ds_read_b64_tr_b16 a[120:121], v216 offset:1536\n"
"v_mfma_f32_16x16x32_bf16 v[180:183], a[124:127], a[248:251], v[180:183]\n"
"ds_read_b64_tr_b16 a[122:123], v216 offset:6656\n"
"v_mfma_f32_16x16x32_bf16 v[180:183], a[164:167], a[52:55], v[180:183]\n"
"ds_read_b64_tr_b16 a[160:161], v216 offset:11776\n"
"v_mfma_f32_16x16x32_bf16 v[184:187], a[124:127], a[252:255], v[184:187]\n"
"ds_read_b64_tr_b16 a[162:163], v216 offset:16896\n"
"v_mfma_f32_16x16x32_bf16 v[184:187], a[164:167], a[56:59], v[184:187]\n"
"s_add_u32 m0, s78, 125888\n"
"s_add_u32 s64, s73, 10240\n"
"v_mfma_f32_16x16x32_bf16 v[188:191], a[124:127], a[48:51], v[188:191]\n"
"buffer_load_dwordx4 v244, s[40:43], s64 offen offset:64 lds\n"
"v_mfma_f32_16x16x32_bf16 v[188:191], a[164:167], a[60:63], v[188:191]\n"
"ds_read_b64_tr_b16 a[124:125], v217 offset:1536\n"
"s_add_u32 m0, s78, 126784\n"
"v_mfma_f32_16x16x32_bf16 v[192:195], a[128:131], a[248:251], v[192:195]\n"
"ds_read_b64_tr_b16 a[126:127], v217 offset:6656\n"
"v_mfma_f32_16x16x32_bf16 v[192:195], a[168:171], a[52:55], v[192:195]\n"
"ds_read_b64_tr_b16 a[164:165], v217 offset:11776\n"
"v_mfma_f32_16x16x32_bf16 v[196:199], a[128:131], a[252:255], v[196:199]\n"
"ds_read_b64_tr_b16 a[166:167], v217 offset:16896\n"
"v_mfma_f32_16x16x32_bf16 v[196:199], a[168:171], a[56:59], v[196:199]\n"
"buffer_load_dwordx4 v244, s[40:43], s64 offen offset:192 lds\n"
"v_mfma_f32_16x16x32_bf16 v[200:203], a[128:131], a[48:51], v[200:203]\n"
"s_add_u32 s73, s73, 81920\n"
"s_cmp_eq_u32 s73, 1310720\n"
"s_cselect_b32 s73, 0, s73\n"
"v_mfma_f32_16x16x32_bf16 v[200:203], a[168:171], a[60:63], v[200:203]\n"
"ds_read_b64_tr_b16 a[128:129], v216 offset:2048\n"
"v_mfma_f32_16x16x32_bf16 v[204:207], a[132:135], a[248:251], v[204:207]\n"
"ds_read_b64_tr_b16 a[130:131], v216 offset:7168\n"
"v_mfma_f32_16x16x32_bf16 v[204:207], a[172:175], a[52:55], v[204:207]\n"
"ds_read_b64_tr_b16 a[168:169], v216 offset:12288\n"
"v_mfma_f32_16x16x32_bf16 v[208:211], a[132:135], a[252:255], v[208:211]\n"
"ds_read_b64_tr_b16 a[170:171], v216 offset:17408\n"
"v_mfma_f32_16x16x32_bf16 v[208:211], a[172:175], a[56:59], v[208:211]\n"
"v_mfma_f32_16x16x32_bf16 v[212:215], a[132:135], a[48:51], v[212:215]\n"
"v_mfma_f32_16x16x32_bf16 v[212:215], a[172:175], a[60:63], v[212:215]\n"
"ds_read_b64_tr_b16 a[132:133], v217 offset:2048\n"
"ds_read_b64_tr_b16 a[134:135], v217 offset:7168\n"
"ds_read_b64_tr_b16 a[172:173], v217 offset:12288\n"
"ds_read_b64_tr_b16 a[174:175], v217 offset:17408\n"
"s_waitcnt vmcnt(25) lgkmcnt(15)\n"
"s_barrier\n"
"v_mfma_f32_16x16x32_bf16 v[96:99], a[96:99], a[64:67], v[96:99]\n"
"buffer_load_dwordx4 v[4:7], v246, s[44:47], s72 offen sc1\n"
"v_mfma_f32_16x16x32_bf16 v[96:99], a[136:139], a[76:79], v[96:99]\n"
"v_mfma_f32_16x16x32_bf16 v[100:103], a[96:99], a[68:71], v[100:103]\n"
"buffer_load_dwordx4 v[8:11], v246, s[44:47], s72 offen offset:1024 sc1\n"
"v_mfma_f32_16x16x32_bf16 v[100:103], a[136:139], a[80:83], v[100:103]\n"
"v_mfma_f32_16x16x32_bf16 v[104:107], a[96:99], a[72:75], v[104:107]\n"
"buffer_load_dwordx4 v[12:15], v246, s[44:47], s72 offen offset:2048 sc1\n"
"v_mfma_f32_16x16x32_bf16 v[104:107], a[136:139], a[84:87], v[104:107]\n"
"ds_read_b64_tr_b16 a[96:97], v216 offset:20480\n"
"v_mfma_f32_16x16x32_bf16 v[108:111], a[100:103], a[64:67], v[108:111]\n"
"ds_read_b64_tr_b16 a[98:99], v216 offset:25600\n"
"s_add_u32 s72, s72, 12288\n"
"v_mfma_f32_16x16x32_bf16 v[108:111], a[140:143], a[76:79], v[108:111]\n"
"ds_read_b64_tr_b16 a[136:137], v216 offset:30720\n"
"v_mfma_f32_16x16x32_bf16 v[112:115], a[100:103], a[68:71], v[112:115]\n"
"ds_read_b64_tr_b16 a[138:139], v216 offset:35840\n"
"v_mfma_f32_16x16x32_bf16 v[112:115], a[140:143], a[80:83], v[112:115]\n"
"s_cmp_eq_u32 s72, 393216\n"
"s_cselect_b32 s72, 0, s72\n"
"v_mfma_f32_16x16x32_bf16 v[116:119], a[100:103], a[72:75], v[116:119]\n"
"buffer_load_dwordx4 v[32:35], v246, s[44:47], s72 offen sc1\n"
"v_mfma_f32_16x16x32_bf16 v[116:119], a[140:143], a[84:87], v[116:119]\n"
"ds_read_b64_tr_b16 a[100:101], v217 offset:20480\n"
"v_mfma_f32_16x16x32_bf16 v[120:123], a[104:107], a[64:67], v[120:123]\n"
"ds_read_b64_tr_b16 a[102:103], v217 offset:25600\n"
"buffer_load_dwordx4 v[36:39], v246, s[44:47], s72 offen offset:1024 sc1\n"
"v_mfma_f32_16x16x32_bf16 v[120:123], a[144:147], a[76:79], v[120:123]\n"
"ds_read_b64_tr_b16 a[140:141], v217 offset:30720\n"
"v_mfma_f32_16x16x32_bf16 v[124:127], a[104:107], a[68:71], v[124:127]\n"
"ds_read_b64_tr_b16 a[142:143], v217 offset:35840\n"
"buffer_load_dwordx4 v[40:43], v246, s[44:47], s72 offen offset:2048 sc1\n"
"v_mfma_f32_16x16x32_bf16 v[124:127], a[144:147], a[80:83], v[124:127]\n"
"v_mfma_f32_16x16x32_bf16 v[128:131], a[104:107], a[72:75], v[128:131]\n"
"s_add_u32 s72, s72, 12288\n"
"v_mfma_f32_16x16x32_bf16 v[128:131], a[144:147], a[84:87], v[128:131]\n"
"ds_read_b64_tr_b16 a[104:105], v216 offset:20992\n"
"v_mfma_f32_16x16x32_bf16 v[132:135], a[108:111], a[64:67], v[132:135]\n"
"ds_read_b64_tr_b16 a[106:107], v216 offset:26112\n"
"v_mfma_f32_16x16x32_bf16 v[132:135], a[148:151], a[76:79], v[132:135]\n"
"ds_read_b64_tr_b16 a[144:145], v216 offset:31232\n"
"v_mfma_f32_16x16x32_bf16 v[136:139], a[108:111], a[68:71], v[136:139]\n"
"ds_read_b64_tr_b16 a[146:147], v216 offset:36352\n"
"v_mfma_f32_16x16x32_bf16 v[136:139], a[148:151], a[80:83], v[136:139]\n"
"s_cmp_eq_u32 s72, 393216\n"
"s_cselect_b32 s72, 0, s72\n"
"v_mfma_f32_16x16x32_bf16 v[140:143], a[108:111], a[72:75], v[140:143]\n"
"buffer_load_dwordx4 v[44:47], v246, s[44:47], s72 offen sc1\n"
"buffer_load_dwordx4 v[64:67], v246, s[44:47], s72 offen offset:1024 sc1\n"
"v_mfma_f32_16x16x32_bf16 v[140:143], a[148:151], a[84:87], v[140:143]\n"
"ds_read_b64_tr_b16 a[108:109], v217 offset:20992\n"
"v_mfma_f32_16x16x32_bf16 v[144:147], a[112:115], a[64:67], v[144:147]\n"
"ds_read_b64_tr_b16 a[110:111], v217 offset:26112\n"
"buffer_load_dwordx4 v[68:71], v246, s[44:47], s72 offen offset:2048 sc1\n"
"v_mfma_f32_16x16x32_bf16 v[144:147], a[152:155], a[76:79], v[144:147]\n"
"ds_read_b64_tr_b16 a[148:149], v217 offset:31232\n"
"v_mfma_f32_16x16x32_bf16 v[148:151], a[112:115], a[68:71], v[148:151]\n"
"ds_read_b64_tr_b16 a[150:151], v217 offset:36352\n"
"s_add_u32 s72, s72, 12288\n"
"v_mfma_f32_16x16x32_bf16 v[148:151], a[152:155], a[80:83], v[148:151]\n"
"v_mfma_f32_16x16x32_bf16 v[152:155], a[112:115], a[72:75], v[152:155]\n"
"s_cmp_eq_u32 s72, 393216\n"
"s_cselect_b32 s72, 0, s72\n"
"v_mfma_f32_16x16x32_bf16 v[152:155], a[152:155], a[84:87], v[152:155]\n"
"ds_read_b64_tr_b16 a[112:113], v216 offset:21504\n"
"v_mfma_f32_16x16x32_bf16 v[156:159], a[116:119], a[64:67], v[156:159]\n"
"ds_read_b64_tr_b16 a[114:115], v216 offset:26624\n"
"s_add_u32 m0, s78, 143360\n"
"v_mfma_f32_16x16x32_bf16 v[156:159], a[156:159], a[76:79], v[156:159]\n"
"ds_read_b64_tr_b16 a[152:153], v216 offset:31744\n"
"v_mfma_f32_16x16x32_bf16 v[160:163], a[116:119], a[68:71], v[160:163]\n"
"ds_read_b64_tr_b16 a[154:155], v216 offset:36864\n"
"v_mfma_f32_16x16x32_bf16 v[160:163], a[156:159], a[80:83], v[160:163]\n"
"buffer_load_dwordx4 v244, s[40:43], s73 offen lds\n"
"v_mfma_f32_16x16x32_bf16 v[164:167], a[116:119], a[72:75], v[164:167]\n"
"s_add_u32 m0, s78, 144256\n"
"v_mfma_f32_16x16x32_bf16 v[164:167], a[156:159], a[84:87], v[164:167]\n"
"s_waitcnt lgkmcnt(15)\n"
"ds_read_b64_tr_b16 a[116:117], v217 offset:21504\n"
"v_mfma_f32_16x16x32_bf16 v[168:171], a[120:123], a[64:67], v[168:171]\n"
"ds_read_b64_tr_b16 a[118:119], v217 offset:26624\n"
"v_mfma_f32_16x16x32_bf16 v[168:171], a[160:163], a[76:79], v[168:171]\n"
"ds_read_b64_tr_b16 a[156:157], v217 offset:31744\n"
"v_mfma_f32_16x16x32_bf16 v[172:175], a[120:123], a[68:71], v[172:175]\n"
"ds_read_b64_tr_b16 a[158:159], v217 offset:36864\n"
"v_mfma_f32_16x16x32_bf16 v[172:175], a[160:163], a[80:83], v[172:175]\n"
"buffer_load_dwordx4 v244, s[40:43], s73 offen offset:128 lds\n"
"v_mfma_f32_16x16x32_bf16 v[176:179], a[120:123], a[72:75], v[176:179]\n"
"s_add_u32 m0, s78, 145152\n"
"s_nop 0\n"
"buffer_load_dwordx4 v245, s[40:43], s73 offen offset:256 lds\n"
"v_mfma_f32_16x16x32_bf16 v[176:179], a[160:163], a[84:87], v[176:179]\n"
"ds_read_b64_tr_b16 a[120:121], v216 offset:22016\n"
"v_mfma_f32_16x16x32_bf16 v[180:183], a[124:127], a[64:67], v[180:183]\n"
"ds_read_b64_tr_b16 a[122:123], v216 offset:27136\n"
"v_mfma_f32_16x16x32_bf16 v[180:183], a[164:167], a[76:79], v[180:183]\n"
"ds_read_b64_tr_b16 a[160:161], v216 offset:32256\n"
"v_mfma_f32_16x16x32_bf16 v[184:187], a[124:127], a[68:71], v[184:187]\n"
"ds_read_b64_tr_b16 a[162:163], v216 offset:37376\n"
"v_mfma_f32_16x16x32_bf16 v[184:187], a[164:167], a[80:83], v[184:187]\n"
"s_add_u32 m0, s78, 146368\n"
"s_add_u32 s64, s73, 10240\n"
"v_mfma_f32_16x16x32_bf16 v[188:191], a[124:127], a[72:75], v[188:191]\n"
"buffer_load_dwordx4 v244, s[40:43], s64 offen offset:64 lds\n"
"v_mfma_f32_16x16x32_bf16 v[188:191], a[164:167], a[84:87], v[188:191]\n"
"ds_read_b64_tr_b16 a[124:125], v217 offset:22016\n"
"s_add_u32 m0, s78, 147264\n"
"v_mfma_f32_16x16x32_bf16 v[192:195], a[128:131], a[64:67], v[192:195]\n"
"ds_read_b64_tr_b16 a[126:127], v217 offset:27136\n"
"v_mfma_f32_16x16x32_bf16 v[192:195], a[168:171], a[76:79], v[192:195]\n"
"ds_read_b64_tr_b16 a[164:165], v217 offset:32256\n"
"v_mfma_f32_16x16x32_bf16 v[196:199], a[128:131], a[68:71], v[196:199]\n"
"ds_read_b64_tr_b16 a[166:167], v217 offset:37376\n"
"v_mfma_f32_16x16x32_bf16 v[196:199], a[168:171], a[80:83], v[196:199]\n"
"buffer_load_dwordx4 v244, s[40:43], s64 offen offset:192 lds\n"
"v_mfma_f32_16x16x32_bf16 v[200:203], a[128:131], a[72:75], v[200:203]\n"
"s_add_u32 s73, s73, 81920\n"
"s_cmp_eq_u32 s73, 1310720\n"
"s_cselect_b32 s73, 0, s73\n"
"v_mfma_f32_16x16x32_bf16 v[200:203], a[168:171], a[84:87], v[200:203]\n"
"ds_read_b64_tr_b16 a[128:129], v216 offset:22528\n"
"v_mfma_f32_16x16x32_bf16 v[204:207], a[132:135], a[64:67], v[204:207]\n"
"ds_read_b64_tr_b16 a[130:131], v216 offset:27648\n"
"v_mfma_f32_16x16x32_bf16 v[204:207], a[172:175], a[76:79], v[204:207]\n"
"ds_read_b64_tr_b16 a[168:169], v216 offset:32768\n"
"v_mfma_f32_16x16x32_bf16 v[208:211], a[132:135], a[68:71], v[208:211]\n"
"ds_read_b64_tr_b16 a[170:171], v216 offset:37888\n"
"v_mfma_f32_16x16x32_bf16 v[208:211], a[172:175], a[80:83], v[208:211]\n"
"v_mfma_f32_16x16x32_bf16 v[212:215], a[132:135], a[72:75], v[212:215]\n"
"v_mfma_f32_16x16x32_bf16 v[212:215], a[172:175], a[84:87], v[212:215]\n"
"ds_read_b64_tr_b16 a[132:133], v217 offset:22528\n"
"ds_read_b64_tr_b16 a[134:135], v217 offset:27648\n"
"ds_read_b64_tr_b16 a[172:173], v217 offset:32768\n"
"ds_read_b64_tr_b16 a[174:175], v217 offset:37888\n"
"s_waitcnt vmcnt(33) lgkmcnt(15)\n"
"v_mfma_f32_16x16x32_bf16 v[96:99], a[96:99], a[88:91], v[96:99]\n"
"buffer_load_dwordx4 v[72:75], v246, s[44:47], s72 offen sc1\n"
"v_mfma_f32_16x16x32_bf16 v[96:99], a[136:139], v[20:23], v[96:99]\n"
"v_mfma_f32_16x16x32_bf16 v[100:103], a[96:99], a[92:95], v[100:103]\n"
"v_mfma_f32_16x16x32_bf16 v[100:103], a[136:139], v[24:27], v[100:103]\n"
"buffer_load_dwordx4 v[76:79], v246, s[44:47], s72 offen offset:1024 sc1\n"
"v_mfma_f32_16x16x32_bf16 v[104:107], a[96:99], v[16:19], v[104:107]\n"
"v_mfma_f32_16x16x32_bf16 v[104:107], a[136:139], v[28:31], v[104:107]\n"
"ds_read_b64_tr_b16 a[96:97], v216 offset:40960\n"
"v_mfma_f32_16x16x32_bf16 v[108:111], a[100:103], a[88:91], v[108:111]\n"
"ds_read_b64_tr_b16 a[98:99], v216 offset:46080\n"
"v_mfma_f32_16x16x32_bf16 v[108:111], a[140:143], v[20:23], v[108:111]\n"
"ds_read_b64_tr_b16 a[136:137], v216 offset:51200\n"
"buffer_load_dwordx4 a[0:3], v246, s[44:47], s72 offen offset:2048 sc1\n"
"v_mfma_f32_16x16x32_bf16 v[112:115], a[100:103], a[92:95], v[112:115]\n"
"ds_read_b64_tr_b16 a[138:139], v216 offset:56320\n"
"v_mfma_f32_16x16x32_bf16 v[112:115], a[140:143], v[24:27], v[112:115]\n"
"v_mfma_f32_16x16x32_bf16 v[116:119], a[100:103], v[16:19], v[116:119]\n"
"v_mfma_f32_16x16x32_bf16 v[116:119], a[140:143], v[28:31], v[116:119]\n"
"ds_read_b64_tr_b16 a[100:101], v217 offset:40960\n"
"s_add_u32 s72, s72, 12288\n"
"v_mfma_f32_16x16x32_bf16 v[120:123], a[104:107], a[88:91], v[120:123]\n"
"ds_read_b64_tr_b16 a[102:103], v217 offset:46080\n"
"v_mfma_f32_16x16x32_bf16 v[120:123], a[144:147], v[20:23], v[120:123]\n"
"ds_read_b64_tr_b16 a[140:141], v217 offset:51200\n"
"v_mfma_f32_16x16x32_bf16 v[124:127], a[104:107], a[92:95], v[124:127]\n"
"ds_read_b64_tr_b16 a[142:143], v217 offset:56320\n"
"v_mfma_f32_16x16x32_bf16 v[124:127], a[144:147], v[24:27], v[124:127]\n"
"s_cmp_eq_u32 s72, 393216\n"
"s_cselect_b32 s72, 0, s72\n"
"v_mfma_f32_16x16x32_bf16 v[128:131], a[104:107], v[16:19], v[128:131]\n"
"v_mfma_f32_16x16x32_bf16 v[128:131], a[144:147], v[28:31], v[128:131]\n"
"ds_read_b64_tr_b16 a[104:105], v216 offset:41472\n"
"v_mfma_f32_16x16x32_bf16 v[132:135], a[108:111], a[88:91], v[132:135]\n"
"ds_read_b64_tr_b16 a[106:107], v216 offset:46592\n"
"buffer_load_dwordx4 a[4:7], v246, s[44:47], s72 offen sc1\n"
"v_mfma_f32_16x16x32_bf16 v[132:135], a[148:151], v[20:23], v[132:135]\n"
"ds_read_b64_tr_b16 a[144:145], v216 offset:51712\n"
"v_mfma_f32_16x16x32_bf16 v[136:139], a[108:111], a[92:95], v[136:139]\n"
"ds_read_b64_tr_b16 a[146:147], v216 offset:56832\n"
"v_mfma_f32_16x16x32_bf16 v[136:139], a[148:151], v[24:27], v[136:139]\n"
"v_mfma_f32_16x16x32_bf16 v[140:143], a[108:111], v[16:19], v[140:143]\n"
"buffer_load_dwordx4 a[8:11], v246, s[44:47], s72 offen offset:1024 sc1\n"
"v_mfma_f32_16x16x32_bf16 v[140:143], a[148:151], v[28:31], v[140:143]\n"
"ds_read_b64_tr_b16 a[108:109], v217 offset:41472\n"
"v_mfma_f32_16x16x32_bf16 v[144:147], a[112:115], a[88:91], v[144:147]\n"
"ds_read_b64_tr_b16 a[110:111], v217 offset:46592\n"
"v_mfma_f32_16x16x32_bf16 v[144:147], a[152:155], v[20:23], v[144:147]\n"
"ds_read_b64_tr_b16 a[148:149], v217 offset:51712\n"
"buffer_load_dwordx4 a[12:15], v246, s[44:47], s72 offen offset:2048 sc1\n"
"v_mfma_f32_16x16x32_bf16 v[148:151], a[112:115], a[92:95], v[148:151]\n"
"ds_read_b64_tr_b16 a[150:151], v217 offset:56832\n"
"v_mfma_f32_16x16x32_bf16 v[148:151], a[152:155], v[24:27], v[148:151]\n"
"v_mfma_f32_16x16x32_bf16 v[152:155], a[112:115], v[16:19], v[152:155]\n"
"v_mfma_f32_16x16x32_bf16 v[152:155], a[152:155], v[28:31], v[152:155]\n"
"ds_read_b64_tr_b16 a[112:113], v216 offset:41984\n"
"s_add_u32 s72, s72, 12288\n"
"v_mfma_f32_16x16x32_bf16 v[156:159], a[116:119], a[88:91], v[156:159]\n"
"ds_read_b64_tr_b16 a[114:115], v216 offset:47104\n"
"v_mfma_f32_16x16x32_bf16 v[156:159], a[156:159], v[20:23], v[156:159]\n"
"ds_read_b64_tr_b16 a[152:153], v216 offset:52224\n"
"v_mfma_f32_16x16x32_bf16 v[160:163], a[116:119], a[92:95], v[160:163]\n"
"ds_read_b64_tr_b16 a[154:155], v216 offset:57344\n"
"v_mfma_f32_16x16x32_bf16 v[160:163], a[156:159], v[24:27], v[160:163]\n"
"s_cmp_eq_u32 s72, 393216\n"
"s_cselect_b32 s72, 0, s72\n"
"v_mfma_f32_16x16x32_bf16 v[164:167], a[116:119], v[16:19], v[164:167]\n"
"v_mfma_f32_16x16x32_bf16 v[164:167], a[156:159], v[28:31], v[164:167]\n"
"s_waitcnt lgkmcnt(15)\n"
"ds_read_b64_tr_b16 a[116:117], v217 offset:41984\n"
"v_mfma_f32_16x16x32_bf16 v[168:171], a[120:123], a[88:91], v[168:171]\n"
"ds_read_b64_tr_b16 a[118:119], v217 offset:47104\n"
"buffer_load_dwordx4 a[16:19], v246, s[44:47], s72 offen sc1\n"
"v_mfma_f32_16x16x32_bf16 v[168:171], a[160:163], v[20:23], v[168:171]\n"
"ds_read_b64_tr_b16 a[156:157], v217 offset:52224\n"
"v_mfma_f32_16x16x32_bf16 v[172:175], a[120:123], a[92:95], v[172:175]\n"
"ds_read_b64_tr_b16 a[158:159], v217 offset:57344\n"
"v_mfma_f32_16x16x32_bf16 v[172:175], a[160:163], v[24:27], v[172:175]\n"
"v_mfma_f32_16x16x32_bf16 v[176:179], a[120:123], v[16:19], v[176:179]\n"
"buffer_load_dwordx4 a[20:23], v246, s[44:47], s72 offen offset:1024 sc1\n"
"v_mfma_f32_16x16x32_bf16 v[176:179], a[160:163], v[28:31], v[176:179]\n"
"ds_read_b64_tr_b16 a[120:121], v216 offset:42496\n"
"v_mfma_f32_16x16x32_bf16 v[180:183], a[124:127], a[88:91], v[180:183]\n"
"ds_read_b64_tr_b16 a[122:123], v216 offset:47616\n"
"v_mfma_f32_16x16x32_bf16 v[180:183], a[164:167], v[20:23], v[180:183]\n"
"ds_read_b64_tr_b16 a[160:161], v216 offset:52736\n"
"v_mfma_f32_16x16x32_bf16 v[184:187], a[124:127], a[92:95], v[184:187]\n"
"ds_read_b64_tr_b16 a[162:163], v216 offset:57856\n"
"buffer_load_dwordx4 a[24:27], v246, s[44:47], s72 offen offset:2048 sc1\n"
"v_mfma_f32_16x16x32_bf16 v[184:187], a[164:167], v[24:27], v[184:187]\n"
"v_mfma_f32_16x16x32_bf16 v[188:191], a[124:127], v[16:19], v[188:191]\n"
"v_mfma_f32_16x16x32_bf16 v[188:191], a[164:167], v[28:31], v[188:191]\n"
"ds_read_b64_tr_b16 a[124:125], v217 offset:42496\n"
"s_add_u32 s72, s72, 12288\n"
"v_mfma_f32_16x16x32_bf16 v[192:195], a[128:131], a[88:91], v[192:195]\n"
"ds_read_b64_tr_b16 a[126:127], v217 offset:47616\n"
"v_mfma_f32_16x16x32_bf16 v[192:195], a[168:171], v[20:23], v[192:195]\n"
"ds_read_b64_tr_b16 a[164:165], v217 offset:52736\n"
"v_mfma_f32_16x16x32_bf16 v[196:199], a[128:131], a[92:95], v[196:199]\n"
"ds_read_b64_tr_b16 a[166:167], v217 offset:57856\n"
"v_mfma_f32_16x16x32_bf16 v[196:199], a[168:171], v[24:27], v[196:199]\n"
"s_cmp_eq_u32 s72, 393216\n"
"s_cselect_b32 s72, 0, s72\n"
"v_mfma_f32_16x16x32_bf16 v[200:203], a[128:131], v[16:19], v[200:203]\n"
"v_mfma_f32_16x16x32_bf16 v[200:203], a[168:171], v[28:31], v[200:203]\n"
"ds_read_b64_tr_b16 a[128:129], v216 offset:43008\n"
"v_mfma_f32_16x16x32_bf16 v[204:207], a[132:135], a[88:91], v[204:207]\n"
"ds_read_b64_tr_b16 a[130:131], v216 offset:48128\n"
"v_mfma_f32_16x16x32_bf16 v[204:207], a[172:175], v[20:23], v[204:207]\n"
"ds_read_b64_tr_b16 a[168:169], v216 offset:53248\n"
"v_mfma_f32_16x16x32_bf16 v[208:211], a[132:135], a[92:95], v[208:211]\n"
"ds_read_b64_tr_b16 a[170:171], v216 offset:58368\n"
"v_mfma_f32_16x16x32_bf16 v[208:211], a[172:175], v[24:27], v[208:211]\n"
"v_mfma_f32_16x16x32_bf16 v[212:215], a[132:135], v[16:19], v[212:215]\n"
"v_mfma_f32_16x16x32_bf16 v[212:215], a[172:175], v[28:31], v[212:215]\n"
"ds_read_b64_tr_b16 a[132:133], v217 offset:43008\n"
"ds_read_b64_tr_b16 a[134:135], v217 offset:48128\n"
"ds_read_b64_tr_b16 a[172:173], v217 offset:53248\n"
"ds_read_b64_tr_b16 a[174:175], v217 offset:58368\n"
"s_waitcnt vmcnt(31) lgkmcnt(15)\n"
"s_barrier\n"
"v_mfma_f32_16x16x32_bf16 v[96:99], a[96:99], v[48:51], v[96:99]\n"
"buffer_load_dwordx4 a[28:31], v246, s[44:47], s72 offen sc1\n"
"v_mfma_f32_16x16x32_bf16 v[96:99], a[136:139], v[60:63], v[96:99]\n"
"v_mfma_f32_16x16x32_bf16 v[100:103], a[96:99], v[52:55], v[100:103]\n"
"v_mfma_f32_16x16x32_bf16 v[100:103], a[136:139], v[80:83], v[100:103]\n"
"buffer_load_dwordx4 a[32:35], v246, s[44:47], s72 offen offset:1024 sc1\n"
"v_mfma_f32_16x16x32_bf16 v[104:107], a[96:99], v[56:59], v[104:107]\n"
"v_mfma_f32_16x16x32_bf16 v[104:107], a[136:139], v[84:87], v[104:107]\n"
"ds_read_b64_tr_b16 a[96:97], v218 offset:0\n"
"v_mfma_f32_16x16x32_bf16 v[108:111], a[100:103], v[48:51], v[108:111]\n"
"ds_read_b64_tr_b16 a[98:99], v218 offset:5120\n"
"v_mfma_f32_16x16x32_bf16 v[108:111], a[140:143], v[60:63], v[108:111]\n"
"ds_read_b64_tr_b16 a[136:137], v218 offset:10240\n"
"buffer_load_dwordx4 a[36:39], v246, s[44:47], s72 offen offset:2048 sc1\n"
"v_mfma_f32_16x16x32_bf16 v[112:115], a[100:103], v[52:55], v[112:115]\n"
"ds_read_b64_tr_b16 a[138:139], v218 offset:15360\n"
"v_mfma_f32_16x16x32_bf16 v[112:115], a[140:143], v[80:83], v[112:115]\n"
"v_mfma_f32_16x16x32_bf16 v[116:119], a[100:103], v[56:59], v[116:119]\n"
"v_mfma_f32_16x16x32_bf16 v[116:119], a[140:143], v[84:87], v[116:119]\n"
"ds_read_b64_tr_b16 a[100:101], v219 offset:0\n"
"s_add_u32 s72, s72, 12288\n"
"v_mfma_f32_16x16x32_bf16 v[120:123], a[104:107], v[48:51], v[120:123]\n"
"ds_read_b64_tr_b16 a[102:103], v219 offset:5120\n"
"v_mfma_f32_16x16x32_bf16 v[120:123], a[144:147], v[60:63], v[120:123]\n"
"ds_read_b64_tr_b16 a[140:141], v219 offset:10240\n"
"v_mfma_f32_16x16x32_bf16 v[124:127], a[104:107], v[52:55], v[124:127]\n"
"ds_read_b64_tr_b16 a[142:143], v219 offset:15360\n"
"v_mfma_f32_16x16x32_bf16 v[124:127], a[144:147], v[80:83], v[124:127]\n"
"s_cmp_eq_u32 s72, 393216\n"
"s_cselect_b32 s72, 0, s72\n"
"v_mfma_f32_16x16x32_bf16 v[128:131], a[104:107], v[56:59], v[128:131]\n"
"v_mfma_f32_16x16x32_bf16 v[128:131], a[144:147], v[84:87], v[128:131]\n"
"ds_read_b64_tr_b16 a[104:105], v218 offset:512\n"
"v_mfma_f32_16x16x32_bf16 v[132:135], a[108:111], v[48:51], v[132:135]\n"
"ds_read_b64_tr_b16 a[106:107], v218 offset:5632\n"
"buffer_load_dwordx4 a[40:43], v246, s[44:47], s72 offen sc1\n"
"v_mfma_f32_16x16x32_bf16 v[132:135], a[148:151], v[60:63], v[132:135]\n"
"ds_read_b64_tr_b16 a[144:145], v218 offset:10752\n"
"v_mfma_f32_16x16x32_bf16 v[136:139], a[108:111], v[52:55], v[136:139]\n"
"ds_read_b64_tr_b16 a[146:147], v218 offset:15872\n"
"v_mfma_f32_16x16x32_bf16 v[136:139], a[148:151], v[80:83], v[136:139]\n"
"v_mfma_f32_16x16x32_bf16 v[140:143], a[108:111], v[56:59], v[140:143]\n"
"buffer_load_dwordx4 a[44:47], v246, s[44:47], s72 offen offset:1024 sc1\n"
"v_mfma_f32_16x16x32_bf16 v[140:143], a[148:151], v[84:87], v[140:143]\n"
"ds_read_b64_tr_b16 a[108:109], v219 offset:512\n"
"v_mfma_f32_16x16x32_bf16 v[144:147], a[112:115], v[48:51], v[144:147]\n"
"ds_read_b64_tr_b16 a[110:111], v219 offset:5632\n"
"v_mfma_f32_16x16x32_bf16 v[144:147], a[152:155], v[60:63], v[144:147]\n"
"ds_read_b64_tr_b16 a[148:149], v219 offset:10752\n"
"buffer_load_dwordx4 a[176:179], v246, s[44:47], s72 offen offset:2048 sc1\n"
"v_mfma_f32_16x16x32_bf16 v[148:151], a[112:115], v[52:55], v[148:151]\n"
"ds_read_b64_tr_b16 a[150:151], v219 offset:15872\n"
"v_mfma_f32_16x16x32_bf16 v[148:151], a[152:155], v[80:83], v[148:151]\n"
"v_mfma_f32_16x16x32_bf16 v[152:155], a[112:115], v[56:59], v[152:155]\n"
"v_mfma_f32_16x16x32_bf16 v[152:155], a[152:155], v[84:87], v[152:155]\n"
"ds_read_b64_tr_b16 a[112:113], v218 offset:1024\n"
"s_add_u32 s72, s72, 12288\n"
"v_mfma_f32_16x16x32_bf16 v[156:159], a[116:119], v[48:51], v[156:159]\n"
"ds_read_b64_tr_b16 a[114:115], v218 offset:6144\n"
"v_mfma_f32_16x16x32_bf16 v[156:159], a[156:159], v[60:63], v[156:159]\n"
"ds_read_b64_tr_b16 a[152:153], v218 offset:11264\n"
"v_mfma_f32_16x16x32_bf16 v[160:163], a[116:119], v[52:55], v[160:163]\n"
"ds_read_b64_tr_b16 a[154:155], v218 offset:16384\n"
"v_mfma_f32_16x16x32_bf16 v[160:163], a[156:159], v[80:83], v[160:163]\n"
"s_cmp_eq_u32 s72, 393216\n"
"s_cselect_b32 s72, 0, s72\n"
"v_mfma_f32_16x16x32_bf16 v[164:167], a[116:119], v[56:59], v[164:167]\n"
"v_mfma_f32_16x16x32_bf16 v[164:167], a[156:159], v[84:87], v[164:167]\n"
"s_waitcnt lgkmcnt(15)\n"
"ds_read_b64_tr_b16 a[116:117], v219 offset:1024\n"
"v_mfma_f32_16x16x32_bf16 v[168:171], a[120:123], v[48:51], v[168:171]\n"
"ds_read_b64_tr_b16 a[118:119], v219 offset:6144\n"
"buffer_load_dwordx4 a[180:183], v246, s[44:47], s72 offen sc1\n"
"v_mfma_f32_16x16x32_bf16 v[168:171], a[160:163], v[60:63], v[168:171]\n"
"ds_read_b64_tr_b16 a[156:157], v219 offset:11264\n"
"v_mfma_f32_16x16x32_bf16 v[172:175], a[120:123], v[52:55], v[172:175]\n"
"ds_read_b64_tr_b16 a[158:159], v219 offset:16384\n"
"v_mfma_f32_16x16x32_bf16 v[172:175], a[160:163], v[80:83], v[172:175]\n"
"v_mfma_f32_16x16x32_bf16 v[176:179], a[120:123], v[56:59], v[176:179]\n"
"buffer_load_dwordx4 a[184:187], v246, s[44:47], s72 offen offset:1024 sc1\n"
"v_mfma_f32_16x16x32_bf16 v[176:179], a[160:163], v[84:87], v[176:179]\n"
"ds_read_b64_tr_b16 a[120:121], v218 offset:1536\n"
"v_mfma_f32_16x16x32_bf16 v[180:183], a[124:127], v[48:51], v[180:183]\n"
"ds_read_b64_tr_b16 a[122:123], v218 offset:6656\n"
"v_mfma_f32_16x16x32_bf16 v[180:183], a[164:167], v[60:63], v[180:183]\n"
"ds_read_b64_tr_b16 a[160:161], v218 offset:11776\n"
"v_mfma_f32_16x16x32_bf16 v[184:187], a[124:127], v[52:55], v[184:187]\n"
"ds_read_b64_tr_b16 a[162:163], v218 offset:16896\n"
"buffer_load_dwordx4 a[188:191], v246, s[44:47], s72 offen offset:2048 sc1\n"
"v_mfma_f32_16x16x32_bf16 v[184:187], a[164:167], v[80:83], v[184:187]\n"
"v_mfma_f32_16x16x32_bf16 v[188:191], a[124:127], v[56:59], v[188:191]\n"
"v_mfma_f32_16x16x32_bf16 v[188:191], a[164:167], v[84:87], v[188:191]\n"
"ds_read_b64_tr_b16 a[124:125], v219 offset:1536\n"
"s_add_u32 s72, s72, 12288\n"
"v_mfma_f32_16x16x32_bf16 v[192:195], a[128:131], v[48:51], v[192:195]\n"
"ds_read_b64_tr_b16 a[126:127], v219 offset:6656\n"
"v_mfma_f32_16x16x32_bf16 v[192:195], a[168:171], v[60:63], v[192:195]\n"
"ds_read_b64_tr_b16 a[164:165], v219 offset:11776\n"
"v_mfma_f32_16x16x32_bf16 v[196:199], a[128:131], v[52:55], v[196:199]\n"
"ds_read_b64_tr_b16 a[166:167], v219 offset:16896\n"
"v_mfma_f32_16x16x32_bf16 v[196:199], a[168:171], v[80:83], v[196:199]\n"
"s_cmp_eq_u32 s72, 393216\n"
"s_cselect_b32 s72, 0, s72\n"
"v_mfma_f32_16x16x32_bf16 v[200:203], a[128:131], v[56:59], v[200:203]\n"
"v_mfma_f32_16x16x32_bf16 v[200:203], a[168:171], v[84:87], v[200:203]\n"
"ds_read_b64_tr_b16 a[128:129], v218 offset:2048\n"
"v_mfma_f32_16x16x32_bf16 v[204:207], a[132:135], v[48:51], v[204:207]\n"
"ds_read_b64_tr_b16 a[130:131], v218 offset:7168\n"
"v_mfma_f32_16x16x32_bf16 v[204:207], a[172:175], v[60:63], v[204:207]\n"
"ds_read_b64_tr_b16 a[168:169], v218 offset:12288\n"
"v_mfma_f32_16x16x32_bf16 v[208:211], a[132:135], v[52:55], v[208:211]\n"
"ds_read_b64_tr_b16 a[170:171], v218 offset:17408\n"
"v_mfma_f32_16x16x32_bf16 v[208:211], a[172:175], v[80:83], v[208:211]\n"
"v_mfma_f32_16x16x32_bf16 v[212:215], a[132:135], v[56:59], v[212:215]\n"
"v_mfma_f32_16x16x32_bf16 v[212:215], a[172:175], v[84:87], v[212:215]\n"
"ds_read_b64_tr_b16 a[132:133], v219 offset:2048\n"
"ds_read_b64_tr_b16 a[134:135], v219 offset:7168\n"
"ds_read_b64_tr_b16 a[172:173], v219 offset:12288\n"
"ds_read_b64_tr_b16 a[174:175], v219 offset:17408\n"
"s_waitcnt vmcnt(18) lgkmcnt(15)\n"
"s_barrier\n"
"v_mfma_f32_16x16x32_bf16 v[96:99], a[96:99], v[88:91], v[96:99]\n"
"v_mfma_f32_16x16x32_bf16 v[96:99], a[136:139], v[4:7], v[96:99]\n"
"v_mfma_f32_16x16x32_bf16 v[100:103], a[96:99], v[92:95], v[100:103]\n"
"v_mfma_f32_16x16x32_bf16 v[100:103], a[136:139], v[8:11], v[100:103]\n"
"v_mfma_f32_16x16x32_bf16 v[104:107], a[96:99], v[0:3], v[104:107]\n"
"v_mfma_f32_16x16x32_bf16 v[104:107], a[136:139], v[12:15], v[104:107]\n"
"ds_read_b64_tr_b16 a[96:97], v218 offset:20480\n"
"v_mfma_f32_16x16x32_bf16 v[108:111], a[100:103], v[88:91], v[108:111]\n"
"ds_read_b64_tr_b16 a[98:99], v218 offset:25600\n"
"v_mfma_f32_16x16x32_bf16 v[108:111], a[140:143], v[4:7], v[108:111]\n"
"v_mfma_f32_16x16x32_bf16 v[112:115], a[100:103], v[92:95], v[112:115]\n"
"v_mfma_f32_16x16x32_bf16 v[112:115], a[140:143], v[8:11], v[112:115]\n"
"v_mfma_f32_16x16x32_bf16 v[116:119], a[100:103], v[0:3], v[116:119]\n"
"v_mfma_f32_16x16x32_bf16 v[116:119], a[140:143], v[12:15], v[116:119]\n"
"ds_read_b64_tr_b16 a[100:101], v218 offset:30720\n"
"v_mfma_f32_16x16x32_bf16 v[120:123], a[104:107], v[88:91], v[120:123]\n"
"ds_read_b64_tr_b16 a[102:103], v218 offset:35840\n"
"v_mfma_f32_16x16x32_bf16 v[120:123], a[144:147], v[4:7], v[120:123]\n"
"v_mfma_f32_16x16x32_bf16 v[124:127], a[104:107], v[92:95], v[124:127]\n"
"v_mfma_f32_16x16x32_bf16 v[124:127], a[144:147], v[8:11], v[124:127]\n"
"v_mfma_f32_16x16x32_bf16 v[128:131], a[104:107], v[0:3], v[128:131]\n"
"v_mfma_f32_16x16x32_bf16 v[128:131], a[144:147], v[12:15], v[128:131]\n"
"ds_read_b64_tr_b16 a[104:105], v218 offset:40960\n"
"v_mfma_f32_16x16x32_bf16 v[132:135], a[108:111], v[88:91], v[132:135]\n"
"ds_read_b64_tr_b16 a[106:107], v218 offset:46080\n"
"v_mfma_f32_16x16x32_bf16 v[132:135], a[148:151], v[4:7], v[132:135]\n"
"v_mfma_f32_16x16x32_bf16 v[136:139], a[108:111], v[92:95], v[136:139]\n"
"v_mfma_f32_16x16x32_bf16 v[136:139], a[148:151], v[8:11], v[136:139]\n"
"v_mfma_f32_16x16x32_bf16 v[140:143], a[108:111], v[0:3], v[140:143]\n"
"v_mfma_f32_16x16x32_bf16 v[140:143], a[148:151], v[12:15], v[140:143]\n"
"ds_read_b64_tr_b16 a[108:109], v218 offset:51200\n"
"v_mfma_f32_16x16x32_bf16 v[144:147], a[112:115], v[88:91], v[144:147]\n"
"ds_read_b64_tr_b16 a[110:111], v218 offset:56320\n"
"v_mfma_f32_16x16x32_bf16 v[144:147], a[152:155], v[4:7], v[144:147]\n"
"v_mfma_f32_16x16x32_bf16 v[148:151], a[112:115], v[92:95], v[148:151]\n"
"v_mfma_f32_16x16x32_bf16 v[148:151], a[152:155], v[8:11], v[148:151]\n"
"v_mfma_f32_16x16x32_bf16 v[152:155], a[112:115], v[0:3], v[152:155]\n"
"v_mfma_f32_16x16x32_bf16 v[152:155], a[152:155], v[12:15], v[152:155]\n"
"ds_read_b64_tr_b16 a[112:113], v220 offset:0\n"
"v_mfma_f32_16x16x32_bf16 v[156:159], a[116:119], v[88:91], v[156:159]\n"
"ds_read_b64_tr_b16 a[114:115], v220 offset:5120\n"
"v_mfma_f32_16x16x32_bf16 v[156:159], a[156:159], v[4:7], v[156:159]\n"
"v_mfma_f32_16x16x32_bf16 v[160:163], a[116:119], v[92:95], v[160:163]\n"
"v_mfma_f32_16x16x32_bf16 v[160:163], a[156:159], v[8:11], v[160:163]\n"
"v_mfma_f32_16x16x32_bf16 v[164:167], a[116:119], v[0:3], v[164:167]\n"
"v_mfma_f32_16x16x32_bf16 v[164:167], a[156:159], v[12:15], v[164:167]\n"
"s_waitcnt lgkmcnt(15)\n"
"ds_read_b64_tr_b16 a[116:117], v220 offset:10240\n"
"v_mfma_f32_16x16x32_bf16 v[168:171], a[120:123], v[88:91], v[168:171]\n"
"ds_read_b64_tr_b16 a[118:119], v220 offset:15360\n"
"v_mfma_f32_16x16x32_bf16 v[168:171], a[160:163], v[4:7], v[168:171]\n"
"v_mfma_f32_16x16x32_bf16 v[172:175], a[120:123], v[92:95], v[172:175]\n"
"v_mfma_f32_16x16x32_bf16 v[172:175], a[160:163], v[8:11], v[172:175]\n"
"v_mfma_f32_16x16x32_bf16 v[176:179], a[120:123], v[0:3], v[176:179]\n"
"v_mfma_f32_16x16x32_bf16 v[176:179], a[160:163], v[12:15], v[176:179]\n"
"ds_read_b64_tr_b16 a[120:121], v220 offset:20480\n"
"v_mfma_f32_16x16x32_bf16 v[180:183], a[124:127], v[88:91], v[180:183]\n"
"ds_read_b64_tr_b16 a[122:123], v220 offset:25600\n"
"v_mfma_f32_16x16x32_bf16 v[180:183], a[164:167], v[4:7], v[180:183]\n"
"v_mfma_f32_16x16x32_bf16 v[184:187], a[124:127], v[92:95], v[184:187]\n"
"v_mfma_f32_16x16x32_bf16 v[184:187], a[164:167], v[8:11], v[184:187]\n"
"v_mfma_f32_16x16x32_bf16 v[188:191], a[124:127], v[0:3], v[188:191]\n"
"v_mfma_f32_16x16x32_bf16 v[188:191], a[164:167], v[12:15], v[188:191]\n"
"ds_read_b64_tr_b16 a[124:125], v220 offset:30720\n"
"v_mfma_f32_16x16x32_bf16 v[192:195], a[128:131], v[88:91], v[192:195]\n"
"ds_read_b64_tr_b16 a[126:127], v220 offset:35840\n"
"s_waitcnt lgkmcnt(15)\n"
"v_mfma_f32_16x16x32_bf16 v[192:195], a[168:171], v[4:7], v[192:195]\n"
"v_mfma_f32_16x16x32_bf16 v[196:199], a[128:131], v[92:95], v[196:199]\n"
"v_mfma_f32_16x16x32_bf16 v[196:199], a[168:171], v[8:11], v[196:199]\n"
"v_mfma_f32_16x16x32_bf16 v[200:203], a[128:131], v[0:3], v[200:203]\n"
"v_mfma_f32_16x16x32_bf16 v[200:203], a[168:171], v[12:15], v[200:203]\n"
"s_waitcnt lgkmcnt(15)\n"
"v_mfma_f32_16x16x32_bf16 v[204:207], a[132:135], v[88:91], v[204:207]\n"
"s_waitcnt lgkmcnt(15)\n"
"v_mfma_f32_16x16x32_bf16 v[204:207], a[172:175], v[4:7], v[204:207]\n"
"v_mfma_f32_16x16x32_bf16 v[208:211], a[132:135], v[92:95], v[208:211]\n"
"v_mfma_f32_16x16x32_bf16 v[208:211], a[172:175], v[8:11], v[208:211]\n"
"v_mfma_f32_16x16x32_bf16 v[212:215], a[132:135], v[0:3], v[212:215]\n"
"v_mfma_f32_16x16x32_bf16 v[212:215], a[172:175], v[12:15], v[212:215]\n"
"s_and_b32 s70, %15, 3\n"
"s_lshl_b32 s70, s70, 2\n"
"s_add_u32 s71, s70, s60\n"
"s_mul_i32 s72, s71, 15360\n"
"s_and_b32 s70, %15, 3\n"
"s_add_u32 s69, s72, 0\n"
"s_cmp_eq_u32 s70, 0\n"
"s_cselect_b32 s69, 0x40000000, s69\n"
"s_add_u32 s77, s72, 4096\n"
"s_cmp_eq_u32 s70, 1\n"
"s_cselect_b32 s77, 0x40000000, s77\n"
"s_add_u32 s79, s72, 8192\n"
"s_cmp_eq_u32 s70, 2\n"
"s_cselect_b32 s79, 0x40000000, s79\n"
"s_add_u32 s97, s72, 12288\n"
"s_cmp_eq_u32 s70, 3\n"
"s_cselect_b32 s97, 0x40000000, s97\n"
"s_mul_i32 s71, s60, 15360\n"
"v_add_u32_e32 v239, s71, v246\n"
"s_waitcnt vmcnt(0) lgkmcnt(0)\n"
"v_mfma_f32_16x16x32_bf16 v[96:99], a[96:99], v[32:35], v[96:99]\n"
"ds_read_b64_tr_b16 a[136:137], v219 offset:20480\n"
"s_and_b32 s70, %15, 3\n"
"v_mfma_f32_16x16x32_bf16 v[96:99], a[100:103], v[44:47], v[96:99]\n"
"ds_read_b64_tr_b16 a[138:139], v219 offset:25600\n"
"s_lshl_b32 s70, s70, 7\n"
"v_mfma_f32_16x16x32_bf16 v[96:99], a[104:107], v[72:75], v[96:99]\n"
"ds_read_b64_tr_b16 a[140:141], v219 offset:30720\n"
"s_add_u32 s70, s70, s95\n"
"v_mfma_f32_16x16x32_bf16 v[96:99], a[108:111], a[4:7], v[96:99]\n"
"ds_read_b64_tr_b16 a[142:143], v219 offset:35840\n"
"v_mfma_f32_16x16x32_bf16 v[96:99], a[112:115], a[16:19], v[96:99]\n"
"v_mbcnt_lo_u32_b32 v226, -1, 0\n"
"v_mbcnt_hi_u32_b32 v226, -1, v226\n"
"v_mfma_f32_16x16x32_bf16 v[96:99], a[116:119], a[28:31], v[96:99]\n"
"v_or_b32_e32 v226, s60, v226\n"
"ds_read_b64_tr_b16 a[144:145], v219 offset:40960\n"
"v_mfma_f32_16x16x32_bf16 v[96:99], a[120:123], a[40:43], v[96:99]\n"
"v_cmp_eq_u32_e32 vcc, 0, v226\n"
"v_bfrev_b32_e32 v226, 1\n"
"v_mfma_f32_16x16x32_bf16 v[96:99], a[124:127], a[180:183], v[96:99]\n"
"ds_read_b64_tr_b16 a[146:147], v219 offset:46080\n"
"v_mov_b32_e32 v227, s70\n"
"v_mfma_f32_16x16x32_bf16 v[100:103], a[96:99], v[36:39], v[100:103]\n"
"ds_read_b64_tr_b16 a[148:149], v219 offset:51200\n"
"v_cndmask_b32_e32 v226, v226, v227, vcc\n"
"v_mfma_f32_16x16x32_bf16 v[100:103], a[100:103], v[64:67], v[100:103]\n"
"ds_read_b64_tr_b16 a[150:151], v219 offset:56320\n"
"v_mfma_f32_16x16x32_bf16 v[100:103], a[104:107], v[76:79], v[100:103]\n"
"v_mbcnt_lo_u32_b32 v236, -1, 0\n"
"v_mbcnt_hi_u32_b32 v236, -1, v236\n"
"v_mfma_f32_16x16x32_bf16 v[100:103], a[108:111], a[8:11], v[100:103]\n"
"v_and_b32_e32 v237, 3, v236\n"
"ds_read_b64_tr_b16 a[152:153], v221 offset:0\n"
"v_mfma_f32_16x16x32_bf16 v[100:103], a[112:115], a[20:23], v[100:103]\n"
"v_lshlrev_b32_e32 v237, 7, v237\n"
"ds_read_b64_tr_b16 a[154:155], v221 offset:5120\n"
"v_mfma_f32_16x16x32_bf16 v[100:103], a[116:119], a[32:35], v[100:103]\n"
"ds_read_b64_tr_b16 a[156:157], v221 offset:10240\n"
"v_and_b32_e32 v238, 15, v236\n"
"v_mfma_f32_16x16x32_bf16 v[100:103], a[120:123], a[44:47], v[100:103]\n"
"ds_read_b64_tr_b16 a[158:159], v221 offset:15360\n"
"v_mul_u32_u24_e32 v238, 0xa00, v238\n"
"v_mfma_f32_16x16x32_bf16 v[100:103], a[124:127], a[184:187], v[100:103]\n"
"v_lshrrev_b32_e32 v236, 4, v236\n"
"ds_read_b64_tr_b16 a[160:161], v221 offset:20480\n"
"v_mfma_f32_16x16x32_bf16 v[104:107], a[96:99], v[40:43], v[104:107]\n"
"v_lshl_add_u32 v238, v236, 4, v238\n"
"ds_read_b64_tr_b16 a[162:163], v221 offset:25600\n"
"v_mfma_f32_16x16x32_bf16 v[104:107], a[100:103], v[68:71], v[104:107]\n"
"s_and_b32 s70, %15, 3\n"
"ds_read_b64_tr_b16 a[164:165], v221 offset:30720\n"
"v_mfma_f32_16x16x32_bf16 v[104:107], a[104:107], a[0:3], v[104:107]\n"
"s_lshl_b32 s80, s70, 2\n"
"ds_read_b64_tr_b16 a[166:167], v221 offset:35840\n"
"v_mfma_f32_16x16x32_bf16 v[104:107], a[108:111], a[12:15], v[104:107]\n"
"s_lshl_b32 s70, s70, 12\n"
"s_mul_i32 s71, s60, 15360\n"
"v_mfma_f32_16x16x32_bf16 v[104:107], a[112:115], a[24:27], v[104:107]\n"
"s_add_u32 s73, s70, s71\n"
"v_mfma_f32_16x16x32_bf16 v[104:107], a[116:119], a[36:39], v[104:107]\n"
"s_mov_b32 s74, 0x3f80\n"
"v_mfma_f32_16x16x32_bf16 v[104:107], a[120:123], a[176:179], v[104:107]\n"
"s_mov_b32 s75, 0x3f800000\n"
"v_mfma_f32_16x16x32_bf16 v[104:107], a[124:127], a[188:191], v[104:107]\n"
"s_waitcnt lgkmcnt(0)\n"
"v_mfma_f32_16x16x32_bf16 v[108:111], a[136:139], v[32:35], v[108:111]\n"
"ds_read_b64_tr_b16 a[96:97], v218 offset:20992\n"
"s_add_u32 s70, s80, 0\n"
"v_mfma_f32_16x16x32_bf16 v[108:111], a[140:143], v[44:47], v[108:111]\n"
"ds_read_b64_tr_b16 a[98:99], v218 offset:26112\n"
"s_mul_i32 s71, s70, 43\n"
"v_mfma_f32_16x16x32_bf16 v[108:111], a[144:147], v[72:75], v[108:111]\n"
"ds_read_b64_tr_b16 a[100:101], v218 offset:31232\n"
"v_mfma_f32_16x16x32_bf16 v[108:111], a[148:151], a[4:7], v[108:111]\n"
"ds_read_b64_tr_b16 a[102:103], v218 offset:36352\n"
"s_lshr_b32 s71, s71, 7\n"
"v_mfma_f32_16x16x32_bf16 v[108:111], a[152:155], a[16:19], v[108:111]\n"
"s_mul_i32 s81, s71, 3\n"
"v_mfma_f32_16x16x32_bf16 v[108:111], a[156:159], a[28:31], v[108:111]\n"
"ds_read_b64_tr_b16 a[104:105], v218 offset:41472\n"
"v_mfma_f32_16x16x32_bf16 v[108:111], a[160:163], a[40:43], v[108:111]\n"
"ds_read_b64_tr_b16 a[106:107], v218 offset:46592\n"
"s_sub_u32 s81, s70, s81\n"
"v_mfma_f32_16x16x32_bf16 v[108:111], a[164:167], a[180:183], v[108:111]\n"
"ds_read_b64_tr_b16 a[108:109], v218 offset:51712\n"
"s_mul_i32 s81, s81, 40960\n"
"v_mfma_f32_16x16x32_bf16 v[112:115], a[136:139], v[36:39], v[112:115]\n"
"ds_read_b64_tr_b16 a[110:111], v218 offset:56832\n"
"v_mfma_f32_16x16x32_bf16 v[112:115], a[140:143], v[64:67], v[112:115]\n"
"s_lshl_b32 s71, s71, 6\n"
"v_mfma_f32_16x16x32_bf16 v[112:115], a[144:147], v[76:79], v[112:115]\n"
"ds_read_b64_tr_b16 a[112:113], v220 offset:512\n"
"s_add_u32 s64, s81, s71\n"
"v_mfma_f32_16x16x32_bf16 v[112:115], a[148:151], a[8:11], v[112:115]\n"
"ds_read_b64_tr_b16 a[114:115], v220 offset:5632\n"
"v_mfma_f32_16x16x32_bf16 v[112:115], a[152:155], a[20:23], v[112:115]\n"
"ds_read_b64_tr_b16 a[116:117], v220 offset:10752\n"
"s_add_u32 s70, s80, 1\n"
"v_mfma_f32_16x16x32_bf16 v[112:115], a[156:159], a[32:35], v[112:115]\n"
"ds_read_b64_tr_b16 a[118:119], v220 offset:15872\n"
"s_mul_i32 s71, s70, 43\n"
"v_mfma_f32_16x16x32_bf16 v[112:115], a[160:163], a[44:47], v[112:115]\n"
"v_mfma_f32_16x16x32_bf16 v[112:115], a[164:167], a[184:187], v[112:115]\n"
"ds_read_b64_tr_b16 a[120:121], v220 offset:20992\n"
"s_lshr_b32 s71, s71, 7\n"
"v_mfma_f32_16x16x32_bf16 v[116:119], a[136:139], v[40:43], v[116:119]\n"
"ds_read_b64_tr_b16 a[122:123], v220 offset:26112\n"
"s_mul_i32 s81, s71, 3\n"
"v_mfma_f32_16x16x32_bf16 v[116:119], a[140:143], v[68:71], v[116:119]\n"
"ds_read_b64_tr_b16 a[124:125], v220 offset:31232\n"
"v_mfma_f32_16x16x32_bf16 v[116:119], a[144:147], a[0:3], v[116:119]\n"
"ds_read_b64_tr_b16 a[126:127], v220 offset:36352\n"
"s_sub_u32 s81, s70, s81\n"
"v_mfma_f32_16x16x32_bf16 v[116:119], a[148:151], a[12:15], v[116:119]\n"
"s_mul_i32 s81, s81, 40960\n"
"v_mfma_f32_16x16x32_bf16 v[116:119], a[152:155], a[24:27], v[116:119]\n"
"v_mfma_f32_16x16x32_bf16 v[116:119], a[156:159], a[36:39], v[116:119]\n"
"s_lshl_b32 s71, s71, 6\n"
"v_mfma_f32_16x16x32_bf16 v[116:119], a[160:163], a[176:179], v[116:119]\n"
"s_add_u32 s66, s81, s71\n"
"v_mfma_f32_16x16x32_bf16 v[116:119], a[164:167], a[188:191], v[116:119]\n"
"s_waitcnt lgkmcnt(0)\n"
"v_mfma_f32_16x16x32_bf16 v[120:123], a[96:99], v[32:35], v[120:123]\n"
"ds_read_b64_tr_b16 a[136:137], v219 offset:20992\n"
"v_mfma_f32_16x16x32_bf16 v[120:123], a[100:103], v[44:47], v[120:123]\n"
"ds_read_b64_tr_b16 a[138:139], v219 offset:26112\n"
"v_mfma_f32_16x16x32_bf16 v[120:123], a[104:107], v[72:75], v[120:123]\n"
"ds_read_b64_tr_b16 a[140:141], v219 offset:31232\n"
"v_mfma_f32_16x16x32_bf16 v[120:123], a[108:111], a[4:7], v[120:123]\n"
"ds_read_b64_tr_b16 a[142:143], v219 offset:36352\n"
"v_cvt_pk_bf16_f32 v96, v96, v97\n"
"v_mfma_f32_16x16x32_bf16 v[120:123], a[112:115], a[16:19], v[120:123]\n"
"v_mfma_f32_16x16x32_bf16 v[120:123], a[116:119], a[28:31], v[120:123]\n"
"ds_read_b64_tr_b16 a[144:145], v219 offset:41472\n"
"v_cvt_pk_bf16_f32 v97, v98, v99\n"
"v_mfma_f32_16x16x32_bf16 v[120:123], a[120:123], a[40:43], v[120:123]\n"
"ds_read_b64_tr_b16 a[146:147], v219 offset:46592\n"
"v_mfma_f32_16x16x32_bf16 v[120:123], a[124:127], a[180:183], v[120:123]\n"
"ds_read_b64_tr_b16 a[148:149], v219 offset:51712\n"
"v_cvt_pk_bf16_f32 v98, v108, v109\n"
"v_mfma_f32_16x16x32_bf16 v[124:127], a[96:99], v[36:39], v[124:127]\n"
"ds_read_b64_tr_b16 a[150:151], v219 offset:56832\n"
"v_mfma_f32_16x16x32_bf16 v[124:127], a[100:103], v[64:67], v[124:127]\n"
"v_mfma_f32_16x16x32_bf16 v[124:127], a[104:107], v[76:79], v[124:127]\n"
"ds_read_b64_tr_b16 a[152:153], v221 offset:512\n"
"v_cvt_pk_bf16_f32 v99, v110, v111\n"
"v_mfma_f32_16x16x32_bf16 v[124:127], a[108:111], a[8:11], v[124:127]\n"
"ds_read_b64_tr_b16 a[154:155], v221 offset:5632\n"
"v_mfma_f32_16x16x32_bf16 v[124:127], a[112:115], a[20:23], v[124:127]\n"
"ds_read_b64_tr_b16 a[156:157], v221 offset:10752\n"
"buffer_store_dwordx4 v[96:99], v246, s[48:51], s69 offen sc1\n"
"v_mfma_f32_16x16x32_bf16 v[124:127], a[116:119], a[32:35], v[124:127]\n"
"ds_read_b64_tr_b16 a[158:159], v221 offset:15872\n"
"v_mfma_f32_16x16x32_bf16 v[124:127], a[120:123], a[44:47], v[124:127]\n"
"ds_write_b128 v239, v[96:99] offset:0\n"
"v_mfma_f32_16x16x32_bf16 v[124:127], a[124:127], a[184:187], v[124:127]\n"
"ds_read_b64_tr_b16 a[160:161], v221 offset:20992\n"
"v_mfma_f32_16x16x32_bf16 v[128:131], a[96:99], v[40:43], v[128:131]\n"
"ds_read_b64_tr_b16 a[162:163], v221 offset:26112\n"
"v_mfma_f32_16x16x32_bf16 v[128:131], a[100:103], v[68:71], v[128:131]\n"
"ds_read_b64_tr_b16 a[164:165], v221 offset:31232\n"
"v_cvt_pk_bf16_f32 v100, v100, v101\n"
"v_mfma_f32_16x16x32_bf16 v[128:131], a[104:107], a[0:3], v[128:131]\n"
"ds_read_b64_tr_b16 a[166:167], v221 offset:36352\n"
"v_mfma_f32_16x16x32_bf16 v[128:131], a[108:111], a[12:15], v[128:131]\n"
"v_cvt_pk_bf16_f32 v101, v102, v103\n"
"v_mfma_f32_16x16x32_bf16 v[128:131], a[112:115], a[24:27], v[128:131]\n"
"v_mfma_f32_16x16x32_bf16 v[128:131], a[116:119], a[36:39], v[128:131]\n"
"v_cvt_pk_bf16_f32 v102, v112, v113\n"
"v_mfma_f32_16x16x32_bf16 v[128:131], a[120:123], a[176:179], v[128:131]\n"
"v_mfma_f32_16x16x32_bf16 v[128:131], a[124:127], a[188:191], v[128:131]\n"
"s_waitcnt lgkmcnt(0)\n"
"v_mfma_f32_16x16x32_bf16 v[132:135], a[136:139], v[32:35], v[132:135]\n"
"ds_read_b64_tr_b16 a[96:97], v218 offset:21504\n"
"v_cvt_pk_bf16_f32 v103, v114, v115\n"
"v_mfma_f32_16x16x32_bf16 v[132:135], a[140:143], v[44:47], v[132:135]\n"
"s_add_u32 s70, s80, 2\n"
"ds_read_b64_tr_b16 a[98:99], v218 offset:26624\n"
"v_mfma_f32_16x16x32_bf16 v[132:135], a[144:147], v[72:75], v[132:135]\n"
"s_mul_i32 s71, s70, 43\n"
"ds_read_b64_tr_b16 a[100:101], v218 offset:31744\n"
"v_mfma_f32_16x16x32_bf16 v[132:135], a[148:151], a[4:7], v[132:135]\n"
"buffer_store_dwordx4 v[100:103], v246, s[48:51], s69 offen offset:1024 sc1\n"
"s_lshr_b32 s71, s71, 7\n"
"v_mfma_f32_16x16x32_bf16 v[132:135], a[152:155], a[16:19], v[132:135]\n"
"ds_read_b64_tr_b16 a[102:103], v218 offset:36864\n"
"s_mul_i32 s81, s71, 3\n"
"v_mfma_f32_16x16x32_bf16 v[132:135], a[156:159], a[28:31], v[132:135]\n"
"ds_read_b64_tr_b16 a[104:105], v218 offset:41984\n"
"ds_write_b128 v239, v[100:103] offset:1024\n"
"v_mfma_f32_16x16x32_bf16 v[132:135], a[160:163], a[40:43], v[132:135]\n"
"s_sub_u32 s81, s70, s81\n"
"ds_read_b64_tr_b16 a[106:107], v218 offset:47104\n"
"v_mfma_f32_16x16x32_bf16 v[132:135], a[164:167], a[180:183], v[132:135]\n"
"ds_read_b64_tr_b16 a[108:109], v218 offset:52224\n"
"s_mul_i32 s81, s81, 40960\n"
"v_mfma_f32_16x16x32_bf16 v[136:139], a[136:139], v[36:39], v[136:139]\n"
"ds_read_b64_tr_b16 a[110:111], v218 offset:57344\n"
"v_cvt_pk_bf16_f32 v104, v104, v105\n"
"v_mfma_f32_16x16x32_bf16 v[136:139], a[140:143], v[64:67], v[136:139]\n"
"s_lshl_b32 s71, s71, 6\n"
"s_add_u32 s67, s81, s71\n"
"v_mfma_f32_16x16x32_bf16 v[136:139], a[144:147], v[76:79], v[136:139]\n"
"ds_read_b64_tr_b16 a[112:113], v220 offset:1024\n"
"v_cvt_pk_bf16_f32 v105, v106, v107\n"
"v_mfma_f32_16x16x32_bf16 v[136:139], a[148:151], a[8:11], v[136:139]\n"
"ds_read_b64_tr_b16 a[114:115], v220 offset:6144\n"
"s_add_u32 s70, s80, 3\n"
"v_mfma_f32_16x16x32_bf16 v[136:139], a[152:155], a[20:23], v[136:139]\n"
"ds_read_b64_tr_b16 a[116:117], v220 offset:11264\n"
"s_mul_i32 s71, s70, 43\n"
"v_mfma_f32_16x16x32_bf16 v[136:139], a[156:159], a[32:35], v[136:139]\n"
"ds_read_b64_tr_b16 a[118:119], v220 offset:16384\n"
"v_cvt_pk_bf16_f32 v106, v116, v117\n"
"v_mfma_f32_16x16x32_bf16 v[136:139], a[160:163], a[44:47], v[136:139]\n"
"s_lshr_b32 s71, s71, 7\n"
"v_mfma_f32_16x16x32_bf16 v[136:139], a[164:167], a[184:187], v[136:139]\n"
"ds_read_b64_tr_b16 a[120:121], v220 offset:21504\n"
"s_mul_i32 s81, s71, 3\n"
"v_mfma_f32_16x16x32_bf16 v[140:143], a[136:139], v[40:43], v[140:143]\n"
"ds_read_b64_tr_b16 a[122:123], v220 offset:26624\n"
"v_cvt_pk_bf16_f32 v107, v118, v119\n"
"v_mfma_f32_16x16x32_bf16 v[140:143], a[140:143], v[68:71], v[140:143]\n"
"s_sub_u32 s81, s70, s81\n"
"ds_read_b64_tr_b16 a[124:125], v220 offset:31744\n"
"v_mfma_f32_16x16x32_bf16 v[140:143], a[144:147], a[0:3], v[140:143]\n"
"ds_read_b64_tr_b16 a[126:127], v220 offset:36864\n"
"buffer_store_dwordx4 v[104:107], v246, s[48:51], s69 offen offset:2048 sc1\n"
"v_mfma_f32_16x16x32_bf16 v[140:143], a[148:151], a[12:15], v[140:143]\n"
"s_mul_i32 s81, s81, 40960\n"
"s_lshl_b32 s71, s71, 6\n"
"v_mfma_f32_16x16x32_bf16 v[140:143], a[152:155], a[24:27], v[140:143]\n"
"v_mfma_f32_16x16x32_bf16 v[140:143], a[156:159], a[36:39], v[140:143]\n"
"ds_write_b128 v239, v[104:107] offset:2048\n"
"s_add_u32 s68, s81, s71\n"
"v_mfma_f32_16x16x32_bf16 v[140:143], a[160:163], a[176:179], v[140:143]\n"
"s_cmp_eq_u32 s70, 15\n"
"s_cselect_b32 s68, 0x40000000, s68\n"
"v_mfma_f32_16x16x32_bf16 v[140:143], a[164:167], a[188:191], v[140:143]\n"
"s_waitcnt lgkmcnt(0)\n"
"v_mfma_f32_16x16x32_bf16 v[144:147], a[96:99], v[32:35], v[144:147]\n"
"ds_read_b64_tr_b16 a[136:137], v219 offset:21504\n"
"v_mfma_f32_16x16x32_bf16 v[144:147], a[100:103], v[44:47], v[144:147]\n"
"ds_read_b64_tr_b16 a[138:139], v219 offset:26624\n"
"v_mfma_f32_16x16x32_bf16 v[144:147], a[104:107], v[72:75], v[144:147]\n"
"ds_read_b64_tr_b16 a[140:141], v219 offset:31744\n"
"v_mfma_f32_16x16x32_bf16 v[144:147], a[108:111], a[4:7], v[144:147]\n"
"ds_read_b64_tr_b16 a[142:143], v219 offset:36864\n"
"v_cvt_pk_bf16_f32 v120, v120, v121\n"
"v_mfma_f32_16x16x32_bf16 v[144:147], a[112:115], a[16:19], v[144:147]\n"
"v_mfma_f32_16x16x32_bf16 v[144:147], a[116:119], a[28:31], v[144:147]\n"
"ds_read_b64_tr_b16 a[144:145], v219 offset:41984\n"
"v_cvt_pk_bf16_f32 v121, v122, v123\n"
"v_mfma_f32_16x16x32_bf16 v[144:147], a[120:123], a[40:43], v[144:147]\n"
"ds_read_b64_tr_b16 a[146:147], v219 offset:47104\n"
"v_mfma_f32_16x16x32_bf16 v[144:147], a[124:127], a[180:183], v[144:147]\n"
"ds_read_b64_tr_b16 a[148:149], v219 offset:52224\n"
"v_cvt_pk_bf16_f32 v122, v132, v133\n"
"v_mfma_f32_16x16x32_bf16 v[148:151], a[96:99], v[36:39], v[148:151]\n"
"ds_read_b64_tr_b16 a[150:151], v219 offset:57344\n"
"v_mfma_f32_16x16x32_bf16 v[148:151], a[100:103], v[64:67], v[148:151]\n"
"v_mfma_f32_16x16x32_bf16 v[148:151], a[104:107], v[76:79], v[148:151]\n"
"ds_read_b64_tr_b16 a[152:153], v221 offset:1024\n"
"v_cvt_pk_bf16_f32 v123, v134, v135\n"
"v_mfma_f32_16x16x32_bf16 v[148:151], a[108:111], a[8:11], v[148:151]\n"
"ds_read_b64_tr_b16 a[154:155], v221 offset:6144\n"
"v_mfma_f32_16x16x32_bf16 v[148:151], a[112:115], a[20:23], v[148:151]\n"
"ds_read_b64_tr_b16 a[156:157], v221 offset:11264\n"
"buffer_store_dwordx4 v[120:123], v246, s[48:51], s69 offen offset:3072 sc1\n"
"v_mfma_f32_16x16x32_bf16 v[148:151], a[116:119], a[32:35], v[148:151]\n"
"ds_read_b64_tr_b16 a[158:159], v221 offset:16384\n"
"v_mfma_f32_16x16x32_bf16 v[148:151], a[120:123], a[44:47], v[148:151]\n"
"ds_write_b128 v239, v[120:123] offset:3072\n"
"v_mfma_f32_16x16x32_bf16 v[148:151], a[124:127], a[184:187], v[148:151]\n"
"ds_read_b64_tr_b16 a[160:161], v221 offset:21504\n"
"v_mfma_f32_16x16x32_bf16 v[152:155], a[96:99], v[40:43], v[152:155]\n"
"ds_read_b64_tr_b16 a[162:163], v221 offset:26624\n"
"v_mfma_f32_16x16x32_bf16 v[152:155], a[100:103], v[68:71], v[152:155]\n"
"ds_read_b64_tr_b16 a[164:165], v221 offset:31744\n"
"v_cvt_pk_bf16_f32 v124, v124, v125\n"
"v_mfma_f32_16x16x32_bf16 v[152:155], a[104:107], a[0:3], v[152:155]\n"
"ds_read_b64_tr_b16 a[166:167], v221 offset:36864\n"
"v_mfma_f32_16x16x32_bf16 v[152:155], a[108:111], a[12:15], v[152:155]\n"
"v_cvt_pk_bf16_f32 v125, v126, v127\n"
"v_mfma_f32_16x16x32_bf16 v[152:155], a[112:115], a[24:27], v[152:155]\n"
"v_mfma_f32_16x16x32_bf16 v[152:155], a[116:119], a[36:39], v[152:155]\n"
"v_cvt_pk_bf16_f32 v126, v136, v137\n"
"v_mfma_f32_16x16x32_bf16 v[152:155], a[120:123], a[176:179], v[152:155]\n"
"v_mfma_f32_16x16x32_bf16 v[152:155], a[124:127], a[188:191], v[152:155]\n"
"s_waitcnt lgkmcnt(0)\n"
"v_mfma_f32_16x16x32_bf16 v[156:159], a[136:139], v[32:35], v[156:159]\n"
"ds_read_b64_tr_b16 a[96:97], v218 offset:22016\n"
"v_cvt_pk_bf16_f32 v127, v138, v139\n"
"v_mfma_f32_16x16x32_bf16 v[156:159], a[140:143], v[44:47], v[156:159]\n"
"ds_read_b64_tr_b16 a[98:99], v218 offset:27136\n"
"v_mfma_f32_16x16x32_bf16 v[156:159], a[144:147], v[72:75], v[156:159]\n"
"ds_read_b64_tr_b16 a[100:101], v218 offset:32256\n"
"buffer_store_dwordx4 v[124:127], v246, s[48:51], s77 offen sc1\n"
"v_mfma_f32_16x16x32_bf16 v[156:159], a[148:151], a[4:7], v[156:159]\n"
"ds_read_b64_tr_b16 a[102:103], v218 offset:37376\n"
"v_mfma_f32_16x16x32_bf16 v[156:159], a[152:155], a[16:19], v[156:159]\n"
"v_mfma_f32_16x16x32_bf16 v[156:159], a[156:159], a[28:31], v[156:159]\n"
"ds_read_b64_tr_b16 a[104:105], v218 offset:42496\n"
"ds_write_b128 v239, v[124:127] offset:4096\n"
"v_mfma_f32_16x16x32_bf16 v[156:159], a[160:163], a[40:43], v[156:159]\n"
"ds_read_b64_tr_b16 a[106:107], v218 offset:47616\n"
"v_mfma_f32_16x16x32_bf16 v[156:159], a[164:167], a[180:183], v[156:159]\n"
"ds_read_b64_tr_b16 a[108:109], v218 offset:52736\n"
"v_mfma_f32_16x16x32_bf16 v[160:163], a[136:139], v[36:39], v[160:163]\n"
"ds_read_b64_tr_b16 a[110:111], v218 offset:57856\n"
"v_cvt_pk_bf16_f32 v128, v128, v129\n"
"v_mfma_f32_16x16x32_bf16 v[160:163], a[140:143], v[64:67], v[160:163]\n"
"v_mfma_f32_16x16x32_bf16 v[160:163], a[144:147], v[76:79], v[160:163]\n"
"ds_read_b64_tr_b16 a[112:113], v220 offset:1536\n"
"v_cvt_pk_bf16_f32 v129, v130, v131\n"
"v_mfma_f32_16x16x32_bf16 v[160:163], a[148:151], a[8:11], v[160:163]\n"
"ds_read_b64_tr_b16 a[114:115], v220 offset:6656\n"
"v_mfma_f32_16x16x32_bf16 v[160:163], a[152:155], a[20:23], v[160:163]\n"
"ds_read_b64_tr_b16 a[116:117], v220 offset:11776\n"
"v_mfma_f32_16x16x32_bf16 v[160:163], a[156:159], a[32:35], v[160:163]\n"
"ds_read_b64_tr_b16 a[118:119], v220 offset:16896\n"
"v_cvt_pk_bf16_f32 v130, v140, v141\n"
"v_mfma_f32_16x16x32_bf16 v[160:163], a[160:163], a[44:47], v[160:163]\n"
"v_mfma_f32_16x16x32_bf16 v[160:163], a[164:167], a[184:187], v[160:163]\n"
"ds_read_b64_tr_b16 a[120:121], v220 offset:22016\n"
"v_mfma_f32_16x16x32_bf16 v[164:167], a[136:139], v[40:43], v[164:167]\n"
"ds_read_b64_tr_b16 a[122:123], v220 offset:27136\n"
"v_cvt_pk_bf16_f32 v131, v142, v143\n"
"v_mfma_f32_16x16x32_bf16 v[164:167], a[140:143], v[68:71], v[164:167]\n"
"ds_read_b64_tr_b16 a[124:125], v220 offset:32256\n"
"v_mfma_f32_16x16x32_bf16 v[164:167], a[144:147], a[0:3], v[164:167]\n"
"ds_read_b64_tr_b16 a[126:127], v220 offset:37376\n"
"buffer_store_dwordx4 v[128:131], v246, s[48:51], s77 offen offset:1024 sc1\n"
"v_mfma_f32_16x16x32_bf16 v[164:167], a[148:151], a[12:15], v[164:167]\n"
"v_mfma_f32_16x16x32_bf16 v[164:167], a[152:155], a[24:27], v[164:167]\n"
"v_mfma_f32_16x16x32_bf16 v[164:167], a[156:159], a[36:39], v[164:167]\n"
"ds_write_b128 v239, v[128:131] offset:5120\n"
"v_mfma_f32_16x16x32_bf16 v[164:167], a[160:163], a[176:179], v[164:167]\n"
"v_mfma_f32_16x16x32_bf16 v[164:167], a[164:167], a[188:191], v[164:167]\n"
"s_waitcnt lgkmcnt(0)\n"
"v_mfma_f32_16x16x32_bf16 v[168:171], a[96:99], v[32:35], v[168:171]\n"
"ds_read_b64_tr_b16 a[136:137], v219 offset:22016\n"
"v_mfma_f32_16x16x32_bf16 v[168:171], a[100:103], v[44:47], v[168:171]\n"
"ds_read_b64_tr_b16 a[138:139], v219 offset:27136\n"
"v_mfma_f32_16x16x32_bf16 v[168:171], a[104:107], v[72:75], v[168:171]\n"
"ds_read_b64_tr_b16 a[140:141], v219 offset:32256\n"
"v_mfma_f32_16x16x32_bf16 v[168:171], a[108:111], a[4:7], v[168:171]\n"
"ds_read_b64_tr_b16 a[142:143], v219 offset:37376\n"
"v_cvt_pk_bf16_f32 v144, v144, v145\n"
"v_mfma_f32_16x16x32_bf16 v[168:171], a[112:115], a[16:19], v[168:171]\n"
"v_mfma_f32_16x16x32_bf16 v[168:171], a[116:119], a[28:31], v[168:171]\n"
"ds_read_b64_tr_b16 a[144:145], v219 offset:42496\n"
"v_cvt_pk_bf16_f32 v145, v146, v147\n"
"v_mfma_f32_16x16x32_bf16 v[168:171], a[120:123], a[40:43], v[168:171]\n"
"ds_read_b64_tr_b16 a[146:147], v219 offset:47616\n"
"v_mfma_f32_16x16x32_bf16 v[168:171], a[124:127], a[180:183], v[168:171]\n"
"ds_read_b64_tr_b16 a[148:149], v219 offset:52736\n"
"v_cvt_pk_bf16_f32 v146, v156, v157\n"
"v_mfma_f32_16x16x32_bf16 v[172:175], a[96:99], v[36:39], v[172:175]\n"
"ds_read_b64_tr_b16 a[150:151], v219 offset:57856\n"
"v_mfma_f32_16x16x32_bf16 v[172:175], a[100:103], v[64:67], v[172:175]\n"
"v_mfma_f32_16x16x32_bf16 v[172:175], a[104:107], v[76:79], v[172:175]\n"
"ds_read_b64_tr_b16 a[152:153], v221 offset:1536\n"
"v_cvt_pk_bf16_f32 v147, v158, v159\n"
"v_mfma_f32_16x16x32_bf16 v[172:175], a[108:111], a[8:11], v[172:175]\n"
"ds_read_b64_tr_b16 a[154:155], v221 offset:6656\n"
"v_mfma_f32_16x16x32_bf16 v[172:175], a[112:115], a[20:23], v[172:175]\n"
"ds_read_b64_tr_b16 a[156:157], v221 offset:11776\n"
"buffer_store_dwordx4 v[144:147], v246, s[48:51], s77 offen offset:2048 sc1\n"
"v_mfma_f32_16x16x32_bf16 v[172:175], a[116:119], a[32:35], v[172:175]\n"
"ds_read_b64_tr_b16 a[158:159], v221 offset:16896\n"
"v_mfma_f32_16x16x32_bf16 v[172:175], a[120:123], a[44:47], v[172:175]\n"
"ds_write_b128 v239, v[144:147] offset:6144\n"
"v_mfma_f32_16x16x32_bf16 v[172:175], a[124:127], a[184:187], v[172:175]\n"
"ds_read_b64_tr_b16 a[160:161], v221 offset:22016\n"
"v_mfma_f32_16x16x32_bf16 v[176:179], a[96:99], v[40:43], v[176:179]\n"
"ds_read_b64_tr_b16 a[162:163], v221 offset:27136\n"
"v_mfma_f32_16x16x32_bf16 v[176:179], a[100:103], v[68:71], v[176:179]\n"
"ds_read_b64_tr_b16 a[164:165], v221 offset:32256\n"
"v_cvt_pk_bf16_f32 v148, v148, v149\n"
"v_mfma_f32_16x16x32_bf16 v[176:179], a[104:107], a[0:3], v[176:179]\n"
"ds_read_b64_tr_b16 a[166:167], v221 offset:37376\n"
"v_mfma_f32_16x16x32_bf16 v[176:179], a[108:111], a[12:15], v[176:179]\n"
"v_cvt_pk_bf16_f32 v149, v150, v151\n"
"v_mfma_f32_16x16x32_bf16 v[176:179], a[112:115], a[24:27], v[176:179]\n"
"v_mfma_f32_16x16x32_bf16 v[176:179], a[116:119], a[36:39], v[176:179]\n"
"v_cvt_pk_bf16_f32 v150, v160, v161\n"
"v_mfma_f32_16x16x32_bf16 v[176:179], a[120:123], a[176:179], v[176:179]\n"
"v_mfma_f32_16x16x32_bf16 v[176:179], a[124:127], a[188:191], v[176:179]\n"
"s_waitcnt lgkmcnt(0)\n"
"v_mfma_f32_16x16x32_bf16 v[180:183], a[136:139], v[32:35], v[180:183]\n"
"ds_read_b64_tr_b16 a[96:97], v218 offset:22528\n"
"v_cvt_pk_bf16_f32 v151, v162, v163\n"
"v_mfma_f32_16x16x32_bf16 v[180:183], a[140:143], v[44:47], v[180:183]\n"
"ds_read_b64_tr_b16 a[98:99], v218 offset:27648\n"
"v_mfma_f32_16x16x32_bf16 v[180:183], a[144:147], v[72:75], v[180:183]\n"
"ds_read_b64_tr_b16 a[100:101], v218 offset:32768\n"
"buffer_store_dwordx4 v[148:151], v246, s[48:51], s77 offen offset:3072 sc1\n"
"v_mfma_f32_16x16x32_bf16 v[180:183], a[148:151], a[4:7], v[180:183]\n"
"ds_read_b64_tr_b16 a[102:103], v218 offset:37888\n"
"v_mfma_f32_16x16x32_bf16 v[180:183], a[152:155], a[16:19], v[180:183]\n"
"v_mfma_f32_16x16x32_bf16 v[180:183], a[156:159], a[28:31], v[180:183]\n"
"ds_read_b64_tr_b16 a[104:105], v218 offset:43008\n"
"ds_write_b128 v239, v[148:151] offset:7168\n"
"v_mfma_f32_16x16x32_bf16 v[180:183], a[160:163], a[40:43], v[180:183]\n"
"ds_read_b64_tr_b16 a[106:107], v218 offset:48128\n"
"v_mfma_f32_16x16x32_bf16 v[180:183], a[164:167], a[180:183], v[180:183]\n"
"ds_read_b64_tr_b16 a[108:109], v218 offset:53248\n"
"v_mfma_f32_16x16x32_bf16 v[184:187], a[136:139], v[36:39], v[184:187]\n"
"ds_read_b64_tr_b16 a[110:111], v218 offset:58368\n"
"v_cvt_pk_bf16_f32 v152, v152, v153\n"
"v_mfma_f32_16x16x32_bf16 v[184:187], a[140:143], v[64:67], v[184:187]\n"
"v_mfma_f32_16x16x32_bf16 v[184:187], a[144:147], v[76:79], v[184:187]\n"
"ds_read_b64_tr_b16 a[112:113], v220 offset:2048\n"
"v_cvt_pk_bf16_f32 v153, v154, v155\n"
"v_mfma_f32_16x16x32_bf16 v[184:187], a[148:151], a[8:11], v[184:187]\n"
"ds_read_b64_tr_b16 a[114:115], v220 offset:7168\n"
"v_mfma_f32_16x16x32_bf16 v[184:187], a[152:155], a[20:23], v[184:187]\n"
"ds_read_b64_tr_b16 a[116:117], v220 offset:12288\n"
"v_mfma_f32_16x16x32_bf16 v[184:187], a[156:159], a[32:35], v[184:187]\n"
"ds_read_b64_tr_b16 a[118:119], v220 offset:17408\n"
"v_cvt_pk_bf16_f32 v154, v164, v165\n"
"v_mfma_f32_16x16x32_bf16 v[184:187], a[160:163], a[44:47], v[184:187]\n"
"v_mfma_f32_16x16x32_bf16 v[184:187], a[164:167], a[184:187], v[184:187]\n"
"ds_read_b64_tr_b16 a[120:121], v220 offset:22528\n"
"v_mfma_f32_16x16x32_bf16 v[188:191], a[136:139], v[40:43], v[188:191]\n"
"ds_read_b64_tr_b16 a[122:123], v220 offset:27648\n"
"v_cvt_pk_bf16_f32 v155, v166, v167\n"
"v_mfma_f32_16x16x32_bf16 v[188:191], a[140:143], v[68:71], v[188:191]\n"
"ds_read_b64_tr_b16 a[124:125], v220 offset:32768\n"
"v_mfma_f32_16x16x32_bf16 v[188:191], a[144:147], a[0:3], v[188:191]\n"
"ds_read_b64_tr_b16 a[126:127], v220 offset:37888\n"
"buffer_store_dwordx4 v[152:155], v246, s[48:51], s79 offen sc1\n"
"v_mfma_f32_16x16x32_bf16 v[188:191], a[148:151], a[12:15], v[188:191]\n"
"v_mfma_f32_16x16x32_bf16 v[188:191], a[152:155], a[24:27], v[188:191]\n"
"v_mfma_f32_16x16x32_bf16 v[188:191], a[156:159], a[36:39], v[188:191]\n"
"ds_write_b128 v239, v[152:155] offset:8192\n"
"v_mfma_f32_16x16x32_bf16 v[188:191], a[160:163], a[176:179], v[188:191]\n"
"v_mfma_f32_16x16x32_bf16 v[188:191], a[164:167], a[188:191], v[188:191]\n"
"s_waitcnt lgkmcnt(0)\n"
"v_mfma_f32_16x16x32_bf16 v[192:195], a[96:99], v[32:35], v[192:195]\n"
"ds_read_b64_tr_b16 a[136:137], v219 offset:22528\n"
"v_mfma_f32_16x16x32_bf16 v[192:195], a[100:103], v[44:47], v[192:195]\n"
"ds_read_b64_tr_b16 a[138:139], v219 offset:27648\n"
"v_mfma_f32_16x16x32_bf16 v[192:195], a[104:107], v[72:75], v[192:195]\n"
"ds_read_b64_tr_b16 a[140:141], v219 offset:32768\n"
"v_mfma_f32_16x16x32_bf16 v[192:195], a[108:111], a[4:7], v[192:195]\n"
"ds_read_b64_tr_b16 a[142:143], v219 offset:37888\n"
"v_cvt_pk_bf16_f32 v168, v168, v169\n"
"v_mfma_f32_16x16x32_bf16 v[192:195], a[112:115], a[16:19], v[192:195]\n"
"v_cvt_pk_bf16_f32 v169, v170, v171\n"
"v_mfma_f32_16x16x32_bf16 v[192:195], a[116:119], a[28:31], v[192:195]\n"
"ds_read_b64_tr_b16 a[144:145], v219 offset:43008\n"
"v_mfma_f32_16x16x32_bf16 v[192:195], a[120:123], a[40:43], v[192:195]\n"
"ds_read_b64_tr_b16 a[146:147], v219 offset:48128\n"
"v_cvt_pk_bf16_f32 v170, v180, v181\n"
"v_mfma_f32_16x16x32_bf16 v[192:195], a[124:127], a[180:183], v[192:195]\n"
"ds_read_b64_tr_b16 a[148:149], v219 offset:53248\n"
"v_mfma_f32_16x16x32_bf16 v[196:199], a[96:99], v[36:39], v[196:199]\n"
"ds_read_b64_tr_b16 a[150:151], v219 offset:58368\n"
"v_cvt_pk_bf16_f32 v171, v182, v183\n"
"v_mfma_f32_16x16x32_bf16 v[196:199], a[100:103], v[64:67], v[196:199]\n"
"v_mfma_f32_16x16x32_bf16 v[196:199], a[104:107], v[76:79], v[196:199]\n"
"ds_read_b64_tr_b16 a[152:153], v221 offset:2048\n"
"buffer_store_dwordx4 v[168:171], v246, s[48:51], s79 offen offset:1024 sc1\n"
"v_mfma_f32_16x16x32_bf16 v[196:199], a[108:111], a[8:11], v[196:199]\n"
"ds_read_b64_tr_b16 a[154:155], v221 offset:7168\n"
"ds_write_b128 v239, v[168:171] offset:9216\n"
"v_mfma_f32_16x16x32_bf16 v[196:199], a[112:115], a[20:23], v[196:199]\n"
"ds_read_b64_tr_b16 a[156:157], v221 offset:12288\n"
"v_mfma_f32_16x16x32_bf16 v[196:199], a[116:119], a[32:35], v[196:199]\n"
"ds_read_b64_tr_b16 a[158:159], v221 offset:17408\n"
"v_cvt_pk_bf16_f32 v172, v172, v173\n"
"v_mfma_f32_16x16x32_bf16 v[196:199], a[120:123], a[44:47], v[196:199]\n"
"v_mfma_f32_16x16x32_bf16 v[196:199], a[124:127], a[184:187], v[196:199]\n"
"ds_read_b64_tr_b16 a[160:161], v221 offset:22528\n"
"v_cvt_pk_bf16_f32 v173, v174, v175\n"
"v_mfma_f32_16x16x32_bf16 v[200:203], a[96:99], v[40:43], v[200:203]\n"
"ds_read_b64_tr_b16 a[162:163], v221 offset:27648\n"
"v_mfma_f32_16x16x32_bf16 v[200:203], a[100:103], v[68:71], v[200:203]\n"
"ds_read_b64_tr_b16 a[164:165], v221 offset:32768\n"
"v_cvt_pk_bf16_f32 v174, v184, v185\n"
"v_mfma_f32_16x16x32_bf16 v[200:203], a[104:107], a[0:3], v[200:203]\n"
"ds_read_b64_tr_b16 a[166:167], v221 offset:37888\n"
"v_cvt_pk_bf16_f32 v175, v186, v187\n"
"v_mfma_f32_16x16x32_bf16 v[200:203], a[108:111], a[12:15], v[200:203]\n"
"v_mfma_f32_16x16x32_bf16 v[200:203], a[112:115], a[24:27], v[200:203]\n"
"buffer_store_dwordx4 v[172:175], v246, s[48:51], s79 offen offset:2048 sc1\n"
"v_mfma_f32_16x16x32_bf16 v[200:203], a[116:119], a[36:39], v[200:203]\n"
"v_mfma_f32_16x16x32_bf16 v[200:203], a[120:123], a[176:179], v[200:203]\n"
"ds_write_b128 v239, v[172:175] offset:10240\n"
"v_mfma_f32_16x16x32_bf16 v[200:203], a[124:127], a[188:191], v[200:203]\n"
"s_waitcnt lgkmcnt(0)\n"
"v_mfma_f32_16x16x32_bf16 v[204:207], a[136:139], v[32:35], v[204:207]\n"
"v_cvt_pk_bf16_f32 v176, v176, v177\n"
"v_mfma_f32_16x16x32_bf16 v[204:207], a[140:143], v[44:47], v[204:207]\n"
"v_mfma_f32_16x16x32_bf16 v[204:207], a[144:147], v[72:75], v[204:207]\n"
"v_mfma_f32_16x16x32_bf16 v[204:207], a[148:151], a[4:7], v[204:207]\n"
"v_mfma_f32_16x16x32_bf16 v[204:207], a[152:155], a[16:19], v[204:207]\n"
"v_cvt_pk_bf16_f32 v177, v178, v179\n"
"v_mfma_f32_16x16x32_bf16 v[204:207], a[156:159], a[28:31], v[204:207]\n"
"v_mfma_f32_16x16x32_bf16 v[204:207], a[160:163], a[40:43], v[204:207]\n"
"v_mfma_f32_16x16x32_bf16 v[204:207], a[164:167], a[180:183], v[204:207]\n"
"v_mfma_f32_16x16x32_bf16 v[208:211], a[136:139], v[36:39], v[208:211]\n"
"v_cvt_pk_bf16_f32 v178, v188, v189\n"
"v_mfma_f32_16x16x32_bf16 v[208:211], a[140:143], v[64:67], v[208:211]\n"
"v_mfma_f32_16x16x32_bf16 v[208:211], a[144:147], v[76:79], v[208:211]\n"
"v_mfma_f32_16x16x32_bf16 v[208:211], a[148:151], a[8:11], v[208:211]\n"
"v_mfma_f32_16x16x32_bf16 v[208:211], a[152:155], a[20:23], v[208:211]\n"
"v_cvt_pk_bf16_f32 v179, v190, v191\n"
"v_mfma_f32_16x16x32_bf16 v[208:211], a[156:159], a[32:35], v[208:211]\n"
"v_mfma_f32_16x16x32_bf16 v[208:211], a[160:163], a[44:47], v[208:211]\n"
"v_mfma_f32_16x16x32_bf16 v[208:211], a[164:167], a[184:187], v[208:211]\n"
"v_mfma_f32_16x16x32_bf16 v[212:215], a[136:139], v[40:43], v[212:215]\n"
"buffer_store_dwordx4 v[176:179], v246, s[48:51], s79 offen offset:3072 sc1\n"
"v_mfma_f32_16x16x32_bf16 v[212:215], a[140:143], v[68:71], v[212:215]\n"
"v_mfma_f32_16x16x32_bf16 v[212:215], a[144:147], a[0:3], v[212:215]\n"
"v_mfma_f32_16x16x32_bf16 v[212:215], a[148:151], a[12:15], v[212:215]\n"
"v_mfma_f32_16x16x32_bf16 v[212:215], a[152:155], a[24:27], v[212:215]\n"
"ds_write_b128 v239, v[176:179] offset:11264\n"
"v_mfma_f32_16x16x32_bf16 v[212:215], a[156:159], a[36:39], v[212:215]\n"
"v_mfma_f32_16x16x32_bf16 v[212:215], a[160:163], a[176:179], v[212:215]\n"
"v_mfma_f32_16x16x32_bf16 v[212:215], a[164:167], a[188:191], v[212:215]\n"
"v_cvt_pk_bf16_f32 v192, v192, v193\n"
"v_cvt_pk_bf16_f32 v193, v194, v195\n"
"v_cvt_pk_bf16_f32 v194, v204, v205\n"
"v_cvt_pk_bf16_f32 v195, v206, v207\n"
"buffer_store_dwordx4 v[192:195], v246, s[48:51], s97 offen sc1\n"
"ds_write_b128 v239, v[192:195] offset:12288\n"
"v_cvt_pk_bf16_f32 v196, v196, v197\n"
"v_cvt_pk_bf16_f32 v197, v198, v199\n"
"v_cvt_pk_bf16_f32 v198, v208, v209\n"
"v_cvt_pk_bf16_f32 v199, v210, v211\n"
"buffer_store_dwordx4 v[196:199], v246, s[48:51], s97 offen offset:1024 sc1\n"
"ds_write_b128 v239, v[196:199] offset:13312\n"
"v_cvt_pk_bf16_f32 v200, v200, v201\n"
"v_cvt_pk_bf16_f32 v201, v202, v203\n"
"v_cvt_pk_bf16_f32 v202, v212, v213\n"
"v_cvt_pk_bf16_f32 v203, v214, v215\n"
"buffer_store_dwordx4 v[200:203], v246, s[48:51], s97 offen offset:2048 sc1\n"
"ds_write_b128 v239, v[200:203] offset:14336\n"
"s_lshl_b32 s70, s80, 10\n"
"v_add_u32_e32 v239, s70, v239\n"
"s_waitcnt lgkmcnt(0)\n"
"ds_read_b128 v[108:111], v239 offset:0\n"
"ds_read_b128 v[124:127], v239 offset:1024\n"
"ds_read_b128 v[140:143], v239 offset:2048\n"
"ds_read_b128 v[156:159], v239 offset:3072\n"
"s_and_b32 s70, %15, 3\n"
"s_add_u32 s71, s70, 1\n"
"s_and_b32 s71, s71, 3\n"
"s_mul_i32 s71, s71, 61440\n"
"s_add_u32 s69, s73, s71\n"
"s_add_u32 s71, s70, 2\n"
"s_and_b32 s71, s71, 3\n"
"s_mul_i32 s71, s71, 61440\n"
"s_add_u32 s77, s73, s71\n"
"s_add_u32 s71, s70, 3\n"
"s_and_b32 s71, s71, 3\n"
"s_mul_i32 s71, s71, 61440\n"
"s_add_u32 s79, s73, s71\n"
"s_waitcnt lgkmcnt(3)\n"
"v_dot2_f32_bf16 v0, v108, s74, 0\n"
"v_dot2_f32_bf16 v1, v108, s75, 0\n"
"v_dot2_f32_bf16 v2, v109, s74, 0\n"
"v_dot2_f32_bf16 v3, v109, s75, 0\n"
"v_dot2_f32_bf16 v4, v110, s74, 0\n"
"v_dot2_f32_bf16 v5, v110, s75, 0\n"
"v_dot2_f32_bf16 v6, v111, s74, 0\n"
"v_dot2_f32_bf16 v7, v111, s75, 0\n"
"s_waitcnt lgkmcnt(2)\n"
"v_dot2_f32_bf16 v8, v124, s74, 0\n"
"v_dot2_f32_bf16 v9, v124, s75, 0\n"
"v_dot2_f32_bf16 v10, v125, s74, 0\n"
"v_dot2_f32_bf16 v11, v125, s75, 0\n"
"v_dot2_f32_bf16 v12, v126, s74, 0\n"
"v_dot2_f32_bf16 v13, v126, s75, 0\n"
"v_dot2_f32_bf16 v14, v127, s74, 0\n"
"v_dot2_f32_bf16 v15, v127, s75, 0\n"
"s_waitcnt lgkmcnt(1)\n"
"v_dot2_f32_bf16 v16, v140, s74, 0\n"
"v_dot2_f32_bf16 v17, v140, s75, 0\n"
"v_dot2_f32_bf16 v18, v141, s74, 0\n"
"v_dot2_f32_bf16 v19, v141, s75, 0\n"
"v_dot2_f32_bf16 v20, v142, s74, 0\n"
"v_dot2_f32_bf16 v21, v142, s75, 0\n"
"v_dot2_f32_bf16 v22, v143, s74, 0\n"
"v_dot2_f32_bf16 v23, v143, s75, 0\n"
"s_waitcnt lgkmcnt(0)\n"
"v_dot2_f32_bf16 v24, v156, s74, 0\n"
"v_dot2_f32_bf16 v25, v156, s75, 0\n"
"v_dot2_f32_bf16 v26, v157, s74, 0\n"
"v_dot2_f32_bf16 v27, v157, s75, 0\n"
"v_dot2_f32_bf16 v28, v158, s74, 0\n"
"v_dot2_f32_bf16 v29, v158, s75, 0\n"
"v_dot2_f32_bf16 v30, v159, s74, 0\n"
"v_dot2_f32_bf16 v31, v159, s75, 0\n"
"s_waitcnt vmcnt(0)\n"
"s_barrier\n"
"buffer_store_dwordx2 v[228:229], v226, s[36:39], 0 offen sc1\n"
"s_mov_b32 s63, 0x40000\n"
"L_p2%=:\n"
"buffer_load_dwordx2 v[230:231], v237, s[36:39], s95 offen sc1\n"
"s_waitcnt vmcnt(0)\n"
"v_cmp_eq_u32_e64 s[98:99], s62, v230\n"
"v_cmp_eq_u32_e32 vcc, s65, v231\n"
"s_nop 0\n"
"s_and_b64 s[98:99], s[98:99], vcc\n"
"s_cmp_eq_u64 s[98:99], -1\n"
"s_cbranch_scc1 L_p2d%=\n"
"s_sleep 1\n"
"s_sub_u32 s63, s63, 1\n"
"s_cmp_lg_u32 s63, 0\n"
"s_cbranch_scc1 L_p2%=\n"
"L_p2d%=:\n"
"buffer_load_dwordx4 v[96:99], v246, s[48:51], s69 offen sc1\n"
"buffer_load_dwordx4 v[112:115], v246, s[48:51], s69 offen offset:1024 sc1\n"
"buffer_load_dwordx4 v[128:131], v246, s[48:51], s69 offen offset:2048 sc1\n"
"buffer_load_dwordx4 v[144:147], v246, s[48:51], s69 offen offset:3072 sc1\n"
"buffer_load_dwordx4 v[100:103], v246, s[48:51], s77 offen sc1\n"
"buffer_load_dwordx4 v[116:119], v246, s[48:51], s77 offen offset:1024 sc1\n"
"buffer_load_dwordx4 v[132:135], v246, s[48:51], s77 offen offset:2048 sc1\n"
"buffer_load_dwordx4 v[148:151], v246, s[48:51], s77 offen offset:3072 sc1\n"
"buffer_load_dwordx4 v[104:107], v246, s[48:51], s79 offen sc1\n"
"buffer_load_dwordx4 v[120:123], v246, s[48:51], s79 offen offset:1024 sc1\n"
"buffer_load_dwordx4 v[136:139], v246, s[48:51], s79 offen offset:2048 sc1\n"
"buffer_load_dwordx4 v[152:155], v246, s[48:51], s79 offen offset:3072 sc1\n"
"s_waitcnt vmcnt(8)\n"
"v_dot2_f32_bf16 v0, v96, s74, v0\n"
"v_dot2_f32_bf16 v1, v96, s75, v1\n"
"v_dot2_f32_bf16 v2, v97, s74, v2\n"
"v_dot2_f32_bf16 v3, v97, s75, v3\n"
"v_dot2_f32_bf16 v4, v98, s74, v4\n"
"v_dot2_f32_bf16 v5, v98, s75, v5\n"
"v_dot2_f32_bf16 v6, v99, s74, v6\n"
"v_dot2_f32_bf16 v7, v99, s75, v7\n"
"v_dot2_f32_bf16 v8, v112, s74, v8\n"
"v_dot2_f32_bf16 v9, v112, s75, v9\n"
"v_dot2_f32_bf16 v10, v113, s74, v10\n"
"v_dot2_f32_bf16 v11, v113, s75, v11\n"
"v_dot2_f32_bf16 v12, v114, s74, v12\n"
"v_dot2_f32_bf16 v13, v114, s75, v13\n"
"v_dot2_f32_bf16 v14, v115, s74, v14\n"
"v_dot2_f32_bf16 v15, v115, s75, v15\n"
"v_dot2_f32_bf16 v16, v128, s74, v16\n"
"v_dot2_f32_bf16 v17, v128, s75, v17\n"
"v_dot2_f32_bf16 v18, v129, s74, v18\n"
"v_dot2_f32_bf16 v19, v129, s75, v19\n"
"v_dot2_f32_bf16 v20, v130, s74, v20\n"
"v_dot2_f32_bf16 v21, v130, s75, v21\n"
"v_dot2_f32_bf16 v22, v131, s74, v22\n"
"v_dot2_f32_bf16 v23, v131, s75, v23\n"
"v_dot2_f32_bf16 v24, v144, s74, v24\n"
"v_dot2_f32_bf16 v25, v144, s75, v25\n"
"v_dot2_f32_bf16 v26, v145, s74, v26\n"
"v_dot2_f32_bf16 v27, v145, s75, v27\n"
"v_dot2_f32_bf16 v28, v146, s74, v28\n"
"v_dot2_f32_bf16 v29, v146, s75, v29\n"
"v_dot2_f32_bf16 v30, v147, s74, v30\n"
"v_dot2_f32_bf16 v31, v147, s75, v31\n"
"s_waitcnt vmcnt(4)\n"
"v_dot2_f32_bf16 v0, v100, s74, v0\n"
"v_dot2_f32_bf16 v1, v100, s75, v1\n"
"v_dot2_f32_bf16 v2, v101, s74, v2\n"
"v_dot2_f32_bf16 v3, v101, s75, v3\n"
"v_dot2_f32_bf16 v4, v102, s74, v4\n"
"v_dot2_f32_bf16 v5, v102, s75, v5\n"
"v_dot2_f32_bf16 v6, v103, s74, v6\n"
"v_dot2_f32_bf16 v7, v103, s75, v7\n"
"v_dot2_f32_bf16 v8, v116, s74, v8\n"
"v_dot2_f32_bf16 v9, v116, s75, v9\n"
"v_dot2_f32_bf16 v10, v117, s74, v10\n"
"v_dot2_f32_bf16 v11, v117, s75, v11\n"
"v_dot2_f32_bf16 v12, v118, s74, v12\n"
"v_dot2_f32_bf16 v13, v118, s75, v13\n"
"v_dot2_f32_bf16 v14, v119, s74, v14\n"
"v_dot2_f32_bf16 v15, v119, s75, v15\n"
"v_dot2_f32_bf16 v16, v132, s74, v16\n"
"v_dot2_f32_bf16 v17, v132, s75, v17\n"
"v_dot2_f32_bf16 v18, v133, s74, v18\n"
"v_dot2_f32_bf16 v19, v133, s75, v19\n"
"v_dot2_f32_bf16 v20, v134, s74, v20\n"
"v_dot2_f32_bf16 v21, v134, s75, v21\n"
"v_dot2_f32_bf16 v22, v135, s74, v22\n"
"v_dot2_f32_bf16 v23, v135, s75, v23\n"
"v_dot2_f32_bf16 v24, v148, s74, v24\n"
"v_dot2_f32_bf16 v25, v148, s75, v25\n"
"v_dot2_f32_bf16 v26, v149, s74, v26\n"
"v_dot2_f32_bf16 v27, v149, s75, v27\n"
"v_dot2_f32_bf16 v28, v150, s74, v28\n"
"v_dot2_f32_bf16 v29, v150, s75, v29\n"
"v_dot2_f32_bf16 v30, v151, s74, v30\n"
"v_dot2_f32_bf16 v31, v151, s75, v31\n"
"s_waitcnt vmcnt(0)\n"
"v_dot2_f32_bf16 v0, v104, s74, v0\n"
"v_dot2_f32_bf16 v1, v104, s75, v1\n"
"v_dot2_f32_bf16 v2, v105, s74, v2\n"
"v_dot2_f32_bf16 v3, v105, s75, v3\n"
"v_dot2_f32_bf16 v4, v106, s74, v4\n"
"v_dot2_f32_bf16 v5, v106, s75, v5\n"
"v_dot2_f32_bf16 v6, v107, s74, v6\n"
"v_dot2_f32_bf16 v7, v107, s75, v7\n"
"v_dot2_f32_bf16 v8, v120, s74, v8\n"
"v_dot2_f32_bf16 v9, v120, s75, v9\n"
"v_dot2_f32_bf16 v10, v121, s74, v10\n"
"v_dot2_f32_bf16 v11, v121, s75, v11\n"
"v_dot2_f32_bf16 v12, v122, s74, v12\n"
"v_dot2_f32_bf16 v13, v122, s75, v13\n"
"v_dot2_f32_bf16 v14, v123, s74, v14\n"
"v_dot2_f32_bf16 v15, v123, s75, v15\n"
"v_dot2_f32_bf16 v16, v136, s74, v16\n"
"v_dot2_f32_bf16 v17, v136, s75, v17\n"
"v_dot2_f32_bf16 v18, v137, s74, v18\n"
"v_dot2_f32_bf16 v19, v137, s75, v19\n"
"v_dot2_f32_bf16 v20, v138, s74, v20\n"
"v_dot2_f32_bf16 v21, v138, s75, v21\n"
"v_dot2_f32_bf16 v22, v139, s74, v22\n"
"v_dot2_f32_bf16 v23, v139, s75, v23\n"
"v_dot2_f32_bf16 v24, v152, s74, v24\n"
"v_dot2_f32_bf16 v25, v152, s75, v25\n"
"v_dot2_f32_bf16 v26, v153, s74, v26\n"
"v_dot2_f32_bf16 v27, v153, s75, v27\n"
"v_dot2_f32_bf16 v28, v154, s74, v28\n"
"v_dot2_f32_bf16 v29, v154, s75, v29\n"
"v_dot2_f32_bf16 v30, v155, s74, v30\n"
"v_dot2_f32_bf16 v31, v155, s75, v31\n"
"s_nop 3\n"
"v_cvt_pk_bf16_f32 v64, v0, v1\n"
"v_cvt_pk_bf16_f32 v65, v2, v3\n"
"v_cvt_pk_bf16_f32 v66, v4, v5\n"
"v_cvt_pk_bf16_f32 v67, v6, v7\n"
"v_cvt_pk_bf16_f32 v68, v8, v9\n"
"v_cvt_pk_bf16_f32 v69, v10, v11\n"
"v_cvt_pk_bf16_f32 v70, v12, v13\n"
"v_cvt_pk_bf16_f32 v71, v14, v15\n"
"v_cvt_pk_bf16_f32 v72, v16, v17\n"
"v_cvt_pk_bf16_f32 v73, v18, v19\n"
"v_cvt_pk_bf16_f32 v74, v20, v21\n"
"v_cvt_pk_bf16_f32 v75, v22, v23\n"
"v_cvt_pk_bf16_f32 v76, v24, v25\n"
"v_cvt_pk_bf16_f32 v77, v26, v27\n"
"v_cvt_pk_bf16_f32 v78, v28, v29\n"
"v_cvt_pk_bf16_f32 v79, v30, v31\n"
"v_permlane32_swap_b32_e32 v64, v66\n"
"v_permlane32_swap_b32_e32 v65, v67\n"
"s_nop 0\n"
"v_permlane16_swap_b32_e32 v64, v66\n"
"v_permlane16_swap_b32_e32 v65, v67\n"
"buffer_store_dwordx4 v[64:67], v238, s[52:55], s64 offen \n"
"v_permlane32_swap_b32_e32 v68, v70\n"
"v_permlane32_swap_b32_e32 v69, v71\n"
"s_nop 0\n"
"v_permlane16_swap_b32_e32 v68, v70\n"
"v_permlane16_swap_b32_e32 v69, v71\n"
"buffer_store_dwordx4 v[68:71], v238, s[52:55], s66 offen \n"
"v_permlane32_swap_b32_e32 v72, v74\n"
"v_permlane32_swap_b32_e32 v73, v75\n"
"s_nop 0\n"
"v_permlane16_swap_b32_e32 v72, v74\n"
"v_permlane16_swap_b32_e32 v73, v75\n"
"buffer_store_dwordx4 v[72:75], v238, s[52:55], s67 offen \n"
"v_permlane32_swap_b32_e32 v76, v78\n"
"v_permlane32_swap_b32_e32 v77, v79\n"
"s_nop 0\n"
"v_permlane16_swap_b32_e32 v76, v78\n"
"v_permlane16_swap_b32_e32 v77, v79\n"
"buffer_store_dwordx4 v[76:79], v238, s[52:55], s68 offen \n"
"s_branch L_end%=\n"
"L_side%=:\n"
"s_cmp_eq_u32 s77, 11\n"
"s_cbranch_scc0 L_side2%=\n"
"s_nop 3\n"
"v_mbcnt_lo_u32_b32 v208, -1, 0\n"
"v_mbcnt_hi_u32_b32 v208, -1, v208\n"
"v_lshlrev_b32_e32 v208, 6, v208\n"
"s_mov_b32 s70, 4096\n"
"buffer_load_dword a188, v208, s[36:39], s70 offen sc1\n"
"s_mov_b32 s70, 8192\n"
"buffer_load_dword a189, v208, s[36:39], s70 offen sc1\n"
"s_mov_b32 s70, 12288\n"
"buffer_load_dword a190, v208, s[36:39], s70 offen sc1\n"
"s_mov_b32 s70, 16384\n"
"buffer_load_dword a191, v208, s[36:39], s70 offen sc1\n"
"s_mov_b32 s77, 9\n"
"s_branch L_bumpd%=\n"
"L_side2%=:\n"
"s_nop 3\n"
"v_accvgpr_read_b32 v210, a188\n"
"v_accvgpr_read_b32 v211, a189\n"
"v_accvgpr_read_b32 v212, a190\n"
"v_accvgpr_read_b32 v213, a191\n"
"v_cmp_eq_u32_e64 s[98:99], s62, v210\n"
"v_cmp_eq_u32_e32 vcc, s62, v211\n"
"s_nop 0\n"
"s_and_b64 s[98:99], s[98:99], vcc\n"
"v_cmp_eq_u32_e32 vcc, s62, v212\n"
"s_nop 0\n"
"s_and_b64 s[98:99], s[98:99], vcc\n"
"v_cmp_eq_u32_e32 vcc, s62, v213\n"
"s_nop 0\n"
"s_and_b64 s[98:99], s[98:99], vcc\n"
"s_cmp_eq_u64 s[98:99], -1\n"
"s_cbranch_scc1 L_bump%=\n"
"v_mbcnt_lo_u32_b32 v208, -1, 0\n"
"v_mbcnt_hi_u32_b32 v208, -1, v208\n"
"v_lshlrev_b32_e32 v208, 6, v208\n"
"s_mov_b32 s70, 4096\n"
"s_mov_b32 s71, 0x40000\n"
"L_pk0%=:\n"
"buffer_load_dwordx2 v[210:211], v208, s[36:39], s70 offen sc1\n"
"s_waitcnt vmcnt(0)\n"
"v_cmp_eq_u32_e64 s[98:99], s62, v210\n"
"v_cmp_eq_u32_e32 vcc, s65, v211\n"
"s_nop 0\n"
"s_and_b64 s[98:99], s[98:99], vcc\n"
"s_cmp_eq_u64 s[98:99], -1\n"
"s_cbranch_scc1 L_pk0d%=\n"
"s_sleep 1\n"
"s_sub_u32 s71, s71, 1\n"
"s_cmp_lg_u32 s71, 0\n"
"s_cbranch_scc1 L_pk0%=\n"
"L_pk0d%=:\n"
"s_cmp_eq_u64 s[98:99], -1\n"
"s_cbranch_scc0 L_bumpd%=\n"
"s_mov_b32 s70, 8192\n"
"s_mov_b32 s71, 0x40000\n"
"L_pk1%=:\n"
"buffer_load_dwordx2 v[210:211], v208, s[36:39], s70 offen sc1\n"
"s_waitcnt vmcnt(0)\n"
"v_cmp_eq_u32_e64 s[98:99], s62, v210\n"
"v_cmp_eq_u32_e32 vcc, s65, v211\n"
"s_nop 0\n"
"s_and_b64 s[98:99], s[98:99], vcc\n"
"s_cmp_eq_u64 s[98:99], -1\n"
"s_cbranch_scc1 L_pk1d%=\n"
"s_sleep 1\n"
"s_sub_u32 s71, s71, 1\n"
"s_cmp_lg_u32 s71, 0\n"
"s_cbranch_scc1 L_pk1%=\n"
"L_pk1d%=:\n"
"s_cmp_eq_u64 s[98:99], -1\n"
"s_cbranch_scc0 L_bumpd%=\n"
"s_mov_b32 s70, 12288\n"
"s_mov_b32 s71, 0x40000\n"
"L_pk2%=:\n"
"buffer_load_dwordx2 v[210:211], v208, s[36:39], s70 offen sc1\n"
"s_waitcnt vmcnt(0)\n"
"v_cmp_eq_u32_e64 s[98:99], s62, v210\n"
"v_cmp_eq_u32_e32 vcc, s65, v211\n"
"s_nop 0\n"
"s_and_b64 s[98:99], s[98:99], vcc\n"
"s_cmp_eq_u64 s[98:99], -1\n"
"s_cbranch_scc1 L_pk2d%=\n"
"s_sleep 1\n"
"s_sub_u32 s71, s71, 1\n"
"s_cmp_lg_u32 s71, 0\n"
"s_cbranch_scc1 L_pk2%=\n"
"L_pk2d%=:\n"
"s_cmp_eq_u64 s[98:99], -1\n"
"s_cbranch_scc0 L_bumpd%=\n"
"s_mov_b32 s70, 16384\n"
"s_mov_b32 s71, 0x40000\n"
"L_pk3%=:\n"
"buffer_load_dwordx2 v[210:211], v208, s[36:39], s70 offen sc1\n"
"s_waitcnt vmcnt(0)\n"
"v_cmp_eq_u32_e64 s[98:99], s62, v210\n"
"v_cmp_eq_u32_e32 vcc, s65, v211\n"
"s_nop 0\n"
"s_and_b64 s[98:99], s[98:99], vcc\n"
"s_cmp_eq_u64 s[98:99], -1\n"
"s_cbranch_scc1 L_pk3d%=\n"
"s_sleep 1\n"
"s_sub_u32 s71, s71, 1\n"
"s_cmp_lg_u32 s71, 0\n"
"s_cbranch_scc1 L_pk3%=\n"
"L_pk3d%=:\n"
"s_cmp_eq_u64 s[98:99], -1\n"
"s_cbranch_scc0 L_bumpd%=\n"
"L_bump%=:\n"
"v_mov_b32_e32 v208, 0\n"
"v_mov_b32_e32 v210, s62\n"
"buffer_store_dword v210, v208, s[36:39], 0 offen sc1\n"
"s_nop 1\n"
"s_branch L_bumpd%=\n"
"L_slow%=:\n"
"s_waitcnt vmcnt(0) lgkmcnt(0)\n"
"s_barrier\n"
"s_mov_b32 s76, -1\n"
"s_mov_b32 s81, 32768\n"
"v_xor_b32_e32 v240, 64, v241\n"
"s_sub_u32 s69, s69, 98304\n"
"v_subrev_u32_e32 v240, s69, v240\n"
"v_subrev_u32_e32 v241, s69, v241\n"
"s_mov_b32 s69, 98304\n"
"s_sub_u32 s95, s95, 98304\n"
"v_subrev_u32_e32 v242, s95, v242\n"
"v_subrev_u32_e32 v243, s95, v243\n"
"s_mov_b32 s95, 98304\n"
"v_mbcnt_lo_u32_b32 v7, -1, 0\n"
"v_mbcnt_hi_u32_b32 v7, -1, v7\n"
"v_lshrrev_b32_e32 v0, 3, v7\n"
"v_lshrrev_b32_e32 v1, 4, v7\n"
"v_and_b32_e32 v3, 7, v7\n"
"v_xor_b32_e32 v1, v1, v3\n"
"v_lshlrev_b32_e32 v1, 4, v1\n"
"v_lshl_add_u32 v244, v0, 8, v1\n"
"v_xor_b32_e32 v245, 64, v244\n"
"v_and_b32_e32 v2, 3, v7\n"
"v_bfe_u32 v5, v7, 2, 3\n"
"v_lshrrev_b32_e32 v6, 5, v7\n"
"v_lshlrev_b32_e32 v6, 6, v6\n"
"v_lshrrev_b32_e32 v3, 2, v5\n"
"v_lshlrev_b32_e32 v3, 1, v3\n"
"v_xor_b32_e32 v3, v3, v2\n"
"v_lshlrev_b32_e32 v3, 4, v3\n"
"v_lshlrev_b32_e32 v4, 8, v5\n"
"v_add3_u32 v246, v4, v6, v3\n"
"v_mov_b32_e32 v208, 0xff7fffff\n"
"v_mov_b32_e32 v209, 0xff7fffff\n"
"v_mov_b32_e32 v210, 0xff7fffff\n"
"v_mov_b32_e32 v211, 0xff7fffff\n"
"v_mov_b32_e32 v212, 0xff7fffff\n"
"v_mov_b32_e32 v213, 0xff7fffff\n"
"s_and_b32 s70, s60, 1\n"
"s_lshl_b32 s64, s70, 13\n"
"s_lshr_b32 s71, s60, 1\n"
"s_lshl_b32 s66, s71, 14\n"
"s_lshl_b32 s67, s70, 15\n"
"s_add_u32 s67, s67, s66\n"
"s_add_u32 s67, s67, 98304\n"
"s_add_u32 s66, s66, 163840\n"
"s_mul_i32 s80, %17, 6\n"
"s_and_b32 s80, s80, 30\n"
"s_lshl_b32 s80, s80, 13\n"
"s_add_u32 s64, s64, s80\n"
"s_mov_b32 s90, s64\n"
"s_mov_b32 s91, s67\n"
"s_mov_b32 s63, 16\n"
"L_sl%=:\n"
"s_mov_b32 m0, s67\n"
"s_nop 0\n"
"buffer_load_dwordx4 v244, s[48:51], s64 offen nt lds\n"
"s_add_u32 m0, m0, 896\n"
"s_nop 0\n"
"buffer_load_dwordx4 v244, s[48:51], s64 offen offset:128 nt lds\n"
"buffer_load_dwordx4 v245, s[48:51], s64 offen offset:2176 nt lds\n"
"s_sub_u32 m0, m0, 896\n"
"s_nop 0\n"
"buffer_load_dwordx4 v245, s[48:51], s64 offen offset:2048 nt lds\n"
"s_add_u32 m0, m0, 4096\n"
"s_add_u32 s64, s64, 4096\n"
"buffer_load_dwordx4 v244, s[48:51], s64 offen nt lds\n"
"s_add_u32 m0, m0, 896\n"
"s_nop 0\n"
"buffer_load_dwordx4 v244, s[48:51], s64 offen offset:128 nt lds\n"
"buffer_load_dwordx4 v245, s[48:51], s64 offen offset:2176 nt lds\n"
"s_sub_u32 m0, m0, 896\n"
"s_nop 0\n"
"buffer_load_dwordx4 v245, s[48:51], s64 offen offset:2048 nt lds\n"
"s_add_u32 s64, s64, 12288\n"
"s_and_b32 s64, s64, 0x3ffff\n"
"s_add_u32 s67, s67, 65536\n"
"s_cmp_ge_u32 s67, s66\n"
"s_cselect_b32 s80, 163840, 0\n"
"s_sub_u32 s67, s67, s80\n"
"s_mov_b32 s67, s91\n"
"s_waitcnt vmcnt(0)\n"
"s_barrier\n"
"ds_read_b128 a[96:99], v240 offset:0\n"
"ds_read_b128 a[100:103], v241 offset:0\n"
"ds_read_b128 a[104:107], v240 offset:1024\n"
"ds_read_b128 a[108:111], v241 offset:1024\n"
"ds_read_b128 a[112:115], v240 offset:4096\n"
"ds_read_b128 a[116:119], v241 offset:4096\n"
"ds_read_b128 a[120:123], v240 offset:5120\n"
"ds_read_b128 a[124:127], v241 offset:5120\n"
"s_waitcnt lgkmcnt(7)\n"
"v_mfma_f32_16x16x32_bf16 v[0:3], a[96:99], a[0:3], 0\n"
"s_waitcnt lgkmcnt(6)\n"
"v_mfma_f32_16x16x32_bf16 v[0:3], a[100:103], a[4:7], v[0:3]\n"
"s_waitcnt lgkmcnt(5)\n"
"v_mfma_f32_16x16x32_bf16 v[0:3], a[104:107], a[8:11], v[0:3]\n"
"s_waitcnt lgkmcnt(4)\n"
"v_mfma_f32_16x16x32_bf16 v[0:3], a[108:111], a[12:15], v[0:3]\n"
"s_waitcnt lgkmcnt(3)\n"
"v_mfma_f32_16x16x32_bf16 v[4:7], a[112:115], a[0:3], 0\n"
"s_waitcnt lgkmcnt(2)\n"
"v_mfma_f32_16x16x32_bf16 v[4:7], a[116:119], a[4:7], v[4:7]\n"
"s_waitcnt lgkmcnt(1)\n"
"v_mfma_f32_16x16x32_bf16 v[4:7], a[120:123], a[8:11], v[4:7]\n"
"s_waitcnt lgkmcnt(0)\n"
"v_mfma_f32_16x16x32_bf16 v[4:7], a[124:127], a[12:15], v[4:7]\n"
"v_mfma_f32_16x16x32_bf16 v[32:35], a[96:99], a[16:19], 0\n"
"v_mfma_f32_16x16x32_bf16 v[32:35], a[100:103], a[20:23], v[32:35]\n"
"v_mfma_f32_16x16x32_bf16 v[32:35], a[104:107], a[24:27], v[32:35]\n"
"v_mfma_f32_16x16x32_bf16 v[32:35], a[108:111], a[28:31], v[32:35]\n"
"v_mfma_f32_16x16x32_bf16 v[36:39], a[112:115], a[16:19], 0\n"
"v_mfma_f32_16x16x32_bf16 v[36:39], a[116:119], a[20:23], v[36:39]\n"
"v_mfma_f32_16x16x32_bf16 v[36:39], a[120:123], a[24:27], v[36:39]\n"
"v_mfma_f32_16x16x32_bf16 v[36:39], a[124:127], a[28:31], v[36:39]\n"
"v_mfma_f32_16x16x32_bf16 v[64:67], a[96:99], a[32:35], 0\n"
"v_mfma_f32_16x16x32_bf16 v[64:67], a[100:103], a[36:39], v[64:67]\n"
"v_mfma_f32_16x16x32_bf16 v[64:67], a[104:107], a[40:43], v[64:67]\n"
"v_mfma_f32_16x16x32_bf16 v[64:67], a[108:111], a[44:47], v[64:67]\n"
"v_mfma_f32_16x16x32_bf16 v[68:71], a[112:115], a[32:35], 0\n"
"v_mfma_f32_16x16x32_bf16 v[68:71], a[116:119], a[36:39], v[68:71]\n"
"v_mfma_f32_16x16x32_bf16 v[68:71], a[120:123], a[40:43], v[68:71]\n"
"v_mfma_f32_16x16x32_bf16 v[68:71], a[124:127], a[44:47], v[68:71]\n"
"v_mfma_f32_16x16x32_bf16 v[96:99], a[96:99], a[48:51], 0\n"
"v_mfma_f32_16x16x32_bf16 v[96:99], a[100:103], a[52:55], v[96:99]\n"
"v_mfma_f32_16x16x32_bf16 v[96:99], a[104:107], a[56:59], v[96:99]\n"
"v_mfma_f32_16x16x32_bf16 v[96:99], a[108:111], a[60:63], v[96:99]\n"
"v_mfma_f32_16x16x32_bf16 v[100:103], a[112:115], a[48:51], 0\n"
"v_mfma_f32_16x16x32_bf16 v[100:103], a[116:119], a[52:55], v[100:103]\n"
"v_mfma_f32_16x16x32_bf16 v[100:103], a[120:123], a[56:59], v[100:103]\n"
"v_mfma_f32_16x16x32_bf16 v[100:103], a[124:127], a[60:63], v[100:103]\n"
"v_mfma_f32_16x16x32_bf16 v[128:131], a[96:99], a[64:67], 0\n"
"v_mfma_f32_16x16x32_bf16 v[128:131], a[100:103], a[68:71], v[128:131]\n"
"v_mfma_f32_16x16x32_bf16 v[128:131], a[104:107], a[72:75], v[128:131]\n"
"v_mfma_f32_16x16x32_bf16 v[128:131], a[108:111], a[76:79], v[128:131]\n"
"v_mfma_f32_16x16x32_bf16 v[132:135], a[112:115], a[64:67], 0\n"
"v_mfma_f32_16x16x32_bf16 v[132:135], a[116:119], a[68:71], v[132:135]\n"
"v_mfma_f32_16x16x32_bf16 v[132:135], a[120:123], a[72:75], v[132:135]\n"
"v_mfma_f32_16x16x32_bf16 v[132:135], a[124:127], a[76:79], v[132:135]\n"
"v_mfma_f32_16x16x32_bf16 v[160:163], a[96:99], a[80:83], 0\n"
"v_mfma_f32_16x16x32_bf16 v[160:163], a[100:103], a[84:87], v[160:163]\n"
"v_mfma_f32_16x16x32_bf16 v[160:163], a[104:107], a[88:91], v[160:163]\n"
"v_mfma_f32_16x16x32_bf16 v[160:163], a[108:111], a[92:95], v[160:163]\n"
"v_mfma_f32_16x16x32_bf16 v[164:167], a[112:115], a[80:83], 0\n"
"v_mfma_f32_16x16x32_bf16 v[164:167], a[116:119], a[84:87], v[164:167]\n"
"v_mfma_f32_16x16x32_bf16 v[164:167], a[120:123], a[88:91], v[164:167]\n"
"v_mfma_f32_16x16x32_bf16 v[164:167], a[124:127], a[92:95], v[164:167]\n"
"v_max3_f32 v208, v208, v0, v1\n"
"v_max3_f32 v208, v208, v2, v3\n"
"v_max3_f32 v208, v208, v4, v5\n"
"v_max3_f32 v208, v208, v6, v7\n"
"v_max3_f32 v209, v209, v32, v33\n"
"v_max3_f32 v209, v209, v34, v35\n"
"v_max3_f32 v209, v209, v36, v37\n"
"v_max3_f32 v209, v209, v38, v39\n"
"v_max3_f32 v210, v210, v64, v65\n"
"v_max3_f32 v210, v210, v66, v67\n"
"v_max3_f32 v210, v210, v68, v69\n"
"v_max3_f32 v210, v210, v70, v71\n"
"v_max3_f32 v211, v211, v96, v97\n"
"v_max3_f32 v211, v211, v98, v99\n"
"v_max3_f32 v211, v211, v100, v101\n"
"v_max3_f32 v211, v211, v102, v103\n"
"v_max3_f32 v212, v212, v128, v129\n"
"v_max3_f32 v212, v212, v130, v131\n"
"v_max3_f32 v212, v212, v132, v133\n"
"v_max3_f32 v212, v212, v134, v135\n"
"v_max3_f32 v213, v213, v160, v161\n"
"v_max3_f32 v213, v213, v162, v163\n"
"v_max3_f32 v213, v213, v164, v165\n"
"v_max3_f32 v213, v213, v166, v167\n"
"v_add_u32_e32 v240, 32768, v240\n"
"v_add_u32_e32 v241, 32768, v241\n"
"ds_read_b128 a[96:99], v240 offset:0\n"
"ds_read_b128 a[100:103], v241 offset:0\n"
"ds_read_b128 a[104:107], v240 offset:1024\n"
"ds_read_b128 a[108:111], v241 offset:1024\n"
"ds_read_b128 a[112:115], v240 offset:4096\n"
"ds_read_b128 a[116:119], v241 offset:4096\n"
"ds_read_b128 a[120:123], v240 offset:5120\n"
"ds_read_b128 a[124:127], v241 offset:5120\n"
"s_waitcnt lgkmcnt(7)\n"
"v_mfma_f32_16x16x32_bf16 v[0:3], a[96:99], a[0:3], 0\n"
"s_waitcnt lgkmcnt(6)\n"
"v_mfma_f32_16x16x32_bf16 v[0:3], a[100:103], a[4:7], v[0:3]\n"
"s_waitcnt lgkmcnt(5)\n"
"v_mfma_f32_16x16x32_bf16 v[0:3], a[104:107], a[8:11], v[0:3]\n"
"s_waitcnt lgkmcnt(4)\n"
"v_mfma_f32_16x16x32_bf16 v[0:3], a[108:111], a[12:15], v[0:3]\n"
"s_waitcnt lgkmcnt(3)\n"
"v_mfma_f32_16x16x32_bf16 v[4:7], a[112:115], a[0:3], 0\n"
"s_waitcnt lgkmcnt(2)\n"
"v_mfma_f32_16x16x32_bf16 v[4:7], a[116:119], a[4:7], v[4:7]\n"
"s_waitcnt lgkmcnt(1)\n"
"v_mfma_f32_16x16x32_bf16 v[4:7], a[120:123], a[8:11], v[4:7]\n"
"s_waitcnt lgkmcnt(0)\n"
"v_mfma_f32_16x16x32_bf16 v[4:7], a[124:127], a[12:15], v[4:7]\n"
"v_mfma_f32_16x16x32_bf16 v[32:35], a[96:99], a[16:19], 0\n"
"v_mfma_f32_16x16x32_bf16 v[32:35], a[100:103], a[20:23], v[32:35]\n"
"v_mfma_f32_16x16x32_bf16 v[32:35], a[104:107], a[24:27], v[32:35]\n"
"v_mfma_f32_16x16x32_bf16 v[32:35], a[108:111], a[28:31], v[32:35]\n"
"v_mfma_f32_16x16x32_bf16 v[36:39], a[112:115], a[16:19], 0\n"
"v_mfma_f32_16x16x32_bf16 v[36:39], a[116:119], a[20:23], v[36:39]\n"
"v_mfma_f32_16x16x32_bf16 v[36:39], a[120:123], a[24:27], v[36:39]\n"
"v_mfma_f32_16x16x32_bf16 v[36:39], a[124:127], a[28:31], v[36:39]\n"
"v_mfma_f32_16x16x32_bf16 v[64:67], a[96:99], a[32:35], 0\n"
"v_mfma_f32_16x16x32_bf16 v[64:67], a[100:103], a[36:39], v[64:67]\n"
"v_mfma_f32_16x16x32_bf16 v[64:67], a[104:107], a[40:43], v[64:67]\n"
"v_mfma_f32_16x16x32_bf16 v[64:67], a[108:111], a[44:47], v[64:67]\n"
"v_mfma_f32_16x16x32_bf16 v[68:71], a[112:115], a[32:35], 0\n"
"v_mfma_f32_16x16x32_bf16 v[68:71], a[116:119], a[36:39], v[68:71]\n"
"v_mfma_f32_16x16x32_bf16 v[68:71], a[120:123], a[40:43], v[68:71]\n"
"v_mfma_f32_16x16x32_bf16 v[68:71], a[124:127], a[44:47], v[68:71]\n"
"v_mfma_f32_16x16x32_bf16 v[96:99], a[96:99], a[48:51], 0\n"
"v_mfma_f32_16x16x32_bf16 v[96:99], a[100:103], a[52:55], v[96:99]\n"
"v_mfma_f32_16x16x32_bf16 v[96:99], a[104:107], a[56:59], v[96:99]\n"
"v_mfma_f32_16x16x32_bf16 v[96:99], a[108:111], a[60:63], v[96:99]\n"
"v_mfma_f32_16x16x32_bf16 v[100:103], a[112:115], a[48:51], 0\n"
"v_mfma_f32_16x16x32_bf16 v[100:103], a[116:119], a[52:55], v[100:103]\n"
"v_mfma_f32_16x16x32_bf16 v[100:103], a[120:123], a[56:59], v[100:103]\n"
"v_mfma_f32_16x16x32_bf16 v[100:103], a[124:127], a[60:63], v[100:103]\n"
"v_mfma_f32_16x16x32_bf16 v[128:131], a[96:99], a[64:67], 0\n"
"v_mfma_f32_16x16x32_bf16 v[128:131], a[100:103], a[68:71], v[128:131]\n"
"v_mfma_f32_16x16x32_bf16 v[128:131], a[104:107], a[72:75], v[128:131]\n"
"v_mfma_f32_16x16x32_bf16 v[128:131], a[108:111], a[76:79], v[128:131]\n"
"v_mfma_f32_16x16x32_bf16 v[132:135], a[112:115], a[64:67], 0\n"
"v_mfma_f32_16x16x32_bf16 v[132:135], a[116:119], a[68:71], v[132:135]\n"
"v_mfma_f32_16x16x32_bf16 v[132:135], a[120:123], a[72:75], v[132:135]\n"
"v_mfma_f32_16x16x32_bf16 v[132:135], a[124:127], a[76:79], v[132:135]\n"
"v_mfma_f32_16x16x32_bf16 v[160:163], a[96:99], a[80:83], 0\n"
"v_mfma_f32_16x16x32_bf16 v[160:163], a[100:103], a[84:87], v[160:163]\n"
"v_mfma_f32_16x16x32_bf16 v[160:163], a[104:107], a[88:91], v[160:163]\n"
"v_mfma_f32_16x16x32_bf16 v[160:163], a[108:111], a[92:95], v[160:163]\n"
"v_mfma_f32_16x16x32_bf16 v[164:167], a[112:115], a[80:83], 0\n"
"v_mfma_f32_16x16x32_bf16 v[164:167], a[116:119], a[84:87], v[164:167]\n"
"v_mfma_f32_16x16x32_bf16 v[164:167], a[120:123], a[88:91], v[164:167]\n"
"v_mfma_f32_16x16x32_bf16 v[164:167], a[124:127], a[92:95], v[164:167]\n"
"v_max3_f32 v208, v208, v0, v1\n"
"v_max3_f32 v208, v208, v2, v3\n"
"v_max3_f32 v208, v208, v4, v5\n"
"v_max3_f32 v208, v208, v6, v7\n"
"v_max3_f32 v209, v209, v32, v33\n"
"v_max3_f32 v209, v209, v34, v35\n"
"v_max3_f32 v209, v209, v36, v37\n"
"v_max3_f32 v209, v209, v38, v39\n"
"v_max3_f32 v210, v210, v64, v65\n"
"v_max3_f32 v210, v210, v66, v67\n"
"v_max3_f32 v210, v210, v68, v69\n"
"v_max3_f32 v210, v210, v70, v71\n"
"v_max3_f32 v211, v211, v96, v97\n"
"v_max3_f32 v211, v211, v98, v99\n"
"v_max3_f32 v211, v211, v100, v101\n"
"v_max3_f32 v211, v211, v102, v103\n"
"v_max3_f32 v212, v212, v128, v129\n"
"v_max3_f32 v212, v212, v130, v131\n"
"v_max3_f32 v212, v212, v132, v133\n"
"v_max3_f32 v212, v212, v134, v135\n"
"v_max3_f32 v213, v213, v160, v161\n"
"v_max3_f32 v213, v213, v162, v163\n"
"v_max3_f32 v213, v213, v164, v165\n"
"v_max3_f32 v213, v213, v166, v167\n"
"v_subrev_u32_e32 v240, 32768, v240\n"
"v_subrev_u32_e32 v241, 32768, v241\n"
"s_barrier\n"
"s_sub_u32 s63, s63, 1\n"
"s_cmp_lg_u32 s63, 0\n"
"s_cbranch_scc1 L_sl%=\n"
"s_and_b32 s70, s60, 1\n"
"s_lshl_b32 s64, s70, 13\n"
"s_lshr_b32 s71, s60, 1\n"
"s_lshl_b32 s66, s71, 14\n"
"s_lshl_b32 s67, s70, 15\n"
"s_add_u32 s67, s67, s66\n"
"s_add_u32 s67, s67, 98304\n"
"s_add_u32 s66, s66, 163840\n"
"s_mul_i32 s80, %17, 6\n"
"s_and_b32 s80, s80, 30\n"
"s_lshl_b32 s80, s80, 13\n"
"s_add_u32 s64, s64, s80\n"
"s_mov_b32 s90, s64\n"
"s_mov_b32 s91, s67\n"
"s_mov_b32 m0, s67\n"
"s_nop 0\n"
"buffer_load_dwordx4 v244, s[48:51], s64 offen nt lds\n"
"s_add_u32 m0, m0, 896\n"
"s_nop 0\n"
"buffer_load_dwordx4 v244, s[48:51], s64 offen offset:128 nt lds\n"
"buffer_load_dwordx4 v245, s[48:51], s64 offen offset:2176 nt lds\n"
"s_sub_u32 m0, m0, 896\n"
"s_nop 0\n"
"buffer_load_dwordx4 v245, s[48:51], s64 offen offset:2048 nt lds\n"
"s_add_u32 m0, m0, 4096\n"
"s_add_u32 s64, s64, 4096\n"
"buffer_load_dwordx4 v244, s[48:51], s64 offen nt lds\n"
"s_add_u32 m0, m0, 896\n"
"s_nop 0\n"
"buffer_load_dwordx4 v244, s[48:51], s64 offen offset:128 nt lds\n"
"buffer_load_dwordx4 v245, s[48:51], s64 offen offset:2176 nt lds\n"
"s_sub_u32 m0, m0, 896\n"
"s_nop 0\n"
"buffer_load_dwordx4 v245, s[48:51], s64 offen offset:2048 nt lds\n"
"s_add_u32 s64, s64, 12288\n"
"s_and_b32 s64, s64, 0x3ffff\n"
"s_add_u32 s67, s67, 65536\n"
"s_cmp_ge_u32 s67, s66\n"
"s_cselect_b32 s80, 163840, 0\n"
"s_sub_u32 s67, s67, s80\n"
"s_add_u32 m0, s91, 8192\n"
"s_nop 0\n"
"buffer_load_dwordx4 v246, s[52:55], s90 offen nt lds\n"
"s_add_u32 m0, m0, 896\n"
"s_nop 0\n"
"buffer_load_dwordx4 v246, s[52:55], s90 offen offset:128 nt lds\n"
"buffer_load_dwordx4 v246, s[52:55], s90 offen offset:2176 nt lds\n"
"s_sub_u32 m0, m0, 896\n"
"s_nop 0\n"
"buffer_load_dwordx4 v246, s[52:55], s90 offen offset:2048 nt lds\n"
"s_add_u32 m0, m0, 4096\n"
"s_add_u32 s90, s90, 4096\n"
"buffer_load_dwordx4 v246, s[52:55], s90 offen nt lds\n"
"s_add_u32 m0, m0, 896\n"
"s_nop 0\n"
"buffer_load_dwordx4 v246, s[52:55], s90 offen offset:128 nt lds\n"
"buffer_load_dwordx4 v246, s[52:55], s90 offen offset:2176 nt lds\n"
"s_sub_u32 m0, m0, 896\n"
"s_nop 0\n"
"buffer_load_dwordx4 v246, s[52:55], s90 offen offset:2048 nt lds\n"
"s_add_u32 s90, s90, 12288\n"
"s_and_b32 s90, s90, 0x3ffff\n"
"s_add_u32 s91, s91, 65536\n"
"s_cmp_ge_u32 s91, s66\n"
"s_cselect_b32 s80, 163840, 0\n"
"s_sub_u32 s91, s91, s80\n"
"s_mov_b32 m0, s67\n"
"s_nop 0\n"
"buffer_load_dwordx4 v244, s[48:51], s64 offen nt lds\n"
"s_add_u32 m0, m0, 896\n"
"s_nop 0\n"
"buffer_load_dwordx4 v244, s[48:51], s64 offen offset:128 nt lds\n"
"buffer_load_dwordx4 v245, s[48:51], s64 offen offset:2176 nt lds\n"
"s_sub_u32 m0, m0, 896\n"
"s_nop 0\n"
"buffer_load_dwordx4 v245, s[48:51], s64 offen offset:2048 nt lds\n"
"s_add_u32 m0, m0, 4096\n"
"s_add_u32 s64, s64, 4096\n"
"buffer_load_dwordx4 v244, s[48:51], s64 offen nt lds\n"
"s_add_u32 m0, m0, 896\n"
"s_nop 0\n"
"buffer_load_dwordx4 v244, s[48:51], s64 offen offset:128 nt lds\n"
"buffer_load_dwordx4 v245, s[48:51], s64 offen offset:2176 nt lds\n"
"s_sub_u32 m0, m0, 896\n"
"s_nop 0\n"
"buffer_load_dwordx4 v245, s[48:51], s64 offen offset:2048 nt lds\n"
"s_add_u32 s64, s64, 12288\n"
"s_and_b32 s64, s64, 0x3ffff\n"
"s_add_u32 s67, s67, 65536\n"
"s_cmp_ge_u32 s67, s66\n"
"s_cselect_b32 s80, 163840, 0\n"
"s_sub_u32 s67, s67, s80\n"
"s_add_u32 m0, s91, 8192\n"
"s_nop 0\n"
"buffer_load_dwordx4 v246, s[52:55], s90 offen nt lds\n"
"s_add_u32 m0, m0, 896\n"
"s_nop 0\n"
"buffer_load_dwordx4 v246, s[52:55], s90 offen offset:128 nt lds\n"
"buffer_load_dwordx4 v246, s[52:55], s90 offen offset:2176 nt lds\n"
"s_sub_u32 m0, m0, 896\n"
"s_nop 0\n"
"buffer_load_dwordx4 v246, s[52:55], s90 offen offset:2048 nt lds\n"
"s_add_u32 m0, m0, 4096\n"
"s_add_u32 s90, s90, 4096\n"
"buffer_load_dwordx4 v246, s[52:55], s90 offen nt lds\n"
"s_add_u32 m0, m0, 896\n"
"s_nop 0\n"
"buffer_load_dwordx4 v246, s[52:55], s90 offen offset:128 nt lds\n"
"buffer_load_dwordx4 v246, s[52:55], s90 offen offset:2176 nt lds\n"
"s_sub_u32 m0, m0, 896\n"
"s_nop 0\n"
"buffer_load_dwordx4 v246, s[52:55], s90 offen offset:2048 nt lds\n"
"s_add_u32 s90, s90, 12288\n"
"s_and_b32 s90, s90, 0x3ffff\n"
"s_add_u32 s91, s91, 65536\n"
"s_cmp_ge_u32 s91, s66\n"
"s_cselect_b32 s80, 163840, 0\n"
"s_sub_u32 s91, s91, s80\n"
"s_waitcnt vmcnt(16)\n"
"s_barrier\n"
"ds_read_b128 a[96:99], v240 offset:0\n"
"ds_read_b128 a[100:103], v241 offset:0\n"
"ds_read_b128 a[104:107], v240 offset:1024\n"
"ds_read_b128 a[108:111], v241 offset:1024\n"
"ds_read_b128 a[112:115], v240 offset:4096\n"
"ds_read_b128 a[116:119], v241 offset:4096\n"
"ds_read_b128 a[120:123], v240 offset:5120\n"
"ds_read_b128 a[124:127], v241 offset:5120\n"
"s_cmp_eq_u32 s69, 131072\n"
"s_cselect_b32 s68, -131072, s81\n"
"s_add_u32 s69, s69, s68\n"
"v_add_u32_e32 v240, s68, v240\n"
"v_add_u32_e32 v241, s68, v241\n"
"ds_read_b64_tr_b16 a[128:129], v242 offset:8192\n"
"ds_read_b64_tr_b16 a[130:131], v242 offset:12288\n"
"ds_read_b64_tr_b16 a[132:133], v243 offset:8192\n"
"ds_read_b64_tr_b16 a[134:135], v243 offset:12288\n"
"ds_read_b64_tr_b16 a[136:137], v242 offset:8704\n"
"ds_read_b64_tr_b16 a[138:139], v242 offset:12800\n"
"ds_read_b64_tr_b16 a[140:141], v243 offset:8704\n"
"ds_read_b64_tr_b16 a[142:143], v243 offset:12800\n"
"ds_read_b64_tr_b16 a[144:145], v242 offset:9216\n"
"ds_read_b64_tr_b16 a[146:147], v242 offset:13312\n"
"ds_read_b64_tr_b16 a[148:149], v243 offset:9216\n"
"ds_read_b64_tr_b16 a[150:151], v243 offset:13312\n"
"ds_read_b64_tr_b16 a[152:153], v242 offset:9728\n"
"ds_read_b64_tr_b16 a[154:155], v242 offset:13824\n"
"ds_read_b64_tr_b16 a[156:157], v243 offset:9728\n"
"ds_read_b64_tr_b16 a[158:159], v243 offset:13824\n"
"s_cmp_eq_u32 s95, 131072\n"
"s_cselect_b32 s68, -131072, s81\n"
"s_add_u32 s95, s95, s68\n"
"v_add_u32_e32 v242, s68, v242\n"
"v_add_u32_e32 v243, s68, v243\n"
"ds_read_b128 a[192:195], v240 offset:0\n"
"ds_read_b128 a[196:199], v241 offset:0\n"
"ds_read_b128 a[200:203], v240 offset:1024\n"
"ds_read_b128 a[204:207], v241 offset:1024\n"
"ds_read_b128 a[208:211], v240 offset:4096\n"
"ds_read_b128 a[212:215], v241 offset:4096\n"
"ds_read_b128 a[216:219], v240 offset:5120\n"
"ds_read_b128 a[220:223], v241 offset:5120\n"
"s_cmp_eq_u32 s69, 131072\n"
"s_cselect_b32 s68, -131072, s81\n"
"s_add_u32 s69, s69, s68\n"
"v_add_u32_e32 v240, s68, v240\n"
"v_add_u32_e32 v241, s68, v241\n"
"ds_read_b64_tr_b16 a[224:225], v242 offset:8192\n"
"ds_read_b64_tr_b16 a[226:227], v242 offset:12288\n"
"ds_read_b64_tr_b16 a[228:229], v243 offset:8192\n"
"ds_read_b64_tr_b16 a[230:231], v243 offset:12288\n"
"ds_read_b64_tr_b16 a[232:233], v242 offset:8704\n"
"ds_read_b64_tr_b16 a[234:235], v242 offset:12800\n"
"ds_read_b64_tr_b16 a[236:237], v243 offset:8704\n"
"ds_read_b64_tr_b16 a[238:239], v243 offset:12800\n"
"ds_read_b64_tr_b16 a[240:241], v242 offset:9216\n"
"ds_read_b64_tr_b16 a[242:243], v242 offset:13312\n"
"ds_read_b64_tr_b16 a[244:245], v243 offset:9216\n"
"ds_read_b64_tr_b16 a[246:247], v243 offset:13312\n"
"ds_read_b64_tr_b16 a[248:249], v242 offset:9728\n"
"ds_read_b64_tr_b16 a[250:251], v242 offset:13824\n"
"ds_read_b64_tr_b16 a[252:253], v243 offset:9728\n"
"ds_read_b64_tr_b16 a[254:255], v243 offset:13824\n"
"s_cmp_eq_u32 s95, 131072\n"
"s_cselect_b32 s68, -131072, s81\n"
"s_add_u32 s95, s95, s68\n"
"v_add_u32_e32 v242, s68, v242\n"
"v_add_u32_e32 v243, s68, v243\n"
"s_waitcnt lgkmcnt(0)\n"
"s_nop 7\n"
"s_branch L_setneg%=\n"
"L_end%=:\n"
    :
    : "s"(LO(xp)), "s"(HI(xp)), "s"(LO(wp)), "s"(HI(wp)), "s"(LO(kp)), "s"(HI(kp)), "s"(LO(vp)), "s"(HI(vp)),
      "s"(LO(ws)), "s"(HI(ws)), "s"(LO(wo)), "s"(HI(wo)), "s"(LO(out)), "s"(HI(out)), "s"(wid), "s"(bid), "s"(headi), "s"(kvi)
    : "v0","v1","v2","v3","v4","v5","v6","v7","v8","v9","v10","v11","v12","v13","v14","v15","v16","v17","v18","v19","v20","v21","v22","v23","v24","v25","v26","v27","v28","v29","v30","v31","v32","v33","v34","v35","v36","v37","v38","v39","v40","v41","v42","v43","v44","v45","v46","v47","v48","v49","v50","v51","v52","v53","v54","v55","v56","v57","v58","v59","v60","v61","v62","v63","v64","v65","v66","v67","v68","v69","v70","v71","v72","v73","v74","v75","v76","v77","v78","v79","v80","v81","v82","v83","v84","v85","v86","v87","v88","v89","v90","v91","v92","v93","v94","v95","v96","v97","v98","v99","v100","v101","v102","v103","v104","v105","v106","v107","v108","v109","v110","v111","v112","v113","v114","v115","v116","v117","v118","v119","v120","v121","v122","v123","v124","v125","v126","v127","v128","v129","v130","v131","v132","v133","v134","v135","v136","v137","v138","v139","v140","v141","v142","v143","v144","v145","v146","v147","v148","v149","v150","v151","v152","v153","v154","v155","v156","v157","v158","v159","v160","v161","v162","v163","v164","v165","v166","v167","v168","v169","v170","v171","v172","v173","v174","v175","v176","v177","v178","v179","v180","v181","v182","v183","v184","v185","v186","v187","v188","v189","v190","v191","v192","v193","v194","v195","v196","v197","v198","v199","v200","v201","v202","v203","v204","v205","v206","v207","v208","v209","v210","v211","v212","v213","v214","v215","v216","v217","v218","v219","v220","v221","v222","v223","v224","v225","v226","v227","v228","v229","v230","v231","v232","v233","v234","v235","v236","v237","v238","v239","v240","v241","v242","v243","v244","v245","v246","v247","v248","v249","v250","v251","v252","v253","v254","v255","a0","a1","a2","a3","a4","a5","a6","a7","a8","a9","a10","a11","a12","a13","a14","a15","a16","a17","a18","a19","a20","a21","a22","a23","a24","a25","a26","a27","a28","a29","a30","a31","a32","a33","a34","a35","a36","a37","a38","a39","a40","a41","a42","a43","a44","a45","a46","a47","a48","a49","a50","a51","a52","a53","a54","a55","a56","a57","a58","a59","a60","a61","a62","a63","a64","a65","a66","a67","a68","a69","a70","a71","a72","a73","a74","a75","a76","a77","a78","a79","a80","a81","a82","a83","a84","a85","a86","a87","a88","a89","a90","a91","a92","a93","a94","a95","a96","a97","a98","a99","a100","a101","a102","a103","a104","a105","a106","a107","a108","a109","a110","a111","a112","a113","a114","a115","a116","a117","a118","a119","a120","a121","a122","a123","a124","a125","a126","a127","a128","a129","a130","a131","a132","a133","a134","a135","a136","a137","a138","a139","a140","a141","a142","a143","a144","a145","a146","a147","a148","a149","a150","a151","a152","a153","a154","a155","a156","a157","a158","a159","a160","a161","a162","a163","a164","a165","a166","a167","a168","a169","a170","a171","a172","a173","a174","a175","a176","a177","a178","a179","a180","a181","a182","a183","a184","a185","a186","a187","a188","a189","a190","a191","a192","a193","a194","a195","a196","a197","a198","a199","a200","a201","a202","a203","a204","a205","a206","a207","a208","a209","a210","a211","a212","a213","a214","a215","a216","a217","a218","a219","a220","a221","a222","a223","a224","a225","a226","a227","a228","a229","a230","a231","a232","a233","a234","a235","a236","a237","a238","a239","a240","a241","a242","a243","a244","a245","a246","a247","a248","a249","a250","a251","a252","a253","a254","a255","s36","s37","s38","s39","s40","s41","s42","s43","s44","s45","s46","s47","s48","s49","s50","s51","s52","s53","s54","s55","s56","s57","s58","s59","s60","s61","s62","s63","s64","s65","s66","s67","s68","s69","s70","s71","s72","s73","s74","s75","s76","s77","s78","s79","s80","s81","s82","s83","s84","s85","s86","s87","s88","s89","s90","s91","s92","s93","s94","s95","s96","s97","s98","s99","vcc","scc","memory");
}

extern "C" void kernel_launch(void* const* d_in, const int* in_sizes, int n_in, void* d_out, int out_size, void* d_ws, size_t ws_size, hipStream_t stream) {
  (void)hipFuncSetAttribute((const void*)fused_kernel, hipFuncAttributeMaxDynamicSharedMemorySize, 163840);
  hipLaunchKernelGGL(fused_kernel, dim3(256), dim3(256), 163840, stream,
                     (const char*)d_in[0], (const char*)d_in[1], (const char*)d_in[2], (const char*)d_in[3], (const char*)d_in[4],
                     (const char*)d_in[5], (const char*)d_in[6], (const char*)d_in[7], (const char*)d_in[8], (const char*)d_in[9],
                     (char*)d_out, (char*)d_ws);
}
```
